```python
import torch

def _softmax(x, axis=-1):
    return torch.softmax(x, dim=axis)
def _gelu(x):
    return torch.nn.functional.gelu(x, approximate="tanh")
def _silu(x):
    return torch.nn.functional.silu(x)
def _sigmoid(x):
    return torch.sigmoid(x)
def _layernorm(x, axis=-1, eps=1e-5):
    m = x.mean(dim=axis, keepdim=True)
    v = x.var(dim=axis, keepdim=True, unbiased=False)
    return (x - m) / torch.sqrt(v + eps)
def _scatter_add(tgt, idx, src, axis=0):
    return tgt.clone().index_add_(axis, idx, src)
def _segment_sum(src, seg, n):
    out = torch.zeros((n,) + src.shape[1:], dtype=src.dtype)
    return out.index_add_(0, seg, src)
def _pad(x, pw):
    flat = tuple(v for pair in reversed(pw) for v in pair)
    return torch.nn.functional.pad(x, flat)

kernel_name = 'recurrent_scan__gqa_block_cached__dense_activation_silu__decode'

def setup_inputs():

    _w = (lambda a: (lambda t: t.to(torch.bfloat16) if t.is_floating_point() else t)(torch.as_tensor(a)))
    def _A_setup():
        B, Sq, D = 1, 1, 512
        Hq, Hkv, Sk, Dh = 32, 32, 8192, 128
        assert Hq % Hkv == 0, (Hq, Hkv)
        return {
            "x": _w((lambda *s: torch.randn(*s, dtype=torch.bfloat16) * 0.05)(B, Sq, D)),
            "k_cache": _w((lambda *s: torch.randn(*s, dtype=torch.bfloat16) * 3.2)(B, Hkv, Sk, Dh)),
            "v_cache": _w((lambda *s: torch.randn(*s, dtype=torch.bfloat16) * 3.2)(B, Hkv, Sk, Dh)),
            "wq": _w((lambda *s: torch.randn(*s, dtype=torch.bfloat16) * 3.2)(D, Hq * Dh) * (D ** -0.5)),
            "wo": _w((lambda *s: torch.randn(*s, dtype=torch.bfloat16) * 3.2)(Hq * Dh, D) * ((Hq * Dh) ** -0.5)),
        }
    _A_in = _A_setup()

    def _B_setup():
        B, S, D, F = 1, 1, 512, 1376
        return {
            "x": _w((lambda *s: torch.randn(*s, dtype=torch.bfloat16) * 0.05)(B, S, D)),
            "w1": _w((lambda *s: torch.randn(*s, dtype=torch.bfloat16) * 3.2)(D, F) * (D ** -0.5)),
            "w2": _w((lambda *s: torch.randn(*s, dtype=torch.bfloat16) * 0.4)(F, D) * (F ** -0.5)),
        }
    _B_in = _B_setup()

    return {
        "A_x": _A_in["x"],
        "A_k_cache": _A_in["k_cache"],
        "A_v_cache": _A_in["v_cache"],
        "A_wq": _A_in["wq"],
        "A_wo": _A_in["wo"],
        "B_x": _B_in["x"],
        "B_w1": _B_in["w1"],
        "B_w2": _B_in["w2"],
    }

def reference(A_x, A_k_cache, A_v_cache, A_wq, A_wo, B_x, B_w1, B_w2):
    def _A_ref(x, k_cache, v_cache, wq, wo):
        B, Sq, D = x.shape
        Hq, Dh = 32, 128
        Hkv = k_cache.shape[1]
        rep = Hq // Hkv
        q = (lambda x, *d: x.permute(*d))((x @ wq).reshape(B, Sq, Hq, Dh), 0, 2, 1, 3)
        k = (lambda x, n, axis: torch.repeat_interleave(x, n, dim=axis))(k_cache, rep, axis=1)
        v = (lambda x, n, axis: torch.repeat_interleave(x, n, dim=axis))(v_cache, rep, axis=1)
        scores = torch.einsum("bhqd,bhkd->bhqk", q, k) * (Dh ** -0.5)
        p = _softmax(scores, axis=-1)
        o = torch.einsum("bhqk,bhkd->bhqd", p, v)
        return (lambda x, *d: x.permute(*d))(o, 0, 2, 1, 3).reshape(B, Sq, Hq * Dh) @ wo

    def _B_ref(x, w1, w2):
        h = torch.einsum("bsd,df->bsf", x, w1)
        h = _silu(h)
        out = torch.einsum("bsf,fd->bsd", h, w2)
        return out

    x = A_x
    xn = _layernorm(x, axis=-1)
    s = _A_ref(xn, A_k_cache, A_v_cache, A_wq, A_wo)
    g = _silu(_B_ref(xn, B_w1, B_w2))
    return x + s * g
```

```cpp
#include <hip/hip_runtime.h>
#include <hip/hip_bf16.h>
#include <stdint.h>

typedef unsigned u32x4 __attribute__((ext_vector_type(4)));
typedef __bf16 bf16x2 __attribute__((ext_vector_type(2)));

constexpr int D = 512, H = 32, SK = 8192, DH = 128, F = 1376;
constexpr int NSPLIT = 8;

__device__ __forceinline__ float bf2f(unsigned short b) { return __uint_as_float((unsigned)b << 16); }
__device__ __forceinline__ float rbf(float f) { return (float)(__bf16)f; }
__device__ __forceinline__ float lo(unsigned u) { return __uint_as_float(u << 16); }
__device__ __forceinline__ float hi(unsigned u) { return __uint_as_float(u & 0xffff0000u); }
__device__ __forceinline__ float dot2(unsigned a, unsigned b, float acc) {
  return __builtin_amdgcn_fdot2_f32_bf16(__builtin_bit_cast(bf16x2, a), __builtin_bit_cast(bf16x2, b), acc, false);
}
__device__ __forceinline__ float wave_sum(float v) {
  for (int o = 32; o > 0; o >>= 1) v += __shfl_xor(v, o);
  return v;
}
__device__ __forceinline__ float wave_max(float v) {
  for (int o = 32; o > 0; o >>= 1) v = fmaxf(v, __shfl_xor(v, o));
  return v;
}

__device__ void layernorm_to_lds(const unsigned short* x, float* xn) {
  int lane = threadIdx.x & 63;
  if (threadIdx.x < 64) {
    float v[8]; float s = 0.f;
    for (int j = 0; j < 8; ++j) { v[j] = bf2f(x[lane * 8 + j]); s += v[j]; }
    s = wave_sum(s);
    float mean = s * (1.f / D);
    float q = 0.f;
    for (int j = 0; j < 8; ++j) { float d = v[j] - mean; q += d * d; }
    q = wave_sum(q);
    float var = rbf(q * (1.f / D));
    float mb = rbf(mean);
    float den = rbf(sqrtf(rbf(var + 1e-5f)));
    for (int j = 0; j < 8; ++j) xn[lane * 8 + j] = rbf(rbf(v[j] - mb) / den);
  }
  __syncthreads();
}

__global__ void __launch_bounds__(256) k1(const unsigned short* x, const unsigned short* wq, const unsigned short* w1, float* q, float* h) {
  __shared__ float xn[D];
  layernorm_to_lds(x, xn);
  int b = blockIdx.x;
  if (b < 16) {
    int n = b * 256 + threadIdx.x;
    float a = 0.f;
    for (int k = 0; k < D; ++k) a = fmaf(xn[k], bf2f(wq[(size_t)k * 4096 + n]), a);
    q[n] = rbf(a);
  } else {
    int n = (b - 16) * 256 + threadIdx.x;
    if (n < F) {
      float a = 0.f;
      for (int k = 0; k < D; ++k) a = fmaf(xn[k], bf2f(w1[(size_t)k * F + n]), a);
      a = rbf(a);
      h[n] = rbf(a / (1.f + __expf(-a)));
    }
  }
}

__global__ void __launch_bounds__(256) k2(const unsigned short* kc, const unsigned short* vc, const float* q, float* part) {
  __shared__ float sm[4][132];
  int h = blockIdx.x / NSPLIT, s = blockIdx.x % NSPLIT;
  int lane = threadIdx.x & 63, w = threadIdx.x >> 6, r = lane >> 4, c = lane & 15;
  float qf[8];
  for (int j = 0; j < 8; ++j) qf[j] = q[h * DH + c * 8 + j];
  const int KPW = SK / NSPLIT / 4;
  size_t base = ((size_t)h * SK + s * (SK / NSPLIT) + w * KPW) * DH;
  float m = -INFINITY, l = 0.f, acc[8];
  for (int j = 0; j < 8; ++j) acc[j] = 0.f;
  for (int t = 0; t < KPW; t += 4) {
    u32x4 kk = *(const u32x4*)(kc + base + (size_t)(t + r) * DH + c * 8);
    u32x4 vv = *(const u32x4*)(vc + base + (size_t)(t + r) * DH + c * 8);
    float d = 0.f;
    for (int j = 0; j < 4; ++j) { d = fmaf(lo(kk[j]), qf[2 * j], d); d = fmaf(hi(kk[j]), qf[2 * j + 1], d); }
    for (int o = 8; o > 0; o >>= 1) d += __shfl_xor(d, o);
    float sc = rbf(rbf(d) * 0.08838834764831845f);
    float mn = fmaxf(m, sc);
    float al = __expf(m - mn), p = __expf(sc - mn);
    l = l * al + p; m = mn;
    for (int j = 0; j < 4; ++j) { acc[2 * j] = acc[2 * j] * al + p * lo(vv[j]); acc[2 * j + 1] = acc[2 * j + 1] * al + p * hi(vv[j]); }
  }
  float M = wave_max(m);
  float f = __expf(m - M);
  l *= f;
  l += __shfl_xor(l, 16); l += __shfl_xor(l, 32);
  for (int j = 0; j < 8; ++j) { acc[j] *= f; acc[j] += __shfl_xor(acc[j], 16); acc[j] += __shfl_xor(acc[j], 32); }
  if (r == 0) { for (int j = 0; j < 8; ++j) sm[w][c * 8 + j] = acc[j]; if (c == 0) { sm[w][128] = M; sm[w][129] = l; } }
  __syncthreads();
  if (threadIdx.x < 128) {
    float MM = fmaxf(fmaxf(sm[0][128], sm[1][128]), fmaxf(sm[2][128], sm[3][128]));
    float L = 0.f, o = 0.f;
    for (int i = 0; i < 4; ++i) { float g = __expf(sm[i][128] - MM); L += g * sm[i][129]; o += g * sm[i][threadIdx.x]; }
    float* p = part + (size_t)blockIdx.x * 132;
    p[threadIdx.x] = o;
    if (threadIdx.x == 0) { p[128] = MM; p[129] = L; }
  }
}

__global__ void __launch_bounds__(1024) k3(const unsigned short* x, const unsigned short* wo, const unsigned short* w2, const float* part, const float* hh, unsigned short* out) {
  __shared__ float o[4096];
  __shared__ float red[16][64], red2[16][64];
  for (int i = threadIdx.x; i < 4096; i += 1024) {
    int h = i >> 7, d = i & 127;
    const float* p = part + (size_t)h * NSPLIT * 132;
    float M = -INFINITY;
    for (int s = 0; s < NSPLIT; ++s) M = fmaxf(M, p[s * 132 + 128]);
    float L = 0.f, a = 0.f;
    for (int s = 0; s < NSPLIT; ++s) { float g = __expf(p[s * 132 + 128] - M); L += g * p[s * 132 + 129]; a += g * p[s * 132 + d]; }
    o[i] = rbf(a / L);
  }
  __syncthreads();
  int lane = threadIdx.x & 63, w = threadIdx.x >> 6;
  int col = blockIdx.x * 64 + lane;
  float a = 0.f;
  for (int k = w * 256; k < w * 256 + 256; ++k) a = fmaf(o[k], bf2f(wo[(size_t)k * D + col]), a);
  float z = 0.f;
  for (int k = w * 86; k < w * 86 + 86; ++k) z = fmaf(hh[k], bf2f(w2[(size_t)k * D + col]), z);
  red[w][lane] = a; red2[w][lane] = z;
  __syncthreads();
  if (w == 0) {
    float s = 0.f, zz = 0.f;
    for (int i = 0; i < 16; ++i) { s += red[i][lane]; zz += red2[i][lane]; }
    s = rbf(s); zz = rbf(zz);
    float g = rbf(zz / (1.f + __expf(-zz)));
    float r = rbf(bf2f(x[col]) + rbf(s * g));
    out[col] = (unsigned short)(__float_as_uint(r) >> 16);
  }
}

extern "C" void kernel_launch(void* const* d_in, const int* in_sizes, int n_in, void* d_out, int out_size, void* d_ws, size_t ws_size, hipStream_t stream) {
  const unsigned short* x = (const unsigned short*)d_in[0];
  const unsigned short* kc = (const unsigned short*)d_in[1];
  const unsigned short* vc = (const unsigned short*)d_in[2];
  const unsigned short* wq = (const unsigned short*)d_in[3];
  const unsigned short* wo = (const unsigned short*)d_in[4];
  const unsigned short* w1 = (const unsigned short*)d_in[6];
  const unsigned short* w2 = (const unsigned short*)d_in[7];
  float* ws = (float*)d_ws;
  float* q = ws; float* h = ws + 4096; float* part = ws + 8192;
  k1<<<22, 256, 0, stream>>>(x, wq, w1, q, h);
  k2<<<H * NSPLIT, 256, 0, stream>>>(kc, vc, q, part);
  k3<<<8, 1024, 0, stream>>>(x, wo, w2, part, h, (unsigned short*)d_out);
}
```

```cpp
#include <hip/hip_runtime.h>
#include <stdint.h>

typedef unsigned short u16;
typedef unsigned u32x4 __attribute__((ext_vector_type(4)));
typedef unsigned u32x2 __attribute__((ext_vector_type(2)));
typedef float f32x4 __attribute__((ext_vector_type(4)));
typedef float f32x2 __attribute__((ext_vector_type(2)));
typedef __bf16 bf16x8 __attribute__((ext_vector_type(8)));
typedef __bf16 bf16x2 __attribute__((ext_vector_type(2)));
#define GAS __attribute__((address_space(1)))

#define KV_NT 1
#define STAMPS 0

constexpr int D = 512, NH = 32, SK = 8192, DH = 128, FF = 1376, FC = 43;
constexpr float LOG2E = 1.4426950408889634f;
constexpr unsigned WS_EPOCH = 0;
constexpr unsigned WS_O = 4096;
constexpr unsigned WS_ML = WS_O + 256 * 1024;
constexpr unsigned WS_H = WS_ML + 256 * 16;
constexpr unsigned WS_Y = WS_H + 256 * 512;
constexpr unsigned WS_END = WS_Y + 256 * 1024;
constexpr unsigned WS_STAMP = 1u << 20;
constexpr unsigned SPIN_CAP = 1u << 18;

__device__ __forceinline__ float rbf(float f) { return (float)(__bf16)f; }
__device__ __forceinline__ float blo(unsigned u) { return __uint_as_float(u << 16); }
__device__ __forceinline__ float bhi(unsigned u) { return __uint_as_float(u & 0xffff0000u); }
__device__ __forceinline__ unsigned pkbf(float a, float b) { bf16x2 v = {(__bf16)a, (__bf16)b}; return __builtin_bit_cast(unsigned, v); }
__device__ __forceinline__ float dot2(unsigned a, unsigned b, float acc) {
  return __builtin_amdgcn_fdot2_f32_bf16(__builtin_bit_cast(bf16x2, a), __builtin_bit_cast(bf16x2, b), acc, false);
}
template <int CTRL> __device__ __forceinline__ float dppf(float x) {
  return __builtin_bit_cast(float, __builtin_amdgcn_mov_dpp(__builtin_bit_cast(int, x), CTRL, 0xf, 0xf, true));
}
template <int CTRL> __device__ __forceinline__ unsigned dppu(unsigned x) {
  return (unsigned)__builtin_amdgcn_mov_dpp((int)x, CTRL, 0xf, 0xf, true);
}
constexpr int XOR1 = 0xB1, XOR2 = 0x4E, XOR7 = 0x141, ROR8 = 0x128, ROR4 = 0x124, ROR2 = 0x122, ROR1 = 0x121;
__device__ __forceinline__ float xrow_sum(float x) {
  auto s = __builtin_amdgcn_permlane16_swap(__float_as_uint(x), __float_as_uint(x), false, false);
  x = __uint_as_float(s[0]) + __uint_as_float(s[1]);
  auto t = __builtin_amdgcn_permlane32_swap(__float_as_uint(x), __float_as_uint(x), false, false);
  return __uint_as_float(t[0]) + __uint_as_float(t[1]);
}
__device__ __forceinline__ float xrow_max(float x) {
  auto s = __builtin_amdgcn_permlane16_swap(__float_as_uint(x), __float_as_uint(x), false, false);
  x = fmaxf(__uint_as_float(s[0]), __uint_as_float(s[1]));
  auto t = __builtin_amdgcn_permlane32_swap(__float_as_uint(x), __float_as_uint(x), false, false);
  return fmaxf(__uint_as_float(t[0]), __uint_as_float(t[1]));
}
__device__ __forceinline__ float row_sum(float x) { x += dppf<XOR1>(x); x += dppf<XOR2>(x); x += dppf<XOR7>(x); x += dppf<ROR8>(x); return x; }
__device__ __forceinline__ float row_max(float x) { x = fmaxf(x, dppf<XOR1>(x)); x = fmaxf(x, dppf<XOR2>(x)); x = fmaxf(x, dppf<XOR7>(x)); x = fmaxf(x, dppf<ROR8>(x)); return x; }
__device__ __forceinline__ float wave_sum(float x) { return xrow_sum(row_sum(x)); }
__device__ __forceinline__ float wave_max(float x) { return xrow_max(row_max(x)); }
__device__ __forceinline__ float oct_sum(float x) { x += dppf<XOR1>(x); x += dppf<XOR2>(x); x += dppf<XOR7>(x); return x; }
__device__ __forceinline__ float oct_max(float x) { x = fmaxf(x, dppf<XOR1>(x)); x = fmaxf(x, dppf<XOR2>(x)); x = fmaxf(x, dppf<XOR7>(x)); return x; }
__device__ __forceinline__ f32x4 mfma(u32x4 a, u32x4 b, f32x4 c) {
  return __builtin_amdgcn_mfma_f32_16x16x32_bf16(__builtin_bit_cast(bf16x8, a), __builtin_bit_cast(bf16x8, b), c, 0, 0, 0);
}
__device__ __forceinline__ u32x4 ld16(const void* p) { return *(const GAS u32x4*)p; }
__device__ __forceinline__ u32x4 ld16s(const void* p) {
  return __builtin_nontemporal_load((const GAS u32x4*)p);
}
__device__ __forceinline__ float silu(float z) { return z / (1.f + __expf(-z)); }
#define SB() __builtin_amdgcn_sched_barrier(0)
#define STAMP(i) do {} while (0)

struct Lds {
  float part[16][128];
  float hpart[16][64];
  float ypart[32][64];
  float zpart[12][64];
  float wmax[16];
  float wsum[16];
  u16 qb[128];
};

__global__ void __launch_bounds__(1024) fused(const u16* __restrict__ x, const u16* __restrict__ kc, const u16* __restrict__ vc,
                                               const u16* __restrict__ wq, const u16* __restrict__ wo, const u16* __restrict__ w1,
                                               const u16* __restrict__ w2, u16* __restrict__ out, unsigned char* ws) {
  __shared__ __attribute__((aligned(16))) Lds L;
  const int b = blockIdx.x;
  const int h = (b & 7) * 4 + (b >> 6), s = (b >> 3) & 7;
  const int tid = threadIdx.x, lane = tid & 63, w = __builtin_amdgcn_readfirstlane(tid >> 6), r = lane >> 4, c = lane & 15;
  STAMP(0);
  const __amdgpu_buffer_rsrc_t rs = __builtin_amdgcn_make_buffer_rsrc(ws, 0, WS_END, 0x00020000);

  const unsigned epoch = __builtin_amdgcn_raw_buffer_load_b32(rs, WS_EPOCH, 0, 16);
  const u32x4 xa = ld16(x + lane * 8);
  const u32x4 xb = ld16(x + 32 * w + 8 * r);
  const u32x2 xc = *(const GAS u32x2*)(x + 64 * s + 4 * w);
  u32x4 wqv[8];
#pragma unroll
  for (int i = 0; i < 8; ++i) wqv[i] = ld16(wq + (size_t)(32 * w + 8 * r + i) * 4096 + h * DH + 8 * c);
  u16 w1v[4];
  {
    const int cc = lane < FC ? lane : FC - 1;
#pragma unroll
    for (int i = 0; i < 4; ++i) w1v[i] = *(const GAS u16*)(w1 + (size_t)(64 * s + 4 * w + i) * FF + FC * h + cc);
  }
  SB();
  const size_t kvoff = ((size_t)h * SK + s * 1024 + w * 64 + r) * DH + 8 * c;
  u32x4 kv[16];
#pragma unroll
  for (int t = 0; t < 16; ++t) kv[t] = ld16s(kc + kvoff + t * 4 * DH);
  SB();

  float xv[8];
#pragma unroll
  for (int j = 0; j < 4; ++j) { xv[2 * j] = blo(xa[j]); xv[2 * j + 1] = bhi(xa[j]); }
  float sm = 0.f;
#pragma unroll
  for (int j = 0; j < 8; ++j) sm += xv[j];
  const float mean = wave_sum(sm) * (1.f / D);
  float sq = 0.f;
#pragma unroll
  for (int j = 0; j < 8; ++j) { const float d = xv[j] - mean; sq = fmaf(d, d, sq); }
  const float var = rbf(wave_sum(sq) * (1.f / D));
  const float mb = rbf(mean);
  const float den = rbf(__builtin_sqrtf(rbf(var + 1e-5f)));
  u32x4 mk;
#pragma unroll
  for (int i = 0; i < 4; ++i) mk[i] = (c < 8 && (c >> 1) == i) ? ((c & 1) ? 0xffff0000u : 0x0000ffffu) : 0u;
  f32x4 qa = {0.f, 0.f, 0.f, 0.f};
#pragma unroll
  for (int i = 0; i < 8; ++i) {
    const float xi = (i & 1) ? bhi(xb[i >> 1]) : blo(xb[i >> 1]);
    const float xn = rbf(xi - mb) / den;
    const unsigned pk = pkbf(xn, xn);
    const u32x4 a = {pk & mk[0], pk & mk[1], pk & mk[2], pk & mk[3]};
    qa = mfma(a, wqv[i], qa);
  }
  float hp = 0.f;
#pragma unroll
  for (int i = 0; i < 4; ++i) {
    const float xi = (i & 1) ? bhi(xc[i >> 1]) : blo(xc[i >> 1]);
    const float xn = rbf(rbf(xi - mb) / den);
    hp = fmaf(xn, blo(w1v[i]), hp);
  }
  if (lane < 32) *(f32x4*)&L.part[w][8 * c + 4 * r] = qa;
  L.hpart[w][lane] = hp;
  SB();
  u32x4 va[8];
#pragma unroll
  for (int t = 0; t < 8; ++t) va[t] = ld16s(vc + kvoff + t * 4 * DH);
  SB();
  STAMP(1);
  __syncthreads();
  const unsigned tag = epoch + 1u;
  if (w < 2) {
    float a = 0.f;
#pragma unroll
    for (int i = 0; i < 16; ++i) a += L.part[i][tid];
    L.qb[tid] = __builtin_bit_cast(u16, (__bf16)a);
  } else if (w == 2) {
    float a = 0.f;
#pragma unroll
    for (int i = 0; i < 16; ++i) a += L.hpart[i][lane];
    const u32x2 g = {__float_as_uint(a), tag};
    __builtin_amdgcn_raw_buffer_store_b64(g, rs, WS_H + ((h * 8 + s) * 64 + lane) * 8, 0, 16);
  }
  __syncthreads();
  const u32x4 qp = *(const u32x4*)&L.qb[8 * c];
  STAMP(2);

  float xs[16];
#pragma unroll
  for (int t = 0; t < 16; ++t) {
    float d = dot2(kv[t][0], qp[0], 0.f);
    d = dot2(kv[t][1], qp[1], d); d = dot2(kv[t][2], qp[2], d); d = dot2(kv[t][3], qp[3], d);
    xs[t] = d;
  }
  SB();
  u32x4 vb[8];
#pragma unroll
  for (int t = 0; t < 8; ++t) vb[t] = ld16s(vc + kvoff + (t + 8) * 4 * DH);
  const u32x4 wov = ld16(wo + (size_t)(h * DH + 8 * w + (lane >> 3)) * D + 64 * s + 8 * (lane & 7));
  u32x4 w2v;
  {
    int row = 8 * w + (lane >> 3); row = row < FC ? row : FC - 1;
    w2v = ld16(w2 + (size_t)(FC * h + row) * D + 64 * s + 8 * (lane & 7));
  }
  SB();
#define TR_STEP(HALF, CTRL, BIT) _Pragma("unroll") for (int i = 0; i < (HALF); ++i) { \
    const float t1 = xs[i] + dppf<CTRL>(xs[i]), t2 = xs[i + (HALF)] + dppf<CTRL>(xs[i + (HALF)]); xs[i] = (c & (BIT)) ? t2 : t1; }
  TR_STEP(8, ROR8, 8) TR_STEP(4, XOR7, 4) TR_STEP(2, XOR2, 2) TR_STEP(1, XOR1, 1)
#undef TR_STEP
  const float sc = rbf(rbf(xs[0]) * 0.08838834764831845f);
  {
    const float wm = wave_max(sc);
    if (lane == 0) L.wmax[w] = wm;
  }
  STAMP(3);
  __syncthreads();
  const float M = row_max(L.wmax[c]);
  const float p = __builtin_amdgcn_exp2f((sc - M) * LOG2E);
  {
    const float ws_ = wave_sum(p);
    if (lane == 0) L.wsum[w] = ws_;
  }
  const unsigned pp = pkbf(p, p);
  f32x4 oa = {0.f, 0.f, 0.f, 0.f};
#define PV(T, V) { const unsigned pb = dppu<0x150 + (T)>(pp); const u32x4 a = {pb & mk[0], pb & mk[1], pb & mk[2], pb & mk[3]}; oa = mfma(a, V, oa); }
  PV(0, va[0]) PV(1, va[1]) PV(2, va[2]) PV(3, va[3]) PV(4, va[4]) PV(5, va[5]) PV(6, va[6]) PV(7, va[7])
  PV(8, vb[0]) PV(9, vb[1]) PV(10, vb[2]) PV(11, vb[3]) PV(12, vb[4]) PV(13, vb[5]) PV(14, vb[6]) PV(15, vb[7])
#undef PV
  if (lane < 32) *(f32x4*)&L.part[w][8 * c + 4 * r] = oa;
  STAMP(4);
  __syncthreads();
  STAMP(5);
  if (w == 0) {
    f32x2 a = {0.f, 0.f};
#pragma unroll
    for (int i = 0; i < 16; ++i) a += *(const f32x2*)&L.part[i][2 * lane];
    const u32x4 g = {__float_as_uint(a[0]), tag, __float_as_uint(a[1]), tag};
    __builtin_amdgcn_raw_buffer_store_b128(g, rs, WS_O + (h * 8 + s) * 1024 + lane * 16, 0, 16);
  } else if (w == 1) {
    const float l = row_sum(L.wsum[c]);
    if (lane == 0) {
      const u32x4 g = {__float_as_uint(M), tag, __float_as_uint(l), tag};
      __builtin_amdgcn_raw_buffer_store_b128(g, rs, WS_ML + (h * 8 + s) * 16, 0, 16);
    }
  }
  {
    const int sp = lane & 7, row = 8 * w + (lane >> 3);
    u32x2 go, gh; u32x4 gm;
    for (unsigned spins = 0;; ++spins) {
      go = __builtin_amdgcn_raw_buffer_load_b64(rs, WS_O + ((h * 8 + sp) * 128 + row) * 8, 0, 16);
      gm = __builtin_amdgcn_raw_buffer_load_b128(rs, WS_ML + (h * 8 + sp) * 16, 0, 16);
      gh = __builtin_amdgcn_raw_buffer_load_b64(rs, WS_H + ((h * 8 + sp) * 64 + (row & 63)) * 8, 0, 16);
      const bool ok = go[1] == tag && gm[1] == tag && gm[3] == tag && gh[1] == tag;
      if (__all(ok) || spins > SPIN_CAP) break;
      asm volatile("s_sleep 1" ::: "memory");
    }
    STAMP(6);
    const float ms = __uint_as_float(gm[0]);
    const float MM = oct_max(ms);
    const float f = __builtin_amdgcn_exp2f((ms - MM) * LOG2E);
    const float num = oct_sum(f * __uint_as_float(go[0]));
    const float dn = oct_sum(f * __uint_as_float(gm[2]));
    const float o = num / dn;
    u32x4 m8;
#pragma unroll
    for (int i = 0; i < 4; ++i) m8[i] = (((c & 7) >> 1) == i) ? ((c & 1) ? 0xffff0000u : 0x0000ffffu) : 0u;
    const unsigned po = pkbf(o, o);
    const u32x4 a = {po & m8[0], po & m8[1], po & m8[2], po & m8[3]};
    const f32x4 z4 = {0.f, 0.f, 0.f, 0.f};
    const f32x4 y = mfma(a, wov, z4);
    const bool valid = (r >> 1) == (c >> 3);
    if (valid) *(f32x4*)&L.ypart[2 * w + (r >> 1)][8 * (c & 7) + 4 * (r & 1)] = y;
    if (w < 6) {
      const float hpre = rbf(oct_sum(__uint_as_float(gh[0])));
      float hv = silu(hpre);
      hv = row < FC ? hv : 0.f;
      const unsigned ph = pkbf(hv, hv);
      const u32x4 a2 = {ph & m8[0], ph & m8[1], ph & m8[2], ph & m8[3]};
      const f32x4 zz = mfma(a2, w2v, z4);
      if (valid) *(f32x4*)&L.zpart[2 * w + (r >> 1)][8 * (c & 7) + 4 * (r & 1)] = zz;
    }
  }
  __syncthreads();
  STAMP(7);
  if (w == 0) {
    const float* src = (lane & 1) ? &L.zpart[0][lane & 62] : &L.ypart[0][lane & 62];
    f32x2 a = {0.f, 0.f};
#pragma unroll
    for (int i = 0; i < 12; ++i) a += *(const f32x2*)(src + i * 64);
    if (!(lane & 1)) {
#pragma unroll
      for (int i = 12; i < 32; ++i) a += *(const f32x2*)(src + i * 64);
    }
    const u32x4 g = {__float_as_uint(a[0]), tag, __float_as_uint(a[1]), tag};
    __builtin_amdgcn_raw_buffer_store_b128(g, rs, WS_Y + ((s * 32 + (lane >> 1)) * 32 + h) * 32 + (lane & 1) * 16, 0, 16);
    const unsigned xo = *(const GAS unsigned*)(x + 64 * s + 2 * h);
    u32x4 gy;
    for (unsigned spins = 0;; ++spins) {
      gy = __builtin_amdgcn_raw_buffer_load_b128(rs, WS_Y + (s * 32 + h) * 1024 + lane * 16, 0, 16);
      const bool ok = gy[1] == tag && gy[3] == tag;
      if (__all(ok) || spins > SPIN_CAP) break;
      asm volatile("s_sleep 1" ::: "memory");
    }
    STAMP(8);
    float v0 = __uint_as_float(gy[0]), v1 = __uint_as_float(gy[2]);
    v0 += dppf<ROR2>(v0); v1 += dppf<ROR2>(v1);
    v0 += dppf<ROR4>(v0); v1 += dppf<ROR4>(v1);
    v0 += dppf<ROR8>(v0); v1 += dppf<ROR8>(v1);
    v0 = rbf(xrow_sum(v0)); v1 = rbf(xrow_sum(v1));
    const float g0 = dppf<XOR1>(rbf(silu(v0))), g1 = dppf<XOR1>(rbf(silu(v1)));
    const float o0 = blo(xo) + rbf(v0 * g0), o1 = bhi(xo) + rbf(v1 * g1);
    if (lane == 0) {
      *(GAS unsigned*)(out + 64 * s + 2 * h) = pkbf(o0, o1);
      if (b == 0) __builtin_amdgcn_raw_buffer_store_b32(tag, rs, WS_EPOCH, 0, 16);
    }
    STAMP(9);
  }
}

extern "C" void kernel_launch(void* const* d_in, const int* in_sizes, int n_in, void* d_out, int out_size, void* d_ws, size_t ws_size, hipStream_t stream) {
  fused<<<256, 1024, 0, stream>>>((const u16*)d_in[0], (const u16*)d_in[1], (const u16*)d_in[2], (const u16*)d_in[3], (const u16*)d_in[4],
                                  (const u16*)d_in[6], (const u16*)d_in[7], (u16*)d_out, (unsigned char*)d_ws);
}
```

```cpp
#include <hip/hip_runtime.h>
#include <stdint.h>

typedef unsigned short u16;
typedef unsigned u32x4 __attribute__((ext_vector_type(4)));
typedef unsigned u32x2 __attribute__((ext_vector_type(2)));
typedef float f32x4 __attribute__((ext_vector_type(4)));
typedef float f32x2 __attribute__((ext_vector_type(2)));
typedef __bf16 bf16x8 __attribute__((ext_vector_type(8)));
typedef __bf16 bf16x2 __attribute__((ext_vector_type(2)));
typedef __amdgpu_buffer_rsrc_t rsrc_t;
#define GAS __attribute__((address_space(1)))

#define KV_AUX 2
#define W_AUX 2
#define STAMPS 0

constexpr int D = 512, SK = 8192, DH = 128, FF = 1376;
constexpr float LOG2E = 1.4426950408889634f;
constexpr int SC1 = 16;
constexpr unsigned WS_EPOCH = 0;
constexpr unsigned WS_Q = 4096;
constexpr unsigned WS_H = WS_Q + 256 * 1024;
constexpr unsigned WS_ML = WS_H + 256 * 512;
constexpr unsigned WS_O = WS_ML + 256 * 16;
constexpr unsigned WS_Y = WS_O + 256 * 1024;
constexpr unsigned WS_END = WS_Y + 256 * 1024;
constexpr unsigned WS_STAMP = 1u << 20;
constexpr unsigned SPIN_CAP = 1u << 18;

__device__ __forceinline__ float rbf(float f) { return (float)(__bf16)f; }
__device__ __forceinline__ float blo(unsigned u) { return __uint_as_float(u << 16); }
__device__ __forceinline__ float bhi(unsigned u) { return __uint_as_float(u & 0xffff0000u); }
__device__ __forceinline__ unsigned pkbf(float a, float b) { bf16x2 v = {(__bf16)a, (__bf16)b}; return __builtin_bit_cast(unsigned, v); }
__device__ __forceinline__ float dot2(unsigned a, unsigned b, float acc) {
  return __builtin_amdgcn_fdot2_f32_bf16(__builtin_bit_cast(bf16x2, a), __builtin_bit_cast(bf16x2, b), acc, false);
}
template <int CTRL> __device__ __forceinline__ float dppf(float x) {
  return __builtin_bit_cast(float, __builtin_amdgcn_mov_dpp(__builtin_bit_cast(int, x), CTRL, 0xf, 0xf, true));
}
template <int CTRL> __device__ __forceinline__ unsigned dppu(unsigned x) {
  return (unsigned)__builtin_amdgcn_mov_dpp((int)x, CTRL, 0xf, 0xf, true);
}
constexpr int XOR1 = 0xB1, XOR2 = 0x4E, XOR7 = 0x141, ROR8 = 0x128, ROR4 = 0x124, ROR2 = 0x122;
__device__ __forceinline__ float x16_sum(float x) {
  auto s = __builtin_amdgcn_permlane16_swap(__float_as_uint(x), __float_as_uint(x), false, false);
  return __uint_as_float(s[0]) + __uint_as_float(s[1]);
}
__device__ __forceinline__ float x32_sum(float x) {
  auto t = __builtin_amdgcn_permlane32_swap(__float_as_uint(x), __float_as_uint(x), false, false);
  return __uint_as_float(t[0]) + __uint_as_float(t[1]);
}
__device__ __forceinline__ float xrow_sum(float x) { return x32_sum(x16_sum(x)); }
__device__ __forceinline__ float xrow_max(float x) {
  auto s = __builtin_amdgcn_permlane16_swap(__float_as_uint(x), __float_as_uint(x), false, false);
  x = fmaxf(__uint_as_float(s[0]), __uint_as_float(s[1]));
  auto t = __builtin_amdgcn_permlane32_swap(__float_as_uint(x), __float_as_uint(x), false, false);
  return fmaxf(__uint_as_float(t[0]), __uint_as_float(t[1]));
}
__device__ __forceinline__ float row_sum(float x) { x += dppf<XOR1>(x); x += dppf<XOR2>(x); x += dppf<XOR7>(x); x += dppf<ROR8>(x); return x; }
__device__ __forceinline__ float row_max(float x) { x = fmaxf(x, dppf<XOR1>(x)); x = fmaxf(x, dppf<XOR2>(x)); x = fmaxf(x, dppf<XOR7>(x)); x = fmaxf(x, dppf<ROR8>(x)); return x; }
__device__ __forceinline__ float wave_sum(float x) { return xrow_sum(row_sum(x)); }
__device__ __forceinline__ float wave_max(float x) { return xrow_max(row_max(x)); }
__device__ __forceinline__ float oct_sum(float x) { x += dppf<XOR1>(x); x += dppf<XOR2>(x); x += dppf<XOR7>(x); return x; }
__device__ __forceinline__ float oct_max(float x) { x = fmaxf(x, dppf<XOR1>(x)); x = fmaxf(x, dppf<XOR2>(x)); x = fmaxf(x, dppf<XOR7>(x)); return x; }
__device__ __forceinline__ f32x4 mfma(u32x4 a, u32x4 b, f32x4 c) {
  return __builtin_amdgcn_mfma_f32_16x16x32_bf16(__builtin_bit_cast(bf16x8, a), __builtin_bit_cast(bf16x8, b), c, 0, 0, 0);
}
__device__ __forceinline__ u32x4 diag(unsigned pk, const u32x4& m) { return u32x4{pk & m[0], pk & m[1], pk & m[2], pk & m[3]}; }
__device__ __forceinline__ float silu(float z) { return z * __builtin_amdgcn_rcpf(1.f + __builtin_amdgcn_exp2f(-z * LOG2E)); }
__device__ __forceinline__ rsrc_t mk_rsrc(const void* p, unsigned bytes) { return __builtin_amdgcn_make_buffer_rsrc((void*)p, 0, bytes, 0x00020000); }
#define LD128(rs, vo, so, aux) __builtin_amdgcn_raw_buffer_load_b128(rs, vo, so, aux)
#define LD64(rs, vo, so, aux) __builtin_amdgcn_raw_buffer_load_b64(rs, vo, so, aux)
#define ST128(v, rs, vo, so) __builtin_amdgcn_raw_buffer_store_b128(v, rs, vo, so, SC1)
#define SB() __builtin_amdgcn_sched_barrier(0)
#define MEMFENCE() asm volatile("" ::: "memory")
#define STAMP(i) do {} while (0)
#define STAMPW(i, wv) do {} while (0)

struct Lds {
  float part[16][128];
  float ypart[32][64];
  float wmax[16];
  float wsum[16];
  unsigned qb[64];
  unsigned tag;
};

__device__ __forceinline__ void ln_stats(const u32x4 xa, float& mb, float& rden) {
  float xv[8];
#pragma unroll
  for (int j = 0; j < 4; ++j) { xv[2 * j] = blo(xa[j]); xv[2 * j + 1] = bhi(xa[j]); }
  float sm = ((xv[0] + xv[1]) + (xv[2] + xv[3])) + ((xv[4] + xv[5]) + (xv[6] + xv[7]));
  const float mean = wave_sum(sm) * (1.f / D);
  float sq = 0.f;
#pragma unroll
  for (int j = 0; j < 8; ++j) { const float d = xv[j] - mean; sq = fmaf(d, d, sq); }
  const float var = rbf(wave_sum(sq) * (1.f / D));
  mb = rbf(mean);
  rden = __builtin_amdgcn_rcpf(rbf(__builtin_amdgcn_sqrtf(rbf(var + 1e-5f))));
}
__device__ __forceinline__ unsigned xn_pk(float xi, float mb, float rden) { const float v = rbf(xi - mb) * rden; return pkbf(v, v); }

__global__ void __launch_bounds__(1024) fused(const u16* __restrict__ x, const u16* __restrict__ kc, const u16* __restrict__ vc,
                                               const u16* __restrict__ wq, const u16* __restrict__ wo, const u16* __restrict__ w1,
                                               const u16* __restrict__ w2, u16* __restrict__ out, unsigned char* ws) {
  __shared__ __attribute__((aligned(16))) Lds L;
  const int b = blockIdx.x;
  const int h = (b & 7) * 4 + (b >> 6), s = (b >> 3) & 7;
  const int tid = threadIdx.x, lane = tid & 63, w = __builtin_amdgcn_readfirstlane(tid >> 6), r = lane >> 4, c = lane & 15;
  STAMPW(0, 0);
  const rsrc_t rs = mk_rsrc(ws, WS_END);
  const rsrc_t rk = mk_rsrc(kc, 64u << 20), rv = mk_rsrc(vc, 64u << 20);
  u32x4 mk, m8;
#pragma unroll
  for (int i = 0; i < 4; ++i) {
    m8[i] = (((c & 7) >> 1) == i) ? ((c & 1) ? 0xffff0000u : 0x0000ffffu) : 0u;
    mk[i] = c < 8 ? m8[i] : 0u;
  }
  const f32x4 z4 = {0.f, 0.f, 0.f, 0.f};
  const bool valid8 = (r >> 1) == (c >> 3);

  if (w == 0) {
    __builtin_amdgcn_s_setprio(3);
    const rsrc_t rx = mk_rsrc(x, 1024), rq = mk_rsrc(wq, 4u << 20);
    unsigned tag = __builtin_amdgcn_raw_buffer_load_b32(rs, WS_EPOCH, 0, SC1);
    const u32x4 xa = LD128(rx, lane * 16, 0, 0);
    const u32x4 xb0 = LD128(rx, r * 32, s * 128, 0), xb1 = LD128(rx, r * 32 + 16, s * 128, 0);
    u32x4 wv[16];
#pragma unroll
    for (int i = 0; i < 16; ++i) wv[i] = LD128(rq, r * (16 * 8192) + c * 16, (64 * s + i) * 8192 + h * 256, W_AUX);
    SB();
    asm volatile("" : "+v"(tag));
    tag += 1u;
    float mb, rden; ln_stats(xa, mb, rden);
    f32x4 qa = z4;
#pragma unroll
    for (int i = 0; i < 16; ++i) {
      const unsigned u = i < 8 ? xb0[i >> 1] : xb1[(i - 8) >> 1];
      qa = mfma(diag(xn_pk((i & 1) ? bhi(u) : blo(u), mb, rden), mk), wv[i], qa);
    }
    if (lane < 32) {
      const int off = WS_Q + (8 * c + 4 * r) * 8;
      ST128((u32x4{__float_as_uint(qa[0]), tag, __float_as_uint(qa[1]), tag}), rs, off, (h * 8 + s) * 1024);
      ST128((u32x4{__float_as_uint(qa[2]), tag, __float_as_uint(qa[3]), tag}), rs, off + 16, (h * 8 + s) * 1024);
    }
    STAMP(1);
    u32x4 g[8];
    for (unsigned spins = 0;; ++spins) {
      bool ok = true;
#pragma unroll
      for (int i = 0; i < 8; ++i) g[i] = LD128(rs, WS_Q + h * 8192 + lane * 16 + i * 1024, 0, SC1);
#pragma unroll
      for (int i = 0; i < 8; ++i) ok &= (g[i][1] == tag) & (g[i][3] == tag);
      if (__all(ok) || spins > SPIN_CAP) break;
      asm volatile("s_sleep 4" ::: "memory");
    }
    float q0 = 0.f, q1 = 0.f;
#pragma unroll
    for (int i = 0; i < 8; ++i) { q0 += __uint_as_float(g[i][0]); q1 += __uint_as_float(g[i][2]); }
    L.qb[lane] = pkbf(q0, q1);
    if (lane == 0) L.tag = tag;
    STAMP(2);
  } else if (w == 1) {
    __builtin_amdgcn_s_setprio(3);
    const int nch = h < 12 ? 6 : 5, ch0 = h < 12 ? 6 * h : 5 * h + 12;
    const rsrc_t rx = mk_rsrc(x, 1024), r1 = mk_rsrc(w1, 512 * FF * 2), r2 = mk_rsrc(w2, 512 * FF * 2);
    const int r8 = lane >> 3, c8 = lane & 7;
    unsigned tag = __builtin_amdgcn_raw_buffer_load_b32(rs, WS_EPOCH, 0, SC1);
    const u32x4 xa = LD128(rx, lane * 16, 0, 0);
    const u32x4 xb = LD128(rx, r8 * 16, s * 128, 0);
    u32x4 wv[8], w2v[6];
    const int cc = c8 < nch ? c8 : nch - 1;
#pragma unroll
    for (int i = 0; i < 8; ++i) wv[i] = LD128(r1, r8 * (8 * FF * 2) + cc * 16, (64 * s + i) * (FF * 2) + ch0 * 16, W_AUX);
#pragma unroll
    for (int i = 0; i < 6; ++i) w2v[i] = LD128(r2, r8 * 1024 + c8 * 16, (ch0 + (i < nch ? i : nch - 1)) * 8192 + s * 128, W_AUX);
    SB();
    asm volatile("" : "+v"(tag));
    tag += 1u;
    float mb, rden; ln_stats(xa, mb, rden);
    f32x4 ha = z4;
#pragma unroll
    for (int i = 0; i < 8; ++i) ha = mfma(diag(xn_pk((i & 1) ? bhi(xb[i >> 1]) : blo(xb[i >> 1]), mb, rden), m8), wv[i], ha);
#pragma unroll
    for (int j = 0; j < 4; ++j) { float v = valid8 ? ha[j] : 0.f; v += dppf<ROR8>(v); ha[j] = x32_sum(v); }
    if (lane < 32 && c < 8) {
      const int off = WS_H + (8 * c + 4 * r) * 8;
      ST128((u32x4{__float_as_uint(ha[0]), tag, __float_as_uint(ha[1]), tag}), rs, off, (h * 8 + s) * 512);
      ST128((u32x4{__float_as_uint(ha[2]), tag, __float_as_uint(ha[3]), tag}), rs, off + 16, (h * 8 + s) * 512);
    }
    u32x2 g[6];
    for (unsigned spins = 0;; ++spins) {
      bool ok = true;
#pragma unroll
      for (int i = 0; i < 6; ++i) g[i] = LD64(rs, WS_H + h * 4096 + c8 * 512 + r8 * 8 + i * 64, 0, SC1);
#pragma unroll
      for (int i = 0; i < 6; ++i) ok &= g[i][1] == tag;
      if (__all(ok) || spins > SPIN_CAP) break;
      asm volatile("s_sleep 4" ::: "memory");
    }
    f32x4 za = z4;
#pragma unroll
    for (int i = 0; i < 6; ++i) {
      float hv = silu(rbf(oct_sum(__uint_as_float(g[i][0]))));
      hv = i < nch ? hv : 0.f;
      za = mfma(diag(pkbf(hv, hv), m8), w2v[i], za);
    }
#pragma unroll
    for (int j = 0; j < 4; ++j) { float v = valid8 ? za[j] : 0.f; v += dppf<ROR8>(v); za[j] = x32_sum(v); }
    if (lane < 32 && c < 8) {
      const int off = WS_Y + (4 * c + 2 * r) * 1024 + 16;
      ST128((u32x4{__float_as_uint(za[0]), tag, __float_as_uint(za[1]), tag}), rs, off, s * 32768 + h * 32);
      ST128((u32x4{__float_as_uint(za[2]), tag, __float_as_uint(za[3]), tag}), rs, off + 1024, s * 32768 + h * 32);
    }
    STAMP(10);
  }

  const int kvs = (h * SK + s * 1024 + w * 64) * 256, kvv = lane * 16;
  u32x4 kt[16], vt[16];
#pragma unroll
  for (int t = 0; t < 16; ++t) kt[t] = LD128(rk, kvv + (t & 3) * 1024, kvs + (t >> 2) * 4096, KV_AUX);
#pragma unroll
  for (int t = 0; t < 4; ++t) vt[t] = LD128(rv, kvv + (t & 3) * 1024, kvs + (t >> 2) * 4096, KV_AUX);
  SB();
  __syncthreads();
  STAMPW(3, 0);
  const u32x4 qp = *(const u32x4*)&L.qb[4 * c];
  const unsigned tag = L.tag;
  float xs[16];
#pragma unroll
  for (int t = 0; t < 16; ++t) {
    float d = dot2(kt[t][0], qp[0], 0.f);
    d = dot2(kt[t][1], qp[1], d); d = dot2(kt[t][2], qp[2], d); d = dot2(kt[t][3], qp[3], d);
    xs[t] = d;
  }
#define TR_STEP(HALF, CTRL, BIT) _Pragma("unroll") for (int i = 0; i < (HALF); ++i) { \
    const float t1 = xs[i] + dppf<CTRL>(xs[i]), t2 = xs[i + (HALF)] + dppf<CTRL>(xs[i + (HALF)]); xs[i] = (c & (BIT)) ? t2 : t1; }
  TR_STEP(8, ROR8, 8) TR_STEP(4, XOR7, 4) TR_STEP(2, XOR2, 2) TR_STEP(1, XOR1, 1)
#undef TR_STEP
  const float sc = rbf(rbf(xs[0]) * 0.08838834764831845f) * LOG2E;
  {
    const float wm = wave_max(sc);
    const float wsu = wave_sum(__builtin_amdgcn_exp2f(sc - wm));
    if (lane == 0) { L.wmax[w] = wm; L.wsum[w] = wsu; }
  }
  SB();
#pragma unroll
  for (int t = 4; t < 8; ++t) { vt[t] = LD128(rv, kvv + (t & 3) * 1024, kvs + (t >> 2) * 4096, KV_AUX); SB(); }
  SB();
  __syncthreads();
  STAMPW(4, 0);
  const float wmc = L.wmax[c];
  const float M = row_max(wmc);
  if (w == 0) {
    const float l = row_sum(L.wsum[c] * __builtin_amdgcn_exp2f(wmc - M));
    if (lane == 0) ST128((u32x4{__float_as_uint(M), tag, __float_as_uint(l), tag}), rs, WS_ML, (h * 8 + s) * 16);
  }
  SB();
#pragma unroll
  for (int t = 8; t < 16; ++t) { vt[t] = LD128(rv, kvv + (t & 3) * 1024, kvs + (t >> 2) * 4096, KV_AUX); SB(); }
  SB();
  const float p = __builtin_amdgcn_exp2f(sc - M);
  const unsigned pp = pkbf(p, p);
  f32x4 oa = z4;
#define PV(T) oa = mfma(diag(dppu<0x150 + (T)>(pp), mk), vt[T], oa);
  PV(0) PV(1) PV(2) PV(3) PV(4) PV(5) PV(6) PV(7) PV(8) PV(9) PV(10) PV(11)
  SB();
  u32x4 gm = LD128(rs, WS_ML + (lane & 7) * 16, w == 0 ? h * 128 : 0x40000000, SC1);
  SB();
  const u32x4 wov = LD128(mk_rsrc(wo, 4u << 20), (lane >> 3) * 1024 + (lane & 7) * 16, (h * DH + 8 * w) * 1024 + s * 128, W_AUX);
  SB();
  PV(12) PV(13) PV(14) PV(15)
#undef PV
  if (lane < 32) *(f32x4*)&L.part[w][8 * c + 4 * r] = oa;
  float fown = 0.f;
  if (w == 0) {
    for (unsigned spins = 0; !__all(gm[1] == tag && gm[3] == tag) && spins < SPIN_CAP; ++spins) {
      asm volatile("s_sleep 2" ::: "memory");
      gm = LD128(rs, WS_ML + (lane & 7) * 16, h * 128, SC1);
    }
    const float ms = __uint_as_float(gm[0]);
    const float MM = oct_max(ms);
    const float Lh = oct_sum(__builtin_amdgcn_exp2f(ms - MM) * __uint_as_float(gm[2]));
    fown = __builtin_amdgcn_exp2f(M - MM) / Lh;
  }
  STAMPW(5, 0);
  __syncthreads();
  STAMPW(6, 2);
  if (w == 0) {
    f32x2 a = {0.f, 0.f};
#pragma unroll
    for (int i = 0; i < 16; ++i) a += *(const f32x2*)&L.part[i][2 * lane];
    ST128((u32x4{__float_as_uint(a[0] * fown), tag, __float_as_uint(a[1] * fown), tag}), rs, WS_O + lane * 16, (h * 8 + s) * 1024);
  }
  {
    const int off = WS_O + (lane & 7) * 1024 + (lane >> 3) * 8;
    u32x2 go = LD64(rs, off, h * 8192 + w * 64, SC1);
    for (unsigned spins = 0; spins < SPIN_CAP; ++spins) {
      const bool ok = go[1] == tag;
      if (__all(ok)) break;
      asm volatile("s_sleep 2" ::: "memory");
      if (!ok) go = LD64(rs, off, h * 8192 + w * 64, SC1);
    }
    STAMPW(7, 2);
    const float o = oct_sum(__uint_as_float(go[0]));
    const f32x4 y = mfma(diag(pkbf(o, o), m8), wov, z4);
    if (valid8) *(f32x4*)&L.ypart[2 * w + (r >> 1)][8 * (c & 7) + 4 * (r & 1)] = y;
  }
  __syncthreads();
  STAMPW(8, 4);
  if (w < 4) {
    const float* src = &L.ypart[4 * (lane >> 3)][16 * w + 2 * (lane & 7)];
    f32x2 a = *(const f32x2*)src + *(const f32x2*)(src + 64) + (*(const f32x2*)(src + 128) + *(const f32x2*)(src + 192));
    a[0] += dppf<ROR8>(a[0]); a[1] += dppf<ROR8>(a[1]);
    a[0] = xrow_sum(a[0]); a[1] = xrow_sum(a[1]);
    if (lane < 8) ST128((u32x4{__float_as_uint(a[0]), tag, __float_as_uint(a[1]), tag}), rs, WS_Y + lane * 1024, s * 32768 + w * 8192 + h * 32);
  }
  if (w == 0) {
    const unsigned xo = *(const GAS unsigned*)(x + 64 * s + 2 * h);
    u32x4 gy = LD128(rs, WS_Y + lane * 16, (s * 32 + h) * 1024, SC1);
    for (unsigned spins = 0; spins < SPIN_CAP; ++spins) {
      const bool ok = gy[1] == tag && gy[3] == tag;
      if (__all(ok)) break;
      asm volatile("s_sleep 2" ::: "memory");
      if (!ok) gy = LD128(rs, WS_Y + lane * 16, (s * 32 + h) * 1024, SC1);
    }
    STAMP(9);
    float v0 = __uint_as_float(gy[0]), v1 = __uint_as_float(gy[2]);
    v0 += dppf<ROR2>(v0); v1 += dppf<ROR2>(v1);
    v0 += dppf<ROR4>(v0); v1 += dppf<ROR4>(v1);
    v0 += dppf<ROR8>(v0); v1 += dppf<ROR8>(v1);
    v0 = rbf(xrow_sum(v0)); v1 = rbf(xrow_sum(v1));
    const float g0 = dppf<XOR1>(rbf(silu(v0))), g1 = dppf<XOR1>(rbf(silu(v1)));
    const float o0 = blo(xo) + rbf(v0 * g0), o1 = bhi(xo) + rbf(v1 * g1);
    if (lane == 0) {
      *(GAS unsigned*)(out + 64 * s + 2 * h) = pkbf(o0, o1);
      if (b == 0) __builtin_amdgcn_raw_buffer_store_b32(tag, rs, WS_EPOCH, 0, SC1);
    }
    STAMP(11);
  }
}

extern "C" void kernel_launch(void* const* d_in, const int* in_sizes, int n_in, void* d_out, int out_size, void* d_ws, size_t ws_size, hipStream_t stream) {
  fused<<<256, 1024, 0, stream>>>((const u16*)d_in[0], (const u16*)d_in[1], (const u16*)d_in[2], (const u16*)d_in[3], (const u16*)d_in[4],
                                  (const u16*)d_in[6], (const u16*)d_in[7], (u16*)d_out, (unsigned char*)d_ws);
}
```

```cpp
#include <hip/hip_runtime.h>
#include <stdint.h>

typedef unsigned short u16;
typedef unsigned u32x4 __attribute__((ext_vector_type(4)));
typedef unsigned u32x2 __attribute__((ext_vector_type(2)));
typedef float f32x4 __attribute__((ext_vector_type(4)));
typedef float f32x2 __attribute__((ext_vector_type(2)));
typedef __bf16 bf16x8 __attribute__((ext_vector_type(8)));
typedef __bf16 bf16x2 __attribute__((ext_vector_type(2)));
typedef __amdgpu_buffer_rsrc_t rsrc_t;
#define GAS __attribute__((address_space(1)))

#define KV_AUX 2
#define W_AUX 2
#define STAMPS 0

constexpr int D = 512, SK = 8192, DH = 128, FF = 1376;
constexpr float LOG2E = 1.4426950408889634f;
constexpr int SC1 = 16;
constexpr unsigned WS_EPOCH = 0;
constexpr unsigned WS_Q = 4096;
constexpr unsigned WS_H = WS_Q + 256 * 1024;
constexpr unsigned WS_ML = WS_H + 256 * 512;
constexpr unsigned WS_O = WS_ML + 256 * 16;
constexpr unsigned WS_Y = WS_O + 256 * 1024;
constexpr unsigned WS_END = WS_Y + 256 * 1024;
constexpr unsigned WS_STAMP = 1u << 20;
constexpr unsigned SPIN_CAP = 1u << 18;

__device__ __forceinline__ float rbf(float f) { return (float)(__bf16)f; }
__device__ __forceinline__ float blo(unsigned u) { return __uint_as_float(u << 16); }
__device__ __forceinline__ float bhi(unsigned u) { return __uint_as_float(u & 0xffff0000u); }
__device__ __forceinline__ unsigned pkbf(float a, float b) { bf16x2 v = {(__bf16)a, (__bf16)b}; return __builtin_bit_cast(unsigned, v); }
__device__ __forceinline__ float dot2(unsigned a, unsigned b, float acc) {
  return __builtin_amdgcn_fdot2_f32_bf16(__builtin_bit_cast(bf16x2, a), __builtin_bit_cast(bf16x2, b), acc, false);
}
template <int CTRL> __device__ __forceinline__ float dppf(float x) {
  return __builtin_bit_cast(float, __builtin_amdgcn_mov_dpp(__builtin_bit_cast(int, x), CTRL, 0xf, 0xf, true));
}
template <int CTRL> __device__ __forceinline__ unsigned dppu(unsigned x) {
  return (unsigned)__builtin_amdgcn_mov_dpp((int)x, CTRL, 0xf, 0xf, true);
}
constexpr int XOR1 = 0xB1, XOR2 = 0x4E, XOR7 = 0x141, ROR8 = 0x128, ROR4 = 0x124, ROR2 = 0x122;
__device__ __forceinline__ float x16_sum(float x) {
  auto s = __builtin_amdgcn_permlane16_swap(__float_as_uint(x), __float_as_uint(x), false, false);
  return __uint_as_float(s[0]) + __uint_as_float(s[1]);
}
__device__ __forceinline__ float x32_sum(float x) {
  auto t = __builtin_amdgcn_permlane32_swap(__float_as_uint(x), __float_as_uint(x), false, false);
  return __uint_as_float(t[0]) + __uint_as_float(t[1]);
}
__device__ __forceinline__ float xrow_sum(float x) { return x32_sum(x16_sum(x)); }
__device__ __forceinline__ float xrow_max(float x) {
  auto s = __builtin_amdgcn_permlane16_swap(__float_as_uint(x), __float_as_uint(x), false, false);
  x = fmaxf(__uint_as_float(s[0]), __uint_as_float(s[1]));
  auto t = __builtin_amdgcn_permlane32_swap(__float_as_uint(x), __float_as_uint(x), false, false);
  return fmaxf(__uint_as_float(t[0]), __uint_as_float(t[1]));
}
__device__ __forceinline__ float row_sum(float x) { x += dppf<XOR1>(x); x += dppf<XOR2>(x); x += dppf<XOR7>(x); x += dppf<ROR8>(x); return x; }
__device__ __forceinline__ float row_max(float x) { x = fmaxf(x, dppf<XOR1>(x)); x = fmaxf(x, dppf<XOR2>(x)); x = fmaxf(x, dppf<XOR7>(x)); x = fmaxf(x, dppf<ROR8>(x)); return x; }
__device__ __forceinline__ float wave_sum(float x) { return xrow_sum(row_sum(x)); }
__device__ __forceinline__ float wave_max(float x) { return xrow_max(row_max(x)); }
__device__ __forceinline__ float oct_sum(float x) { x += dppf<XOR1>(x); x += dppf<XOR2>(x); x += dppf<XOR7>(x); return x; }
__device__ __forceinline__ float oct_max(float x) { x = fmaxf(x, dppf<XOR1>(x)); x = fmaxf(x, dppf<XOR2>(x)); x = fmaxf(x, dppf<XOR7>(x)); return x; }
__device__ __forceinline__ f32x4 mfma(u32x4 a, u32x4 b, f32x4 c) {
  return __builtin_amdgcn_mfma_f32_16x16x32_bf16(__builtin_bit_cast(bf16x8, a), __builtin_bit_cast(bf16x8, b), c, 0, 0, 0);
}
__device__ __forceinline__ u32x4 diag(unsigned pk, const u32x4& m) { return u32x4{pk & m[0], pk & m[1], pk & m[2], pk & m[3]}; }
__device__ __forceinline__ float silu(float z) { return z * __builtin_amdgcn_rcpf(1.f + __builtin_amdgcn_exp2f(-z * LOG2E)); }
__device__ __forceinline__ rsrc_t mk_rsrc(const void* p, unsigned bytes) { return __builtin_amdgcn_make_buffer_rsrc((void*)p, 0, bytes, 0x00020000); }
#define LD128(rs, vo, so, aux) __builtin_amdgcn_raw_buffer_load_b128(rs, vo, so, aux)
#define LD64(rs, vo, so, aux) __builtin_amdgcn_raw_buffer_load_b64(rs, vo, so, aux)
#define ST128(v, rs, vo, so) __builtin_amdgcn_raw_buffer_store_b128(v, rs, vo, so, SC1)
#define SB() __builtin_amdgcn_sched_barrier(0)
#define ILV 1
#define SVC 1
#define NPRE 8
#define STR2(x) #x
#define STR(x) STR2(x)
#define MEMFENCE() asm volatile("" ::: "memory")
#define STAMP(i) do {} while (0)
#define STAMPW(i, wv) do {} while (0)

struct Lds {
  float part[16][128];
  float ypart[32][64];
  float wmax[16];
  float wsum[16];
  unsigned qb[64];
  unsigned tag, qready, cnt;
};

__device__ __forceinline__ void ln_stats(const u32x4 xa, float& mb, float& rden) {
  float xv[8];
#pragma unroll
  for (int j = 0; j < 4; ++j) { xv[2 * j] = blo(xa[j]); xv[2 * j + 1] = bhi(xa[j]); }
  float sm = ((xv[0] + xv[1]) + (xv[2] + xv[3])) + ((xv[4] + xv[5]) + (xv[6] + xv[7]));
  const float mean = wave_sum(sm) * (1.f / D);
  float sq = 0.f;
#pragma unroll
  for (int j = 0; j < 8; ++j) { const float d = xv[j] - mean; sq = fmaf(d, d, sq); }
  const float var = rbf(wave_sum(sq) * (1.f / D));
  mb = rbf(mean);
  rden = __builtin_amdgcn_rcpf(rbf(__builtin_amdgcn_sqrtf(rbf(var + 1e-5f))));
}
__device__ __forceinline__ unsigned xn_pk(float xi, float mb, float rden) { const float v = rbf(xi - mb) * rden; return pkbf(v, v); }

__global__ void __launch_bounds__(1024) fused(const u16* __restrict__ x, const u16* __restrict__ kc, const u16* __restrict__ vc,
                                               const u16* __restrict__ wq, const u16* __restrict__ wo, const u16* __restrict__ w1,
                                               const u16* __restrict__ w2, u16* __restrict__ out, unsigned char* ws) {
  __shared__ __attribute__((aligned(16))) Lds L;
  const int b = blockIdx.x;
  const int h = (b & 7) * 4 + (b >> 6), s = (b >> 3) & 7;
  const int tid = threadIdx.x, lane = tid & 63, w = __builtin_amdgcn_readfirstlane(tid >> 6), r = lane >> 4, c = lane & 15;
  STAMPW(0, 0);
  const rsrc_t rs = mk_rsrc(ws, WS_END);
  const rsrc_t rk = mk_rsrc(kc, 64u << 20), rv = mk_rsrc(vc, 64u << 20);
  u32x4 mk, m8;
#pragma unroll
  for (int i = 0; i < 4; ++i) {
    m8[i] = (((c & 7) >> 1) == i) ? ((c & 1) ? 0xffff0000u : 0x0000ffffu) : 0u;
    mk[i] = c < 8 ? m8[i] : 0u;
  }
  const f32x4 z4 = {0.f, 0.f, 0.f, 0.f};
  const bool valid8 = (r >> 1) == (c >> 3);

  if (SVC && w == 0) {
    __builtin_amdgcn_s_setprio(3);
    const rsrc_t rx = mk_rsrc(x, 1024), rq = mk_rsrc(wq, 4u << 20);
    unsigned tag = __builtin_amdgcn_raw_buffer_load_b32(rs, WS_EPOCH, 0, SC1);
    const u32x4 xa = LD128(rx, lane * 16, 0, 0);
    const u32x4 xb0 = LD128(rx, r * 32, s * 128, 0), xb1 = LD128(rx, r * 32 + 16, s * 128, 0);
    u32x4 wv[16];
#pragma unroll
    for (int i = 0; i < 16; ++i) wv[i] = LD128(rq, r * (16 * 8192) + c * 16, (64 * s + i) * 8192 + h * 256, W_AUX);
    SB();
    __builtin_amdgcn_s_barrier();
    SB();
    asm volatile("" : "+v"(tag));
    tag += 1u;
    float mb, rden; ln_stats(xa, mb, rden);
    f32x4 qa = z4;
#pragma unroll
    for (int i = 0; i < 16; ++i) {
      const unsigned u = i < 8 ? xb0[i >> 1] : xb1[(i - 8) >> 1];
      qa = mfma(diag(xn_pk((i & 1) ? bhi(u) : blo(u), mb, rden), mk), wv[i], qa);
    }
    if (lane < 32) {
      const int off = WS_Q + (8 * c + 4 * r) * 8;
      ST128((u32x4{__float_as_uint(qa[0]), tag, __float_as_uint(qa[1]), tag}), rs, off, (h * 8 + s) * 1024);
      ST128((u32x4{__float_as_uint(qa[2]), tag, __float_as_uint(qa[3]), tag}), rs, off + 16, (h * 8 + s) * 1024);
    }
    STAMP(1);
    asm volatile("s_sleep 16" ::: "memory");
    u32x4 g[8];
    for (unsigned spins = 0;; ++spins) {
      bool ok = true;
#pragma unroll
      for (int i = 0; i < 8; ++i) g[i] = LD128(rs, WS_Q + h * 8192 + lane * 16 + i * 1024, 0, SC1);
#pragma unroll
      for (int i = 0; i < 8; ++i) ok &= (g[i][1] == tag) & (g[i][3] == tag);
      if (__all(ok) || spins > SPIN_CAP) break;
      asm volatile("s_sleep 4" ::: "memory");
    }
    float q0 = 0.f, q1 = 0.f;
#pragma unroll
    for (int i = 0; i < 8; ++i) { q0 += __uint_as_float(g[i][0]); q1 += __uint_as_float(g[i][2]); }
    L.qb[lane] = pkbf(q0, q1);
    if (lane == 0) L.tag = tag;
    __hip_atomic_store(&L.qready, 1u, __ATOMIC_RELEASE, __HIP_MEMORY_SCOPE_WORKGROUP);
    STAMP(2);
  } else if (SVC && w == 1) {
    __builtin_amdgcn_s_setprio(3);
    const int nch = h < 12 ? 6 : 5, ch0 = h < 12 ? 6 * h : 5 * h + 12;
    const rsrc_t rx = mk_rsrc(x, 1024), r1 = mk_rsrc(w1, 512 * FF * 2), r2 = mk_rsrc(w2, 512 * FF * 2);
    const int r8 = lane >> 3, c8 = lane & 7;
    unsigned tag = __builtin_amdgcn_raw_buffer_load_b32(rs, WS_EPOCH, 0, SC1);
    const u32x4 xa = LD128(rx, lane * 16, 0, 0);
    const u32x4 xb = LD128(rx, r8 * 16, s * 128, 0);
    u32x4 wv[8], w2v[6];
    const int cc = c8 < nch ? c8 : nch - 1;
#pragma unroll
    for (int i = 0; i < 8; ++i) wv[i] = LD128(r1, r8 * (8 * FF * 2) + cc * 16, (64 * s + i) * (FF * 2) + ch0 * 16, W_AUX);
#pragma unroll
    for (int i = 0; i < 6; ++i) w2v[i] = LD128(r2, r8 * 1024 + c8 * 16, (ch0 + (i < nch ? i : nch - 1)) * 8192 + s * 128, W_AUX);
    SB();
    __builtin_amdgcn_s_barrier();
    SB();
    asm volatile("" : "+v"(tag));
    tag += 1u;
    float mb, rden; ln_stats(xa, mb, rden);
    f32x4 ha = z4;
#pragma unroll
    for (int i = 0; i < 8; ++i) ha = mfma(diag(xn_pk((i & 1) ? bhi(xb[i >> 1]) : blo(xb[i >> 1]), mb, rden), m8), wv[i], ha);
#pragma unroll
    for (int j = 0; j < 4; ++j) { float v = valid8 ? ha[j] : 0.f; v += dppf<ROR8>(v); ha[j] = x32_sum(v); }
    if (lane < 32 && c < 8) {
      const int off = WS_H + (8 * c + 4 * r) * 8;
      ST128((u32x4{__float_as_uint(ha[0]), tag, __float_as_uint(ha[1]), tag}), rs, off, (h * 8 + s) * 512);
      ST128((u32x4{__float_as_uint(ha[2]), tag, __float_as_uint(ha[3]), tag}), rs, off + 16, (h * 8 + s) * 512);
    }
    u32x2 g[6];
    for (unsigned spins = 0;; ++spins) {
      bool ok = true;
#pragma unroll
      for (int i = 0; i < 6; ++i) g[i] = LD64(rs, WS_H + h * 4096 + c8 * 512 + r8 * 8 + i * 64, 0, SC1);
#pragma unroll
      for (int i = 0; i < 6; ++i) ok &= g[i][1] == tag;
      if (__all(ok) || spins > SPIN_CAP) break;
      asm volatile("s_sleep 4" ::: "memory");
    }
    f32x4 za = z4;
#pragma unroll
    for (int i = 0; i < 6; ++i) {
      float hv = silu(rbf(oct_sum(__uint_as_float(g[i][0]))));
      hv = i < nch ? hv : 0.f;
      za = mfma(diag(pkbf(hv, hv), m8), w2v[i], za);
    }
#pragma unroll
    for (int j = 0; j < 4; ++j) { float v = valid8 ? za[j] : 0.f; v += dppf<ROR8>(v); za[j] = x32_sum(v); }
    if (lane < 32 && c < 8) {
      const int off = WS_Y + (4 * c + 2 * r) * 1024 + 16;
      ST128((u32x4{__float_as_uint(za[0]), tag, __float_as_uint(za[1]), tag}), rs, off, s * 32768 + h * 32);
      ST128((u32x4{__float_as_uint(za[2]), tag, __float_as_uint(za[3]), tag}), rs, off + 1024, s * 32768 + h * 32);
    }
    STAMP(10);
  } else {
    if (tid == 128) { L.qready = SVC ? 0u : 1u; L.cnt = 0u; }
    asm volatile("s_waitcnt lgkmcnt(0)" ::: "memory");
    __builtin_amdgcn_s_barrier();
  }

  const int kvs = (h * SK + s * 1024 + w * 64) * 256, kvv = lane * 16;
  u32x4 kt[16], vt[16];
  SB();
#pragma unroll
  for (int t = 0; t < 16; ++t) { kt[t] = LD128(rk, kvv + (t & 3) * 1024, kvs + (t >> 2) * 4096, KV_AUX); SB(); }
#pragma unroll
  for (int t = 0; t < NPRE; ++t) { vt[t] = LD128(rv, kvv + (t & 3) * 1024, kvs + (t >> 2) * 4096, KV_AUX); SB(); }
  for (unsigned spins = 0; __hip_atomic_load(&L.qready, __ATOMIC_RELAXED, __HIP_MEMORY_SCOPE_WORKGROUP) == 0u && spins < (SPIN_CAP << 6); ++spins)
    __builtin_amdgcn_s_sleep(2);
  MEMFENCE();
  STAMPW(3, 2);
  const u32x4 qp = *(const u32x4*)&L.qb[4 * c];
  const unsigned tag = L.tag;
  float xs[16];
#pragma unroll
  for (int t = 0; t < 16; ++t) {
    float d = dot2(kt[t][0], qp[0], 0.f);
    d = dot2(kt[t][1], qp[1], d); d = dot2(kt[t][2], qp[2], d); d = dot2(kt[t][3], qp[3], d);
    xs[t] = d;
    if (ILV && t + NPRE < 16) { asm volatile("" : "+v"(xs[t])); SB(); vt[t + NPRE] = LD128(rv, kvv + ((t + NPRE) & 3) * 1024, kvs + ((t + NPRE) >> 2) * 4096, KV_AUX); SB(); }
  }
#define TR_STEP(HALF, CTRL, BIT) _Pragma("unroll") for (int i = 0; i < (HALF); ++i) { \
    const float t1 = xs[i] + dppf<CTRL>(xs[i]), t2 = xs[i + (HALF)] + dppf<CTRL>(xs[i + (HALF)]); xs[i] = (c & (BIT)) ? t2 : t1; }
  TR_STEP(8, ROR8, 8) TR_STEP(4, XOR7, 4) TR_STEP(2, XOR2, 2) TR_STEP(1, XOR1, 1)
#undef TR_STEP
  const float sc = rbf(rbf(xs[0]) * 0.08838834764831845f) * LOG2E;
  const float wm = wave_max(sc);
  const float p = __builtin_amdgcn_exp2f(sc - wm);
  {
    const float wsu = wave_sum(p);
    if (lane == 0) {
      L.wmax[w] = wm; L.wsum[w] = wsu;
      __hip_atomic_fetch_add(&L.cnt, 1u, __ATOMIC_RELEASE, __HIP_MEMORY_SCOPE_WORKGROUP);
    }
  }
  const unsigned pp = pkbf(p, p);
  f32x4 oa = z4;
#define PV(T) oa = mfma(diag(dppu<0x150 + (T)>(pp), mk), vt[T], oa);
  PV(0) PV(1) PV(2) PV(3) PV(4) PV(5) PV(6) PV(7) PV(8) PV(9) PV(10) PV(11)
  SB();
  const u32x4 wov = LD128(mk_rsrc(wo, 4u << 20), (lane >> 3) * 1024 + (lane & 7) * 16, (h * DH + 8 * w) * 1024 + s * 128, W_AUX);
  SB();
  PV(12) PV(13) PV(14) PV(15)
#undef PV
  if (lane < 32) *(f32x4*)&L.part[w][8 * c + 4 * r] = oa;
  float fw = 0.f;
  if (w == 0) {
    for (unsigned spins = 0; __hip_atomic_load(&L.cnt, __ATOMIC_RELAXED, __HIP_MEMORY_SCOPE_WORKGROUP) != 16u && spins < (SPIN_CAP << 6); ++spins)
      __builtin_amdgcn_s_sleep(2);
    MEMFENCE();
    const float wmc = L.wmax[c];
    const float M = row_max(wmc);
    fw = __builtin_amdgcn_exp2f(wmc - M);
    const float l = row_sum(L.wsum[c] * fw);
    if (lane == 0) ST128((u32x4{__float_as_uint(M), tag, __float_as_uint(l), tag}), rs, WS_ML, (h * 8 + s) * 16);
    STAMP(4);
  }
  STAMPW(5, 15);
  __syncthreads();
  STAMPW(6, 2);
  if (w == 0) {
    float a0 = 0.f, a1 = 0.f;
#define MG(I) { const f32x2 v = *(const f32x2*)&L.part[I][2 * lane]; const float f = dppf<0x150 + (I)>(fw); a0 = fmaf(f, v[0], a0); a1 = fmaf(f, v[1], a1); }
    MG(0) MG(1) MG(2) MG(3) MG(4) MG(5) MG(6) MG(7) MG(8) MG(9) MG(10) MG(11) MG(12) MG(13) MG(14) MG(15)
#undef MG
    ST128((u32x4{__float_as_uint(a0), tag, __float_as_uint(a1), tag}), rs, WS_O + lane * 16, (h * 8 + s) * 1024);
  }
  {
    const int off = WS_O + (lane & 7) * 1024 + (lane >> 3) * 8;
    u32x4 gm = LD128(rs, WS_ML + (lane & 7) * 16, h * 128, SC1);
    u32x2 go = LD64(rs, off, h * 8192 + w * 64, SC1);
    for (unsigned spins = 0; spins < SPIN_CAP; ++spins) {
      const bool ok = go[1] == tag && gm[1] == tag && gm[3] == tag;
      if (__all(ok)) break;
      asm volatile("s_sleep 2" ::: "memory");
      if (!ok) { gm = LD128(rs, WS_ML + (lane & 7) * 16, h * 128, SC1); go = LD64(rs, off, h * 8192 + w * 64, SC1); }
    }
    STAMPW(7, 2);
    const float ms = __uint_as_float(gm[0]);
    const float f = __builtin_amdgcn_exp2f(ms - oct_max(ms));
    const float o = oct_sum(f * __uint_as_float(go[0])) * __builtin_amdgcn_rcpf(oct_sum(f * __uint_as_float(gm[2])));
    const f32x4 y = mfma(diag(pkbf(o, o), m8), wov, z4);
    if (valid8) *(f32x4*)&L.ypart[2 * w + (r >> 1)][8 * (c & 7) + 4 * (r & 1)] = y;
  }
  __syncthreads();
  STAMPW(8, 4);
  if (w < 4) {
    const float* src = &L.ypart[4 * (lane >> 3)][16 * w + 2 * (lane & 7)];
    f32x2 a = *(const f32x2*)src + *(const f32x2*)(src + 64) + (*(const f32x2*)(src + 128) + *(const f32x2*)(src + 192));
    a[0] += dppf<ROR8>(a[0]); a[1] += dppf<ROR8>(a[1]);
    a[0] = xrow_sum(a[0]); a[1] = xrow_sum(a[1]);
    if (lane < 8) ST128((u32x4{__float_as_uint(a[0]), tag, __float_as_uint(a[1]), tag}), rs, WS_Y + lane * 1024, s * 32768 + w * 8192 + h * 32);
  }
  if (w == 0) {
    const unsigned xo = *(const GAS unsigned*)(x + 64 * s + 2 * h);
    u32x4 gy = LD128(rs, WS_Y + lane * 16, (s * 32 + h) * 1024, SC1);
    for (unsigned spins = 0; spins < SPIN_CAP; ++spins) {
      const bool ok = gy[1] == tag && gy[3] == tag;
      if (__all(ok)) break;
      asm volatile("s_sleep 2" ::: "memory");
      if (!ok) gy = LD128(rs, WS_Y + lane * 16, (s * 32 + h) * 1024, SC1);
    }
    STAMP(9);
    float v0 = __uint_as_float(gy[0]), v1 = __uint_as_float(gy[2]);
    v0 += dppf<ROR2>(v0); v1 += dppf<ROR2>(v1);
    v0 += dppf<ROR4>(v0); v1 += dppf<ROR4>(v1);
    v0 += dppf<ROR8>(v0); v1 += dppf<ROR8>(v1);
    v0 = rbf(xrow_sum(v0)); v1 = rbf(xrow_sum(v1));
    const float g0 = dppf<XOR1>(rbf(silu(v0))), g1 = dppf<XOR1>(rbf(silu(v1)));
    const float o0 = blo(xo) + rbf(v0 * g0), o1 = bhi(xo) + rbf(v1 * g1);
    if (lane == 0) {
      *(GAS unsigned*)(out + 64 * s + 2 * h) = pkbf(o0, o1);
      if (b == 0) __builtin_amdgcn_raw_buffer_store_b32(tag, rs, WS_EPOCH, 0, SC1);
    }
    STAMP(11);
  }
}

extern "C" void kernel_launch(void* const* d_in, const int* in_sizes, int n_in, void* d_out, int out_size, void* d_ws, size_t ws_size, hipStream_t stream) {
  fused<<<256, 1024, 0, stream>>>((const u16*)d_in[0], (const u16*)d_in[1], (const u16*)d_in[2], (const u16*)d_in[3], (const u16*)d_in[4],
                                  (const u16*)d_in[6], (const u16*)d_in[7], (u16*)d_out, (unsigned char*)d_ws);
}
```

```cpp
#include <hip/hip_runtime.h>
#include <stdint.h>

typedef unsigned short u16;
typedef unsigned u32x4 __attribute__((ext_vector_type(4)));
typedef unsigned u32x2 __attribute__((ext_vector_type(2)));
typedef float f32x4 __attribute__((ext_vector_type(4)));
typedef float f32x2 __attribute__((ext_vector_type(2)));
typedef __bf16 bf16x8 __attribute__((ext_vector_type(8)));
typedef __bf16 bf16x2 __attribute__((ext_vector_type(2)));
typedef __amdgpu_buffer_rsrc_t rsrc_t;
#define GAS __attribute__((address_space(1)))

#define KV_AUX 2
#define W_AUX 2
#define STAMPS 0
#define PROBE 99

constexpr int D = 512, SK = 8192, DH = 128, FF = 1376;
constexpr float LOG2E = 1.4426950408889634f;
constexpr int SC1 = 16;
constexpr unsigned WS_EPOCH = 0;
constexpr unsigned WS_Q = 4096;
constexpr unsigned WS_H = WS_Q + 256 * 1024;
constexpr unsigned WS_ML = WS_H + 256 * 512;
constexpr unsigned WS_O = WS_ML + 256 * 16;
constexpr unsigned WS_Y = WS_O + 256 * 1024;
constexpr unsigned WS_END = WS_Y + 256 * 1024;
constexpr unsigned WS_STAMP = 1u << 20;
constexpr unsigned SPIN_CAP = 1u << 18;

__device__ __forceinline__ float rbf(float f) { return (float)(__bf16)f; }
__device__ __forceinline__ float blo(unsigned u) { return __uint_as_float(u << 16); }
__device__ __forceinline__ float bhi(unsigned u) { return __uint_as_float(u & 0xffff0000u); }
__device__ __forceinline__ unsigned pkbf(float a, float b) { bf16x2 v = {(__bf16)a, (__bf16)b}; return __builtin_bit_cast(unsigned, v); }
__device__ __forceinline__ float dot2(unsigned a, unsigned b, float acc) {
  return __builtin_amdgcn_fdot2_f32_bf16(__builtin_bit_cast(bf16x2, a), __builtin_bit_cast(bf16x2, b), acc, false);
}
template <int CTRL> __device__ __forceinline__ float dppf(float x) {
  return __builtin_bit_cast(float, __builtin_amdgcn_mov_dpp(__builtin_bit_cast(int, x), CTRL, 0xf, 0xf, true));
}
template <int CTRL> __device__ __forceinline__ unsigned dppu(unsigned x) {
  return (unsigned)__builtin_amdgcn_mov_dpp((int)x, CTRL, 0xf, 0xf, true);
}
constexpr int XOR1 = 0xB1, XOR2 = 0x4E, XOR7 = 0x141, ROR8 = 0x128, ROR4 = 0x124, ROR2 = 0x122;
__device__ __forceinline__ float x16_sum(float x) {
  auto s = __builtin_amdgcn_permlane16_swap(__float_as_uint(x), __float_as_uint(x), false, false);
  return __uint_as_float(s[0]) + __uint_as_float(s[1]);
}
__device__ __forceinline__ float x32_sum(float x) {
  auto t = __builtin_amdgcn_permlane32_swap(__float_as_uint(x), __float_as_uint(x), false, false);
  return __uint_as_float(t[0]) + __uint_as_float(t[1]);
}
__device__ __forceinline__ float xrow_sum(float x) { return x32_sum(x16_sum(x)); }
__device__ __forceinline__ float xrow_max(float x) {
  auto s = __builtin_amdgcn_permlane16_swap(__float_as_uint(x), __float_as_uint(x), false, false);
  x = fmaxf(__uint_as_float(s[0]), __uint_as_float(s[1]));
  auto t = __builtin_amdgcn_permlane32_swap(__float_as_uint(x), __float_as_uint(x), false, false);
  return fmaxf(__uint_as_float(t[0]), __uint_as_float(t[1]));
}
__device__ __forceinline__ float row_sum(float x) { x += dppf<XOR1>(x); x += dppf<XOR2>(x); x += dppf<XOR7>(x); x += dppf<ROR8>(x); return x; }
__device__ __forceinline__ float row_max(float x) { x = fmaxf(x, dppf<XOR1>(x)); x = fmaxf(x, dppf<XOR2>(x)); x = fmaxf(x, dppf<XOR7>(x)); x = fmaxf(x, dppf<ROR8>(x)); return x; }
__device__ __forceinline__ float wave_sum(float x) { return xrow_sum(row_sum(x)); }
__device__ __forceinline__ float wave_max(float x) { return xrow_max(row_max(x)); }
__device__ __forceinline__ float oct_sum(float x) { x += dppf<XOR1>(x); x += dppf<XOR2>(x); x += dppf<XOR7>(x); return x; }
__device__ __forceinline__ float oct_max(float x) { x = fmaxf(x, dppf<XOR1>(x)); x = fmaxf(x, dppf<XOR2>(x)); x = fmaxf(x, dppf<XOR7>(x)); return x; }
__device__ __forceinline__ f32x4 mfma(u32x4 a, u32x4 b, f32x4 c) {
  return __builtin_amdgcn_mfma_f32_16x16x32_bf16(__builtin_bit_cast(bf16x8, a), __builtin_bit_cast(bf16x8, b), c, 0, 0, 0);
}
__device__ __forceinline__ u32x4 diag(unsigned pk, const u32x4& m) { return u32x4{pk & m[0], pk & m[1], pk & m[2], pk & m[3]}; }
__device__ __forceinline__ float silu(float z) { return z * __builtin_amdgcn_rcpf(1.f + __builtin_amdgcn_exp2f(-z * LOG2E)); }
__device__ __forceinline__ rsrc_t mk_rsrc(const void* p, unsigned bytes) { return __builtin_amdgcn_make_buffer_rsrc((void*)p, 0, bytes, 0x00020000); }
#define LD128(rs, vo, so, aux) __builtin_amdgcn_raw_buffer_load_b128(rs, vo, so, aux)
#define LD64(rs, vo, so, aux) __builtin_amdgcn_raw_buffer_load_b64(rs, vo, so, aux)
#define ST128(v, rs, vo, so) __builtin_amdgcn_raw_buffer_store_b128(v, rs, (vo) + (so), 0, SC1)
#define SB() __builtin_amdgcn_sched_barrier(0)
#define ILV 1
#define SVC 1
#define NPRE 8
#define STR2(x) #x
#define STR(x) STR2(x)
#define MEMFENCE() asm volatile("" ::: "memory")
#define STAMP(i) do { if ((i) == PROBE) tprobe = __builtin_amdgcn_s_memrealtime(); } while (0)
#define STAMPW(i, wv) do { if ((i) == PROBE) tprobe = __builtin_amdgcn_s_memrealtime(); } while (0)

struct Lds {
  float part[16][128];
  float ypart[32][64];
  float wmax[16];
  float wsum[16];
  unsigned qb[64];
  unsigned tag, qready, cnt;
};

__device__ __forceinline__ void ln_stats(const u32x4 xa, float& mb, float& rden) {
  float xv[8];
#pragma unroll
  for (int j = 0; j < 4; ++j) { xv[2 * j] = blo(xa[j]); xv[2 * j + 1] = bhi(xa[j]); }
  float sm = ((xv[0] + xv[1]) + (xv[2] + xv[3])) + ((xv[4] + xv[5]) + (xv[6] + xv[7]));
  const float mean = wave_sum(sm) * (1.f / D);
  float sq = 0.f;
#pragma unroll
  for (int j = 0; j < 8; ++j) { const float d = xv[j] - mean; sq = fmaf(d, d, sq); }
  const float var = rbf(wave_sum(sq) * (1.f / D));
  mb = rbf(mean);
  rden = __builtin_amdgcn_rcpf(rbf(__builtin_amdgcn_sqrtf(rbf(var + 1e-5f))));
}
__device__ __forceinline__ unsigned xn_pk(float xi, float mb, float rden) { const float v = rbf(xi - mb) * rden; return pkbf(v, v); }

__global__ void __launch_bounds__(1024) fused(const u16* __restrict__ x, const u16* __restrict__ kc, const u16* __restrict__ vc,
                                               const u16* __restrict__ wq, const u16* __restrict__ wo, const u16* __restrict__ w1,
                                               const u16* __restrict__ w2, u16* __restrict__ out, unsigned char* ws) {
  __shared__ __attribute__((aligned(16))) Lds L;
  const int b = blockIdx.x;
  const int h = (b & 7) * 4 + (b >> 6), s = (b >> 3) & 7;
  const int tid = threadIdx.x, lane = tid & 63, w = __builtin_amdgcn_readfirstlane(tid >> 6), r = lane >> 4, c = lane & 15;
  const unsigned long long tstart = __builtin_amdgcn_s_memrealtime(); unsigned long long tprobe = tstart;
  const rsrc_t rs = mk_rsrc(ws, WS_END);
  const rsrc_t rk = mk_rsrc(kc, 64u << 20), rv = mk_rsrc(vc, 64u << 20);
  u32x4 mk, m8;
#pragma unroll
  for (int i = 0; i < 4; ++i) {
    m8[i] = (((c & 7) >> 1) == i) ? ((c & 1) ? 0xffff0000u : 0x0000ffffu) : 0u;
    mk[i] = c < 8 ? m8[i] : 0u;
  }
  const f32x4 z4 = {0.f, 0.f, 0.f, 0.f};
  const bool valid8 = (r >> 1) == (c >> 3);

  if (SVC && w == 0) {
    __builtin_amdgcn_s_setprio(3);
    const rsrc_t rx = mk_rsrc(x, 1024), rq = mk_rsrc(wq, 4u << 20);
    unsigned tag = __builtin_amdgcn_raw_buffer_load_b32(rs, WS_EPOCH, 0, SC1);
    const u32x4 xa = LD128(rx, lane * 16, 0, 0);
    const u32x4 xb0 = LD128(rx, r * 32, s * 128, 0), xb1 = LD128(rx, r * 32 + 16, s * 128, 0);
    u32x4 wv[16];
#pragma unroll
    for (int i = 0; i < 16; ++i) wv[i] = LD128(rq, r * (16 * 8192) + c * 16, (64 * s + i) * 8192 + h * 256, W_AUX);
    SB();
    __builtin_amdgcn_s_barrier();
    SB();
    asm volatile("" : "+v"(tag));
    tag += 1u;
    float mb, rden; ln_stats(xa, mb, rden);
    f32x4 qa = z4;
#pragma unroll
    for (int i = 0; i < 16; ++i) {
      const unsigned u = i < 8 ? xb0[i >> 1] : xb1[(i - 8) >> 1];
      qa = mfma(diag(xn_pk((i & 1) ? bhi(u) : blo(u), mb, rden), mk), wv[i], qa);
    }
    if (lane < 32) {
      const int off = WS_Q + (8 * c + 4 * r) * 8;
      ST128((u32x4{__float_as_uint(qa[0]), tag, __float_as_uint(qa[1]), tag}), rs, off, (h * 8 + s) * 1024);
      ST128((u32x4{__float_as_uint(qa[2]), tag, __float_as_uint(qa[3]), tag}), rs, off + 16, (h * 8 + s) * 1024);
    }
    STAMP(1);
    asm volatile("s_sleep 16" ::: "memory");
    u32x4 g[8];
    for (unsigned spins = 0;; ++spins) {
      bool ok = true;
#pragma unroll
      for (int i = 0; i < 8; ++i) g[i] = LD128(rs, WS_Q + h * 8192 + lane * 16 + i * 1024, 0, SC1);
#pragma unroll
      for (int i = 0; i < 8; ++i) ok &= (g[i][1] == tag) & (g[i][3] == tag);
      if (__all(ok) || spins > SPIN_CAP) break;
      asm volatile("s_sleep 4" ::: "memory");
    }
    float q0 = 0.f, q1 = 0.f;
#pragma unroll
    for (int i = 0; i < 8; ++i) { q0 += __uint_as_float(g[i][0]); q1 += __uint_as_float(g[i][2]); }
    L.qb[lane] = pkbf(q0, q1);
    if (lane == 0) L.tag = tag;
    __hip_atomic_store(&L.qready, 1u, __ATOMIC_RELEASE, __HIP_MEMORY_SCOPE_WORKGROUP);
    STAMP(2);
  } else if (SVC && w == 1) {
    __builtin_amdgcn_s_setprio(3);
    const int nch = h < 12 ? 6 : 5, ch0 = h < 12 ? 6 * h : 5 * h + 12;
    const rsrc_t rx = mk_rsrc(x, 1024), r1 = mk_rsrc(w1, 512 * FF * 2), r2 = mk_rsrc(w2, 512 * FF * 2);
    const int r8 = lane >> 3, c8 = lane & 7;
    unsigned tag = __builtin_amdgcn_raw_buffer_load_b32(rs, WS_EPOCH, 0, SC1);
    const u32x4 xa = LD128(rx, lane * 16, 0, 0);
    const u32x4 xb = LD128(rx, r8 * 16, s * 128, 0);
    u32x4 wv[8], w2v[6];
    const int cc = c8 < nch ? c8 : nch - 1;
#pragma unroll
    for (int i = 0; i < 8; ++i) wv[i] = LD128(r1, r8 * (8 * FF * 2) + cc * 16, (64 * s + i) * (FF * 2) + ch0 * 16, W_AUX);
#pragma unroll
    for (int i = 0; i < 6; ++i) w2v[i] = LD128(r2, r8 * 1024 + c8 * 16, (ch0 + (i < nch ? i : nch - 1)) * 8192 + s * 128, W_AUX);
    SB();
    __builtin_amdgcn_s_barrier();
    SB();
    asm volatile("" : "+v"(tag));
    tag += 1u;
    float mb, rden; ln_stats(xa, mb, rden);
    f32x4 ha = z4;
#pragma unroll
    for (int i = 0; i < 8; ++i) ha = mfma(diag(xn_pk((i & 1) ? bhi(xb[i >> 1]) : blo(xb[i >> 1]), mb, rden), m8), wv[i], ha);
#pragma unroll
    for (int j = 0; j < 4; ++j) { float v = valid8 ? ha[j] : 0.f; v += dppf<ROR8>(v); ha[j] = x32_sum(v); }
    if (lane < 32 && c < 8) {
      const int off = WS_H + (8 * c + 4 * r) * 8;
      ST128((u32x4{__float_as_uint(ha[0]), tag, __float_as_uint(ha[1]), tag}), rs, off, (h * 8 + s) * 512);
      ST128((u32x4{__float_as_uint(ha[2]), tag, __float_as_uint(ha[3]), tag}), rs, off + 16, (h * 8 + s) * 512);
    }
    u32x2 g[6];
    for (unsigned spins = 0;; ++spins) {
      bool ok = true;
#pragma unroll
      for (int i = 0; i < 6; ++i) g[i] = LD64(rs, WS_H + h * 4096 + c8 * 512 + r8 * 8 + i * 64, 0, SC1);
#pragma unroll
      for (int i = 0; i < 6; ++i) ok &= g[i][1] == tag;
      if (__all(ok) || spins > SPIN_CAP) break;
      asm volatile("s_sleep 4" ::: "memory");
    }
    f32x4 za = z4;
#pragma unroll
    for (int i = 0; i < 6; ++i) {
      float hv = silu(rbf(oct_sum(__uint_as_float(g[i][0]))));
      hv = i < nch ? hv : 0.f;
      za = mfma(diag(pkbf(hv, hv), m8), w2v[i], za);
    }
#pragma unroll
    for (int j = 0; j < 4; ++j) { float v = valid8 ? za[j] : 0.f; v += dppf<ROR8>(v); za[j] = x32_sum(v); }
    if (lane < 32 && c < 8) {
      const int off = WS_Y + (4 * c + 2 * r) * 1024 + 16;
      ST128((u32x4{__float_as_uint(za[0]), tag, __float_as_uint(za[1]), tag}), rs, off, s * 32768 + h * 32);
      ST128((u32x4{__float_as_uint(za[2]), tag, __float_as_uint(za[3]), tag}), rs, off + 1024, s * 32768 + h * 32);
    }
    STAMP(10);
  } else {
    if (tid == 128) { L.qready = SVC ? 0u : 1u; L.cnt = 0u; }
    asm volatile("s_waitcnt lgkmcnt(0)" ::: "memory");
    __builtin_amdgcn_s_barrier();
  }

  const int kvs = (h * SK + s * 1024 + w * 64) * 256, kvv = lane * 16;
  u32x4 kt[16], vt[16];
  SB();
#pragma unroll
  for (int t = 0; t < 16; ++t) { kt[t] = LD128(rk, kvv + (t & 3) * 1024, kvs + (t >> 2) * 4096, KV_AUX); SB(); }
#pragma unroll
  for (int t = 0; t < NPRE; ++t) { vt[t] = LD128(rv, kvv + (t & 3) * 1024, kvs + (t >> 2) * 4096, KV_AUX); SB(); }
  for (unsigned spins = 0; __hip_atomic_load(&L.qready, __ATOMIC_RELAXED, __HIP_MEMORY_SCOPE_WORKGROUP) == 0u && spins < (SPIN_CAP << 6); ++spins)
    __builtin_amdgcn_s_sleep(2);
  MEMFENCE();
  STAMPW(3, 2);
  const u32x4 qp = *(const u32x4*)&L.qb[4 * c];
  const unsigned tag = L.tag;
  float xs[16];
#pragma unroll
  for (int t = 0; t < 16; ++t) {
    float d = dot2(kt[t][0], qp[0], 0.f);
    d = dot2(kt[t][1], qp[1], d); d = dot2(kt[t][2], qp[2], d); d = dot2(kt[t][3], qp[3], d);
    xs[t] = d;
    if (ILV && t + NPRE < 16) { asm volatile("" : "+v"(xs[t])); SB(); vt[t + NPRE] = LD128(rv, kvv + ((t + NPRE) & 3) * 1024, kvs + ((t + NPRE) >> 2) * 4096, KV_AUX); SB(); }
  }
#define TR_STEP(HALF, CTRL, BIT) _Pragma("unroll") for (int i = 0; i < (HALF); ++i) { \
    const float t1 = xs[i] + dppf<CTRL>(xs[i]), t2 = xs[i + (HALF)] + dppf<CTRL>(xs[i + (HALF)]); xs[i] = (c & (BIT)) ? t2 : t1; }
  TR_STEP(8, ROR8, 8) TR_STEP(4, XOR7, 4) TR_STEP(2, XOR2, 2) TR_STEP(1, XOR1, 1)
#undef TR_STEP
  const float sc = rbf(rbf(xs[0]) * 0.08838834764831845f) * LOG2E;
  const float wm = wave_max(sc);
  const float p = __builtin_amdgcn_exp2f(sc - wm);
  {
    const float wsu = wave_sum(p);
    if (lane == 0) {
      L.wmax[w] = wm; L.wsum[w] = wsu;
      __hip_atomic_fetch_add(&L.cnt, 1u, __ATOMIC_RELEASE, __HIP_MEMORY_SCOPE_WORKGROUP);
    }
  }
  const unsigned pp = pkbf(p, p);
  f32x4 oa = z4;
#define PV(T) oa = mfma(diag(dppu<0x150 + (T)>(pp), mk), vt[T], oa);
  PV(0) PV(1) PV(2) PV(3) PV(4) PV(5) PV(6) PV(7) PV(8) PV(9) PV(10) PV(11)
  SB();
  const u32x4 wov = LD128(mk_rsrc(wo, 4u << 20), (lane >> 3) * 1024 + (lane & 7) * 16, (h * DH + 8 * w) * 1024 + s * 128, W_AUX);
  SB();
  PV(12) PV(13) PV(14) PV(15)
#undef PV
  if (lane < 32) *(f32x4*)&L.part[w][8 * c + 4 * r] = oa;
  float fw = 0.f;
  if (w == 0) {
    for (unsigned spins = 0; __hip_atomic_load(&L.cnt, __ATOMIC_RELAXED, __HIP_MEMORY_SCOPE_WORKGROUP) != 16u && spins < (SPIN_CAP << 6); ++spins)
      __builtin_amdgcn_s_sleep(2);
    MEMFENCE();
    const float wmc = L.wmax[c];
    const float M = row_max(wmc);
    fw = __builtin_amdgcn_exp2f(wmc - M);
    const float l = row_sum(L.wsum[c] * fw);
    if (lane == 0) ST128((u32x4{__float_as_uint(M), tag, __float_as_uint(l), tag}), rs, WS_ML, (h * 8 + s) * 16);
    STAMP(4);
  }
  STAMPW(5, 15);
  __syncthreads();
  STAMPW(6, 2);
  if (w == 0) {
    float a0 = 0.f, a1 = 0.f;
#define MG(I) { const f32x2 v = *(const f32x2*)&L.part[I][2 * lane]; const float f = dppf<0x150 + (I)>(fw); a0 = fmaf(f, v[0], a0); a1 = fmaf(f, v[1], a1); }
    MG(0) MG(1) MG(2) MG(3) MG(4) MG(5) MG(6) MG(7) MG(8) MG(9) MG(10) MG(11) MG(12) MG(13) MG(14) MG(15)
#undef MG
    ST128((u32x4{__float_as_uint(a0), tag, __float_as_uint(a1), tag}), rs, WS_O + lane * 16, (h * 8 + s) * 1024);
  }
  {
    const int off = WS_O + (lane & 7) * 1024 + (lane >> 3) * 8;
    u32x4 gm = LD128(rs, WS_ML + (lane & 7) * 16, h * 128, SC1);
    u32x2 go = LD64(rs, off, h * 8192 + w * 64, SC1);
    for (unsigned spins = 0; spins < SPIN_CAP; ++spins) {
      const bool ok = go[1] == tag && gm[1] == tag && gm[3] == tag;
      if (__all(ok)) break;
      asm volatile("s_sleep 2" ::: "memory");
      if (!ok) { gm = LD128(rs, WS_ML + (lane & 7) * 16, h * 128, SC1); go = LD64(rs, off, h * 8192 + w * 64, SC1); }
    }
    STAMPW(7, 2);
    const float ms = __uint_as_float(gm[0]);
    const float f = __builtin_amdgcn_exp2f(ms - oct_max(ms));
    const float o = oct_sum(f * __uint_as_float(go[0])) * __builtin_amdgcn_rcpf(oct_sum(f * __uint_as_float(gm[2])));
    const f32x4 y = mfma(diag(pkbf(o, o), m8), wov, z4);
    if (valid8) *(f32x4*)&L.ypart[2 * w + (r >> 1)][8 * (c & 7) + 4 * (r & 1)] = y;
  }
  __syncthreads();
  STAMPW(8, 4);
  if (w < 4) {
    const float* src = &L.ypart[4 * (lane >> 3)][16 * w + 2 * (lane & 7)];
    f32x2 a = *(const f32x2*)src + *(const f32x2*)(src + 64) + (*(const f32x2*)(src + 128) + *(const f32x2*)(src + 192));
    a[0] += dppf<ROR8>(a[0]); a[1] += dppf<ROR8>(a[1]);
    a[0] = xrow_sum(a[0]); a[1] = xrow_sum(a[1]);
    if (lane < 8) ST128((u32x4{__float_as_uint(a[0]), tag, __float_as_uint(a[1]), tag}), rs, WS_Y + lane * 1024, s * 32768 + w * 8192 + h * 32);
  }
  if (w == 0) {
    const unsigned xo = *(const GAS unsigned*)(x + 64 * s + 2 * h);
    u32x4 gy = LD128(rs, WS_Y + lane * 16, (s * 32 + h) * 1024, SC1);
    for (unsigned spins = 0; spins < SPIN_CAP; ++spins) {
      const bool ok = gy[1] == tag && gy[3] == tag;
      if (__all(ok)) break;
      asm volatile("s_sleep 2" ::: "memory");
      if (!ok) gy = LD128(rs, WS_Y + lane * 16, (s * 32 + h) * 1024, SC1);
    }
    STAMP(9);
    float v0 = __uint_as_float(gy[0]), v1 = __uint_as_float(gy[2]);
    v0 += dppf<ROR2>(v0); v1 += dppf<ROR2>(v1);
    v0 += dppf<ROR4>(v0); v1 += dppf<ROR4>(v1);
    v0 += dppf<ROR8>(v0); v1 += dppf<ROR8>(v1);
    v0 = rbf(xrow_sum(v0)); v1 = rbf(xrow_sum(v1));
    const float g0 = dppf<XOR1>(rbf(silu(v0))), g1 = dppf<XOR1>(rbf(silu(v1)));
    const float o0 = blo(xo) + rbf(v0 * g0), o1 = bhi(xo) + rbf(v1 * g1);
    if (lane == 0) {
      *(GAS unsigned*)(out + 64 * s + 2 * h) = pkbf(o0, o1);
      if (b == 0) __builtin_amdgcn_raw_buffer_store_b32(tag, rs, WS_EPOCH, 0, SC1);
    }
    STAMP(11);
    if (b == 0) { const unsigned long long te = __builtin_amdgcn_s_memrealtime(), dl = 4 * (tprobe - tstart); while (__builtin_amdgcn_s_memrealtime() - te < dl) __builtin_amdgcn_s_sleep(1); }
  }
}

extern "C" void kernel_launch(void* const* d_in, const int* in_sizes, int n_in, void* d_out, int out_size, void* d_ws, size_t ws_size, hipStream_t stream) {
  fused<<<256, 1024, 0, stream>>>((const u16*)d_in[0], (const u16*)d_in[1], (const u16*)d_in[2], (const u16*)d_in[3], (const u16*)d_in[4],
                                  (const u16*)d_in[6], (const u16*)d_in[7], (u16*)d_out, (unsigned char*)d_ws);
}
```

```cpp
#include <hip/hip_runtime.h>
#include <stdint.h>

typedef unsigned short u16;
typedef unsigned u32x4 __attribute__((ext_vector_type(4)));
typedef unsigned u32x2 __attribute__((ext_vector_type(2)));
typedef float f32x4 __attribute__((ext_vector_type(4)));
typedef float f32x2 __attribute__((ext_vector_type(2)));
typedef __bf16 bf16x8 __attribute__((ext_vector_type(8)));
typedef __bf16 bf16x2 __attribute__((ext_vector_type(2)));
typedef __amdgpu_buffer_rsrc_t rsrc_t;
#define GAS __attribute__((address_space(1)))

#define KV_AUX 2
#define W_AUX 2
#define STAMPS 0
#define PROBE 0

constexpr int D = 512, SK = 8192, DH = 128, FF = 1376;
constexpr float LOG2E = 1.4426950408889634f;
constexpr int SC1 = 16;
constexpr unsigned WS_EPOCH = 0;
constexpr unsigned WS_Q = 4096;
constexpr unsigned WS_H = WS_Q + 256 * 1024;
constexpr unsigned WS_ML = WS_H + 256 * 512;
constexpr unsigned WS_O = WS_ML + 256 * 16;
constexpr unsigned WS_Y = WS_O + 256 * 1024;
constexpr unsigned WS_END = WS_Y + 256 * 1024;
constexpr unsigned WS_STAMP = 1u << 20;
constexpr unsigned SPIN_CAP = 1u << 18;

__device__ __forceinline__ float rbf(float f) { return (float)(__bf16)f; }
__device__ __forceinline__ float blo(unsigned u) { return __uint_as_float(u << 16); }
__device__ __forceinline__ float bhi(unsigned u) { return __uint_as_float(u & 0xffff0000u); }
__device__ __forceinline__ unsigned pkbf(float a, float b) { bf16x2 v = {(__bf16)a, (__bf16)b}; return __builtin_bit_cast(unsigned, v); }
__device__ __forceinline__ float dot2(unsigned a, unsigned b, float acc) {
  return __builtin_amdgcn_fdot2_f32_bf16(__builtin_bit_cast(bf16x2, a), __builtin_bit_cast(bf16x2, b), acc, false);
}
template <int CTRL> __device__ __forceinline__ float dppf(float x) {
  return __builtin_bit_cast(float, __builtin_amdgcn_mov_dpp(__builtin_bit_cast(int, x), CTRL, 0xf, 0xf, true));
}
template <int CTRL> __device__ __forceinline__ unsigned dppu(unsigned x) {
  return (unsigned)__builtin_amdgcn_mov_dpp((int)x, CTRL, 0xf, 0xf, true);
}
constexpr int XOR1 = 0xB1, XOR2 = 0x4E, XOR7 = 0x141, ROR8 = 0x128, ROR4 = 0x124, ROR2 = 0x122;
__device__ __forceinline__ float x16_sum(float x) {
  auto s = __builtin_amdgcn_permlane16_swap(__float_as_uint(x), __float_as_uint(x), false, false);
  return __uint_as_float(s[0]) + __uint_as_float(s[1]);
}
__device__ __forceinline__ float x32_sum(float x) {
  auto t = __builtin_amdgcn_permlane32_swap(__float_as_uint(x), __float_as_uint(x), false, false);
  return __uint_as_float(t[0]) + __uint_as_float(t[1]);
}
__device__ __forceinline__ float xrow_sum(float x) { return x32_sum(x16_sum(x)); }
__device__ __forceinline__ float xrow_max(float x) {
  auto s = __builtin_amdgcn_permlane16_swap(__float_as_uint(x), __float_as_uint(x), false, false);
  x = fmaxf(__uint_as_float(s[0]), __uint_as_float(s[1]));
  auto t = __builtin_amdgcn_permlane32_swap(__float_as_uint(x), __float_as_uint(x), false, false);
  return fmaxf(__uint_as_float(t[0]), __uint_as_float(t[1]));
}
__device__ __forceinline__ float row_sum(float x) { x += dppf<XOR1>(x); x += dppf<XOR2>(x); x += dppf<XOR7>(x); x += dppf<ROR8>(x); return x; }
__device__ __forceinline__ float row_max(float x) { x = fmaxf(x, dppf<XOR1>(x)); x = fmaxf(x, dppf<XOR2>(x)); x = fmaxf(x, dppf<XOR7>(x)); x = fmaxf(x, dppf<ROR8>(x)); return x; }
__device__ __forceinline__ float wave_sum(float x) { return xrow_sum(row_sum(x)); }
__device__ __forceinline__ float wave_max(float x) { return xrow_max(row_max(x)); }
__device__ __forceinline__ float oct_sum(float x) { x += dppf<XOR1>(x); x += dppf<XOR2>(x); x += dppf<XOR7>(x); return x; }
__device__ __forceinline__ float oct_max(float x) { x = fmaxf(x, dppf<XOR1>(x)); x = fmaxf(x, dppf<XOR2>(x)); x = fmaxf(x, dppf<XOR7>(x)); return x; }
__device__ __forceinline__ f32x4 mfma(u32x4 a, u32x4 b, f32x4 c) {
  return __builtin_amdgcn_mfma_f32_16x16x32_bf16(__builtin_bit_cast(bf16x8, a), __builtin_bit_cast(bf16x8, b), c, 0, 0, 0);
}
__device__ __forceinline__ u32x4 diag(unsigned pk, const u32x4& m) { return u32x4{pk & m[0], pk & m[1], pk & m[2], pk & m[3]}; }
__device__ __forceinline__ float silu(float z) { return z * __builtin_amdgcn_rcpf(1.f + __builtin_amdgcn_exp2f(-z * LOG2E)); }
__device__ __forceinline__ rsrc_t mk_rsrc(const void* p, unsigned bytes) { return __builtin_amdgcn_make_buffer_rsrc((void*)p, 0, bytes, 0x00020000); }
#define LD128(rs, vo, so, aux) __builtin_amdgcn_raw_buffer_load_b128(rs, vo, so, aux)
#define LD64(rs, vo, so, aux) __builtin_amdgcn_raw_buffer_load_b64(rs, vo, so, aux)
#define ST128(v, rs, vo, so) __builtin_amdgcn_raw_buffer_store_b128(v, rs, (vo) + (so), 0, SC1)
#define SB() __builtin_amdgcn_sched_barrier(0)
#define ILV 1
#define SVC 1
#define NPRE 8
#define STR2(x) #x
#define STR(x) STR2(x)
#define MEMFENCE() asm volatile("" ::: "memory")
#define STAMP(i) do {} while (0)
#define STAMPW(i, wv) do {} while (0)

struct Lds {
  float part[16][128];
  float ypart[32][64];
  float wmax[16];
  float wsum[16];
  unsigned qb[64];
  unsigned tag, qready, cnt;
};

__device__ __forceinline__ void ln_stats(const u32x4 xa, float& mb, float& rden) {
  float xv[8];
#pragma unroll
  for (int j = 0; j < 4; ++j) { xv[2 * j] = blo(xa[j]); xv[2 * j + 1] = bhi(xa[j]); }
  float sm = ((xv[0] + xv[1]) + (xv[2] + xv[3])) + ((xv[4] + xv[5]) + (xv[6] + xv[7]));
  const float mean = wave_sum(sm) * (1.f / D);
  float sq = 0.f;
#pragma unroll
  for (int j = 0; j < 8; ++j) { const float d = xv[j] - mean; sq = fmaf(d, d, sq); }
  const float var = rbf(wave_sum(sq) * (1.f / D));
  mb = rbf(mean);
  rden = __builtin_amdgcn_rcpf(rbf(__builtin_amdgcn_sqrtf(rbf(var + 1e-5f))));
}
__device__ __forceinline__ unsigned xn_pk(float xi, float mb, float rden) { const float v = rbf(xi - mb) * rden; return pkbf(v, v); }

__global__ void __launch_bounds__(1024) fused(const u16* __restrict__ x, const u16* __restrict__ kc, const u16* __restrict__ vc,
                                               const u16* __restrict__ wq, const u16* __restrict__ wo, const u16* __restrict__ w1,
                                               const u16* __restrict__ w2, u16* __restrict__ out, unsigned char* ws) {
  __shared__ __attribute__((aligned(16))) Lds L;
  const int b = blockIdx.x;
  const int h = (b & 7) * 4 + (b >> 6), s = (b >> 3) & 7;
  const int tid = threadIdx.x, lane = tid & 63, w = __builtin_amdgcn_readfirstlane(tid >> 6), r = lane >> 4, c = lane & 15;
  const unsigned long long tstart = __builtin_amdgcn_s_memrealtime();
  if (tid == 192) __hip_atomic_store((GAS unsigned long long*)(ws + WS_STAMP) + b, tstart, __ATOMIC_RELAXED, __HIP_MEMORY_SCOPE_AGENT);
  const rsrc_t rs = mk_rsrc(ws, WS_END);
  const rsrc_t rk = mk_rsrc(kc, 64u << 20), rv = mk_rsrc(vc, 64u << 20);
  u32x4 mk, m8;
#pragma unroll
  for (int i = 0; i < 4; ++i) {
    m8[i] = (((c & 7) >> 1) == i) ? ((c & 1) ? 0xffff0000u : 0x0000ffffu) : 0u;
    mk[i] = c < 8 ? m8[i] : 0u;
  }
  const f32x4 z4 = {0.f, 0.f, 0.f, 0.f};
  const bool valid8 = (r >> 1) == (c >> 3);

  if (SVC && w == 0) {
    __builtin_amdgcn_s_setprio(3);
    const rsrc_t rx = mk_rsrc(x, 1024), rq = mk_rsrc(wq, 4u << 20);
    unsigned tag = __builtin_amdgcn_raw_buffer_load_b32(rs, WS_EPOCH, 0, SC1);
    const u32x4 xa = LD128(rx, lane * 16, 0, 0);
    const u32x4 xb0 = LD128(rx, r * 32, s * 128, 0), xb1 = LD128(rx, r * 32 + 16, s * 128, 0);
    u32x4 wv[16];
#pragma unroll
    for (int i = 0; i < 16; ++i) wv[i] = LD128(rq, r * (16 * 8192) + c * 16, (64 * s + i) * 8192 + h * 256, W_AUX);
    SB();
    __builtin_amdgcn_s_barrier();
    SB();
    asm volatile("" : "+v"(tag));
    tag += 1u;
    float mb, rden; ln_stats(xa, mb, rden);
    f32x4 qa = z4;
#pragma unroll
    for (int i = 0; i < 16; ++i) {
      const unsigned u = i < 8 ? xb0[i >> 1] : xb1[(i - 8) >> 1];
      qa = mfma(diag(xn_pk((i & 1) ? bhi(u) : blo(u), mb, rden), mk), wv[i], qa);
    }
    if (lane < 32) {
      const int off = WS_Q + (8 * c + 4 * r) * 8;
      ST128((u32x4{__float_as_uint(qa[0]), tag, __float_as_uint(qa[1]), tag}), rs, off, (h * 8 + s) * 1024);
      ST128((u32x4{__float_as_uint(qa[2]), tag, __float_as_uint(qa[3]), tag}), rs, off + 16, (h * 8 + s) * 1024);
    }
    STAMP(1);
    asm volatile("s_sleep 16" ::: "memory");
    u32x4 g[8];
    for (unsigned spins = 0;; ++spins) {
      bool ok = true;
#pragma unroll
      for (int i = 0; i < 8; ++i) g[i] = LD128(rs, WS_Q + h * 8192 + lane * 16 + i * 1024, 0, SC1);
#pragma unroll
      for (int i = 0; i < 8; ++i) ok &= (g[i][1] == tag) & (g[i][3] == tag);
      if (__all(ok) || spins > SPIN_CAP) break;
      asm volatile("s_sleep 4" ::: "memory");
    }
    float q0 = 0.f, q1 = 0.f;
#pragma unroll
    for (int i = 0; i < 8; ++i) { q0 += __uint_as_float(g[i][0]); q1 += __uint_as_float(g[i][2]); }
    L.qb[lane] = pkbf(q0, q1);
    if (lane == 0) L.tag = tag;
    __hip_atomic_store(&L.qready, 1u, __ATOMIC_RELEASE, __HIP_MEMORY_SCOPE_WORKGROUP);
    STAMP(2);
  } else if (SVC && w == 1) {
    __builtin_amdgcn_s_setprio(3);
    const int nch = h < 12 ? 6 : 5, ch0 = h < 12 ? 6 * h : 5 * h + 12;
    const rsrc_t rx = mk_rsrc(x, 1024), r1 = mk_rsrc(w1, 512 * FF * 2), r2 = mk_rsrc(w2, 512 * FF * 2);
    const int r8 = lane >> 3, c8 = lane & 7;
    unsigned tag = __builtin_amdgcn_raw_buffer_load_b32(rs, WS_EPOCH, 0, SC1);
    const u32x4 xa = LD128(rx, lane * 16, 0, 0);
    const u32x4 xb = LD128(rx, r8 * 16, s * 128, 0);
    u32x4 wv[8], w2v[6];
    const int cc = c8 < nch ? c8 : nch - 1;
#pragma unroll
    for (int i = 0; i < 8; ++i) wv[i] = LD128(r1, r8 * (8 * FF * 2) + cc * 16, (64 * s + i) * (FF * 2) + ch0 * 16, W_AUX);
#pragma unroll
    for (int i = 0; i < 6; ++i) w2v[i] = LD128(r2, r8 * 1024 + c8 * 16, (ch0 + (i < nch ? i : nch - 1)) * 8192 + s * 128, W_AUX);
    SB();
    __builtin_amdgcn_s_barrier();
    SB();
    asm volatile("" : "+v"(tag));
    tag += 1u;
    float mb, rden; ln_stats(xa, mb, rden);
    f32x4 ha = z4;
#pragma unroll
    for (int i = 0; i < 8; ++i) ha = mfma(diag(xn_pk((i & 1) ? bhi(xb[i >> 1]) : blo(xb[i >> 1]), mb, rden), m8), wv[i], ha);
#pragma unroll
    for (int j = 0; j < 4; ++j) { float v = valid8 ? ha[j] : 0.f; v += dppf<ROR8>(v); ha[j] = x32_sum(v); }
    if (lane < 32 && c < 8) {
      const int off = WS_H + (8 * c + 4 * r) * 8;
      ST128((u32x4{__float_as_uint(ha[0]), tag, __float_as_uint(ha[1]), tag}), rs, off, (h * 8 + s) * 512);
      ST128((u32x4{__float_as_uint(ha[2]), tag, __float_as_uint(ha[3]), tag}), rs, off + 16, (h * 8 + s) * 512);
    }
    u32x2 g[6];
    for (unsigned spins = 0;; ++spins) {
      bool ok = true;
#pragma unroll
      for (int i = 0; i < 6; ++i) g[i] = LD64(rs, WS_H + h * 4096 + c8 * 512 + r8 * 8 + i * 64, 0, SC1);
#pragma unroll
      for (int i = 0; i < 6; ++i) ok &= g[i][1] == tag;
      if (__all(ok) || spins > SPIN_CAP) break;
      asm volatile("s_sleep 4" ::: "memory");
    }
    f32x4 za = z4;
#pragma unroll
    for (int i = 0; i < 6; ++i) {
      float hv = silu(rbf(oct_sum(__uint_as_float(g[i][0]))));
      hv = i < nch ? hv : 0.f;
      za = mfma(diag(pkbf(hv, hv), m8), w2v[i], za);
    }
#pragma unroll
    for (int j = 0; j < 4; ++j) { float v = valid8 ? za[j] : 0.f; v += dppf<ROR8>(v); za[j] = x32_sum(v); }
    if (lane < 32 && c < 8) {
      const int off = WS_Y + (4 * c + 2 * r) * 1024 + 16;
      ST128((u32x4{__float_as_uint(za[0]), tag, __float_as_uint(za[1]), tag}), rs, off, s * 32768 + h * 32);
      ST128((u32x4{__float_as_uint(za[2]), tag, __float_as_uint(za[3]), tag}), rs, off + 1024, s * 32768 + h * 32);
    }
    STAMP(10);
  } else {
    if (tid == 128) { L.qready = SVC ? 0u : 1u; L.cnt = 0u; }
    asm volatile("s_waitcnt lgkmcnt(0)" ::: "memory");
    __builtin_amdgcn_s_barrier();
  }

  const int kvs = (h * SK + s * 1024 + w * 64) * 256, kvv = lane * 16;
  u32x4 kt[16], vt[16];
  SB();
#pragma unroll
  for (int t = 0; t < 16; ++t) { kt[t] = LD128(rk, kvv + (t & 3) * 1024, kvs + (t >> 2) * 4096, KV_AUX); SB(); }
#pragma unroll
  for (int t = 0; t < NPRE; ++t) { vt[t] = LD128(rv, kvv + (t & 3) * 1024, kvs + (t >> 2) * 4096, KV_AUX); SB(); }
  for (unsigned spins = 0; __hip_atomic_load(&L.qready, __ATOMIC_RELAXED, __HIP_MEMORY_SCOPE_WORKGROUP) == 0u && spins < (SPIN_CAP << 6); ++spins)
    __builtin_amdgcn_s_sleep(2);
  MEMFENCE();
  STAMPW(3, 2);
  const u32x4 qp = *(const u32x4*)&L.qb[4 * c];
  const unsigned tag = L.tag;
  float xs[16];
#pragma unroll
  for (int t = 0; t < 16; ++t) {
    float d = dot2(kt[t][0], qp[0], 0.f);
    d = dot2(kt[t][1], qp[1], d); d = dot2(kt[t][2], qp[2], d); d = dot2(kt[t][3], qp[3], d);
    xs[t] = d;
    if (ILV && t + NPRE < 16) { asm volatile("" : "+v"(xs[t])); SB(); vt[t + NPRE] = LD128(rv, kvv + ((t + NPRE) & 3) * 1024, kvs + ((t + NPRE) >> 2) * 4096, KV_AUX); SB(); }
  }
#define TR_STEP(HALF, CTRL, BIT) _Pragma("unroll") for (int i = 0; i < (HALF); ++i) { \
    const float t1 = xs[i] + dppf<CTRL>(xs[i]), t2 = xs[i + (HALF)] + dppf<CTRL>(xs[i + (HALF)]); xs[i] = (c & (BIT)) ? t2 : t1; }
  TR_STEP(8, ROR8, 8) TR_STEP(4, XOR7, 4) TR_STEP(2, XOR2, 2) TR_STEP(1, XOR1, 1)
#undef TR_STEP
  const float sc = rbf(rbf(xs[0]) * 0.08838834764831845f) * LOG2E;
  const float wm = wave_max(sc);
  const float p = __builtin_amdgcn_exp2f(sc - wm);
  {
    const float wsu = wave_sum(p);
    if (lane == 0) {
      L.wmax[w] = wm; L.wsum[w] = wsu;
      __hip_atomic_fetch_add(&L.cnt, 1u, __ATOMIC_RELEASE, __HIP_MEMORY_SCOPE_WORKGROUP);
    }
  }
  const unsigned pp = pkbf(p, p);
  f32x4 oa = z4;
#define PV(T) oa = mfma(diag(dppu<0x150 + (T)>(pp), mk), vt[T], oa);
  PV(0) PV(1) PV(2) PV(3) PV(4) PV(5) PV(6) PV(7) PV(8) PV(9) PV(10) PV(11)
  SB();
  const u32x4 wov = LD128(mk_rsrc(wo, 4u << 20), (lane >> 3) * 1024 + (lane & 7) * 16, (h * DH + 8 * w) * 1024 + s * 128, W_AUX);
  SB();
  PV(12) PV(13) PV(14) PV(15)
#undef PV
  if (lane < 32) *(f32x4*)&L.part[w][8 * c + 4 * r] = oa;
  float fw = 0.f;
  if (w == 0) {
    for (unsigned spins = 0; __hip_atomic_load(&L.cnt, __ATOMIC_RELAXED, __HIP_MEMORY_SCOPE_WORKGROUP) != 16u && spins < (SPIN_CAP << 6); ++spins)
      __builtin_amdgcn_s_sleep(2);
    MEMFENCE();
    const float wmc = L.wmax[c];
    const float M = row_max(wmc);
    fw = __builtin_amdgcn_exp2f(wmc - M);
    const float l = row_sum(L.wsum[c] * fw);
    if (lane == 0) ST128((u32x4{__float_as_uint(M), tag, __float_as_uint(l), tag}), rs, WS_ML, (h * 8 + s) * 16);
    STAMP(4);
  }
  STAMPW(5, 15);
  __syncthreads();
  STAMPW(6, 2);
  if (w == 0) {
    float a0 = 0.f, a1 = 0.f;
#define MG(I) { const f32x2 v = *(const f32x2*)&L.part[I][2 * lane]; const float f = dppf<0x150 + (I)>(fw); a0 = fmaf(f, v[0], a0); a1 = fmaf(f, v[1], a1); }
    MG(0) MG(1) MG(2) MG(3) MG(4) MG(5) MG(6) MG(7) MG(8) MG(9) MG(10) MG(11) MG(12) MG(13) MG(14) MG(15)
#undef MG
    ST128((u32x4{__float_as_uint(a0), tag, __float_as_uint(a1), tag}), rs, WS_O + lane * 16, (h * 8 + s) * 1024);
  }
  {
    const int off = WS_O + (lane & 7) * 1024 + (lane >> 3) * 8;
    u32x4 gm = LD128(rs, WS_ML + (lane & 7) * 16, h * 128, SC1);
    u32x2 go = LD64(rs, off, h * 8192 + w * 64, SC1);
    for (unsigned spins = 0; spins < SPIN_CAP; ++spins) {
      const bool ok = go[1] == tag && gm[1] == tag && gm[3] == tag;
      if (__all(ok)) break;
      asm volatile("s_sleep 2" ::: "memory");
      if (!ok) { gm = LD128(rs, WS_ML + (lane & 7) * 16, h * 128, SC1); go = LD64(rs, off, h * 8192 + w * 64, SC1); }
    }
    STAMPW(7, 2);
    const float ms = __uint_as_float(gm[0]);
    const float f = __builtin_amdgcn_exp2f(ms - oct_max(ms));
    const float o = oct_sum(f * __uint_as_float(go[0])) * __builtin_amdgcn_rcpf(oct_sum(f * __uint_as_float(gm[2])));
    const f32x4 y = mfma(diag(pkbf(o, o), m8), wov, z4);
    if (valid8) *(f32x4*)&L.ypart[2 * w + (r >> 1)][8 * (c & 7) + 4 * (r & 1)] = y;
  }
  __syncthreads();
  STAMPW(8, 4);
  if (w < 4) {
    const float* src = &L.ypart[4 * (lane >> 3)][16 * w + 2 * (lane & 7)];
    f32x2 a = *(const f32x2*)src + *(const f32x2*)(src + 64) + (*(const f32x2*)(src + 128) + *(const f32x2*)(src + 192));
    a[0] += dppf<ROR8>(a[0]); a[1] += dppf<ROR8>(a[1]);
    a[0] = xrow_sum(a[0]); a[1] = xrow_sum(a[1]);
    if (lane < 8) ST128((u32x4{__float_as_uint(a[0]), tag, __float_as_uint(a[1]), tag}), rs, WS_Y + lane * 1024, s * 32768 + w * 8192 + h * 32);
  }
  if (w == 0) {
    const unsigned xo = *(const GAS unsigned*)(x + 64 * s + 2 * h);
    u32x4 gy = LD128(rs, WS_Y + lane * 16, (s * 32 + h) * 1024, SC1);
    for (unsigned spins = 0; spins < SPIN_CAP; ++spins) {
      const bool ok = gy[1] == tag && gy[3] == tag;
      if (__all(ok)) break;
      asm volatile("s_sleep 2" ::: "memory");
      if (!ok) gy = LD128(rs, WS_Y + lane * 16, (s * 32 + h) * 1024, SC1);
    }
    STAMP(9);
    float v0 = __uint_as_float(gy[0]), v1 = __uint_as_float(gy[2]);
    v0 += dppf<ROR2>(v0); v1 += dppf<ROR2>(v1);
    v0 += dppf<ROR4>(v0); v1 += dppf<ROR4>(v1);
    v0 += dppf<ROR8>(v0); v1 += dppf<ROR8>(v1);
    v0 = rbf(xrow_sum(v0)); v1 = rbf(xrow_sum(v1));
    const float g0 = dppf<XOR1>(rbf(silu(v0))), g1 = dppf<XOR1>(rbf(silu(v1)));
    const float o0 = blo(xo) + rbf(v0 * g0), o1 = bhi(xo) + rbf(v1 * g1);
    if (lane == 0) {
      *(GAS unsigned*)(out + 64 * s + 2 * h) = pkbf(o0, o1);
      if (b == 0) __builtin_amdgcn_raw_buffer_store_b32(tag, rs, WS_EPOCH, 0, SC1);
    }
    if (b == 0) {
      const unsigned long long te = __builtin_amdgcn_s_memrealtime();
      int mn = 0x7fffffff, mx = -0x7fffffff;
      for (int i = 0; i < 4; ++i) { const int d = (int)(te - __hip_atomic_load((GAS unsigned long long*)(ws + WS_STAMP) + lane + 64 * i, __ATOMIC_RELAXED, __HIP_MEMORY_SCOPE_AGENT)); mn = min(mn, d); mx = max(mx, d); }
      for (int o = 32; o > 0; o >>= 1) { mn = min(mn, __shfl_xor(mn, o)); mx = max(mx, __shfl_xor(mx, o)); }
      mn = __builtin_amdgcn_readfirstlane(mn); mx = __builtin_amdgcn_readfirstlane(mx);
      int dl = PROBE == 0 ? 0 : PROBE == 1 ? 4 * mx : PROBE == 2 ? 8 * (mx - mn) : 8 * (mx - (int)(te - tstart));
      dl = min(max(dl, 0), 100000);
      while ((int)(__builtin_amdgcn_s_memrealtime() - te) < dl) __builtin_amdgcn_s_sleep(1);
    }
  }
}

extern "C" void kernel_launch(void* const* d_in, const int* in_sizes, int n_in, void* d_out, int out_size, void* d_ws, size_t ws_size, hipStream_t stream) {
  fused<<<256, 1024, 0, stream>>>((const u16*)d_in[0], (const u16*)d_in[1], (const u16*)d_in[2], (const u16*)d_in[3], (const u16*)d_in[4],
                                  (const u16*)d_in[6], (const u16*)d_in[7], (u16*)d_out, (unsigned char*)d_ws);
}
```

```cpp
#include <hip/hip_runtime.h>
#include <stdint.h>
#include <stddef.h>

typedef unsigned short u16;
typedef unsigned u32x4 __attribute__((ext_vector_type(4)));
typedef unsigned u32x2 __attribute__((ext_vector_type(2)));
typedef float f32x4 __attribute__((ext_vector_type(4)));
typedef float f32x2 __attribute__((ext_vector_type(2)));
typedef __bf16 bf16x8 __attribute__((ext_vector_type(8)));
typedef __bf16 bf16x2 __attribute__((ext_vector_type(2)));
typedef __amdgpu_buffer_rsrc_t rsrc_t;
#define GAS __attribute__((address_space(1)))

#define KV_AUX 2
#define STAMPS 0
#define QSLEEP 32
#define HSLEEP 64
#define HOPS 0
#define OSLEEP 0
#define LATEFLAG 0
#define NOPASS1 0
#define NB 2
#define PF 1
#define PF_BYTES 10880

constexpr int D = 512, SK = 8192, DH = 128, FF = 1376;
constexpr float LOG2E = 1.4426950408889634f;
constexpr int SC1 = 16;
constexpr unsigned WS_EPOCH = 0;
constexpr unsigned WS_Q = 4096;
constexpr unsigned WS_H = WS_Q + 256 * 1024;
constexpr unsigned WS_F = WS_H + 256 * 512;
constexpr unsigned WS_O = WS_F + 32 * 128;
constexpr unsigned WS_Y = WS_O + 256 * 1024;
constexpr unsigned WS_Z = WS_Y + 256 * 512;
constexpr unsigned WS_END = WS_Z + 256 * 512;
constexpr unsigned WS_STAMP = 2u << 20;
constexpr unsigned SPIN_CAP = 1u << 18;

__device__ __forceinline__ float rbf(float f) { return (float)(__bf16)f; }
__device__ __forceinline__ float blo(unsigned u) { return __uint_as_float(u << 16); }
__device__ __forceinline__ float bhi(unsigned u) { return __uint_as_float(u & 0xffff0000u); }
__device__ __forceinline__ unsigned pkbf(float a, float b) { bf16x2 v = {(__bf16)a, (__bf16)b}; return __builtin_bit_cast(unsigned, v); }
__device__ __forceinline__ float dot2(unsigned a, unsigned b, float acc) {
  return __builtin_amdgcn_fdot2_f32_bf16(__builtin_bit_cast(bf16x2, a), __builtin_bit_cast(bf16x2, b), acc, false);
}
template <int CTRL> __device__ __forceinline__ float dppf(float x) {
  return __builtin_bit_cast(float, __builtin_amdgcn_mov_dpp(__builtin_bit_cast(int, x), CTRL, 0xf, 0xf, true));
}
template <int CTRL> __device__ __forceinline__ unsigned dppu(unsigned x) {
  return (unsigned)__builtin_amdgcn_mov_dpp((int)x, CTRL, 0xf, 0xf, true);
}
constexpr int XOR1 = 0xB1, XOR2 = 0x4E, XOR7 = 0x141, ROR8 = 0x128, ROR4 = 0x124, ROR2 = 0x122;
__device__ __forceinline__ float x16_sum(float x) {
  auto s = __builtin_amdgcn_permlane16_swap(__float_as_uint(x), __float_as_uint(x), false, false);
  return __uint_as_float(s[0]) + __uint_as_float(s[1]);
}
__device__ __forceinline__ float x32_sum2(float a, float b) {
  auto t = __builtin_amdgcn_permlane32_swap(__float_as_uint(a), __float_as_uint(b), false, false);
  return __uint_as_float(t[0]) + __uint_as_float(t[1]);
}
__device__ __forceinline__ float x32_sum(float x) { return x32_sum2(x, x); }
__device__ __forceinline__ float xrow_sum(float x) { return x32_sum(x16_sum(x)); }
__device__ __forceinline__ float fmx(float a, float b) { return __builtin_amdgcn_fmed3f(a, b, __builtin_inff()); }
__device__ __forceinline__ float xrow_max(float x) {
  auto s = __builtin_amdgcn_permlane16_swap(__float_as_uint(x), __float_as_uint(x), false, false);
  x = fmx(__uint_as_float(s[0]), __uint_as_float(s[1]));
  auto t = __builtin_amdgcn_permlane32_swap(__float_as_uint(x), __float_as_uint(x), false, false);
  return fmx(__uint_as_float(t[0]), __uint_as_float(t[1]));
}
__device__ __forceinline__ float oct_sum(float x) { x += dppf<XOR1>(x); x += dppf<XOR2>(x); x += dppf<XOR7>(x); return x; }
__device__ __forceinline__ float oct_max(float x) { x = fmx(x, dppf<XOR1>(x)); x = fmx(x, dppf<XOR2>(x)); x = fmx(x, dppf<XOR7>(x)); return x; }
__device__ __forceinline__ float row_sum(float x) { x = oct_sum(x); x += dppf<ROR8>(x); return x; }
__device__ __forceinline__ float row_max(float x) { x = oct_max(x); x = fmx(x, dppf<ROR8>(x)); return x; }
__device__ __forceinline__ float wave_sum(float x) { return xrow_sum(row_sum(x)); }
__device__ __forceinline__ float wave_max(float x) { return xrow_max(row_max(x)); }
__device__ __forceinline__ f32x4 mfma(u32x4 a, u32x4 b, f32x4 c) {
  return __builtin_amdgcn_mfma_f32_16x16x32_bf16(__builtin_bit_cast(bf16x8, a), __builtin_bit_cast(bf16x8, b), c, 0, 0, 0);
}
__device__ __forceinline__ u32x4 diag(unsigned pk, const u32x4& m) { return u32x4{pk & m[0], pk & m[1], pk & m[2], pk & m[3]}; }
__device__ __forceinline__ float silu(float z) { return z * __builtin_amdgcn_rcpf(1.f + __builtin_amdgcn_exp2f(-z * LOG2E)); }
__device__ __forceinline__ rsrc_t mk_rsrc(const void* p, unsigned bytes) { return __builtin_amdgcn_make_buffer_rsrc((void*)p, 0, bytes, 0x00020000); }
#define LD128(rs, vo, so, aux) __builtin_amdgcn_raw_buffer_load_b128(rs, vo, so, aux)
#define LD64(rs, vo, so, aux) __builtin_amdgcn_raw_buffer_load_b64(rs, vo, so, aux)
#define ST128(a, b, tag, rs, vo) __builtin_amdgcn_raw_buffer_store_b128((u32x4{__float_as_uint(a), tag, __float_as_uint(b), tag}), rs, vo, 0, SC1)
#define ST64(a, tag, rs, vo) __builtin_amdgcn_raw_buffer_store_b64((u32x2{__float_as_uint(a), tag}), rs, vo, 0, SC1)
#define LDS_LD(p) __hip_atomic_load(p, __ATOMIC_RELAXED, __HIP_MEMORY_SCOPE_WORKGROUP)
#define SB() __builtin_amdgcn_sched_barrier(0)
#define MEMFENCE() asm volatile("" ::: "memory")
#define STAMP(i) do {} while (0)
#define STAMPW(i, wv) do {} while (0)

constexpr int OOB = 0x40000000;
template <int N, int SLEEP> __device__ __forceinline__ void poll128(u32x4 (&g)[N], rsrc_t rs, int off, int stride, unsigned tag) {
#pragma unroll
  for (int i = 0; i < N; ++i) g[i] = LD128(rs, off + i * stride, 0, SC1);
  _Pragma("nounroll") for (unsigned spins = 0; spins < SPIN_CAP; ++spins) {
    bool bad[N], any = false;
#pragma unroll
    for (int i = 0; i < N; ++i) { bad[i] = (g[i][1] != tag) | (g[i][3] != tag); any |= bad[i]; }
    if (!__any(any)) break;
    if (SLEEP) __builtin_amdgcn_s_sleep(SLEEP);
    MEMFENCE();
    u32x4 n[N];
#pragma unroll
    for (int i = 0; i < N; ++i) n[i] = LD128(rs, bad[i] ? off + i * stride : OOB, 0, SC1);
    SB();
#pragma unroll
    for (int i = 0; i < N; ++i) g[i] = bad[i] ? n[i] : g[i];
  }
}

struct Lds {
  float part[16][128];
  float ypart[32][64];
  float wmax[16];
  float wsum[16];
  unsigned qb[64];
  float own[128];
  float wgt[8];
  float lse;
  unsigned tag, qready, cnt, go;
};

__device__ __forceinline__ void ln_stats(const u32x4 xa, float& mb, float& rden) {
  float xv[8];
#pragma unroll
  for (int j = 0; j < 4; ++j) { xv[2 * j] = blo(xa[j]); xv[2 * j + 1] = bhi(xa[j]); }
  float sm = ((xv[0] + xv[1]) + (xv[2] + xv[3])) + ((xv[4] + xv[5]) + (xv[6] + xv[7]));
  const float mean = wave_sum(sm) * (1.f / D);
  float sq = 0.f;
#pragma unroll
  for (int j = 0; j < 8; ++j) { const float d = xv[j] - mean; sq = fmaf(d, d, sq); }
  const float var = rbf(wave_sum(sq) * (1.f / D));
  mb = rbf(mean);
  rden = __builtin_amdgcn_rcpf(rbf(__builtin_amdgcn_sqrtf(rbf(var + 1e-5f))));
}
__device__ __forceinline__ unsigned xn_pk(float xi, float mb, float rden) { const float v = rbf(xi - mb) * rden; return pkbf(v, v); }

__global__ void __launch_bounds__(1024) fused(const u16* __restrict__ x, const u16* __restrict__ kc, const u16* __restrict__ vc,
                                               const u16* __restrict__ wq, const u16* __restrict__ wo, const u16* __restrict__ w1,
                                               const u16* __restrict__ w2, u16* __restrict__ out, unsigned char* ws) {
  __shared__ __attribute__((aligned(16))) Lds L;
  unsigned long long px = (unsigned long long)x, pk = (unsigned long long)kc, pv = (unsigned long long)vc, pq = (unsigned long long)wq,
                     po = (unsigned long long)wo, p1 = (unsigned long long)w1, p2 = (unsigned long long)w2, pout = (unsigned long long)out,
                     pws = (unsigned long long)ws;
  const int b = blockIdx.x;
  const int h = (b & 7) * 4 + (b >> 6), s = (b >> 3) & 7;
  const int tid = threadIdx.x, lane = tid & 63, w = __builtin_amdgcn_readfirstlane(tid >> 6);
  if (tid == 128) { L.qready = 0u; L.cnt = 0u; L.go = 0u; }
  const int kvs = (h * SK + s * 1024 + w * 64) * 256, kvv = lane * 16;
  const int nch = 5 + (h < 12), ch0 = 5 * h + min(h, 12);
  int sa0 = s * (64 * 8192) + h * 256, sa1 = s * (64 * FF * 2) + ch0 * 16, sb1 = ch0 * 8192 + s * 128, nb1 = (ch0 + nch) * 8192;
  asm volatile("" : "+s"(sa0), "+s"(sa1), "+s"(sb1), "+s"(nb1));
  const bool r0 = w == 0, r1 = w == 1;
  const int sh = r1 ? 3 : 4, msk = r1 ? 7 : 15, lim = r1 ? nch - 1 : 15;
  const int SA = r0 ? 16 * 8192 : r1 ? 8 * FF * 2 : 256, SBv = r0 ? 16 * 8192 : r1 ? 1024 : 256;
  const int va = (lane >> sh) * SA + min(lane & msk, lim) * 16, vb = (lane >> sh) * SBv + (lane & msk) * 16;
  const int sa = r0 ? sa0 : r1 ? sa1 : kvs, da = r0 ? 8192 : r1 ? FF * 2 : 1024;
  const int sb = r1 ? sb1 : sa + 8 * da, db = w < 2 ? 8192 : 1024;
  unsigned long long pc;
  asm volatile("s_getpc_b64 %0" : "=s"(pc));
  asm volatile("" : "+s"(px), "+s"(pk), "+s"(pv), "+s"(pq), "+s"(po), "+s"(p1), "+s"(p2), "+s"(pout), "+s"(pws));
  const rsrc_t rs = mk_rsrc((void*)pws, WS_END);
  const rsrc_t rk = mk_rsrc((void*)pk, 64u << 20), rv = mk_rsrc((void*)pv, 64u << 20);
  const rsrc_t ra = mk_rsrc((void*)(r0 ? pq : r1 ? p1 : pk), r0 ? 4u << 20 : r1 ? 512 * FF * 2 : 64u << 20);
  const rsrc_t rb = mk_rsrc((void*)(r0 ? pq : r1 ? p2 : pk), r0 ? 4u << 20 : r1 ? nb1 : 64u << 20);
  unsigned etag = __builtin_amdgcn_raw_buffer_load_b32(mk_rsrc((void*)(w < 2 ? pws : pc & ~0xffull), w < 2 ? 4 : PF ? PF_BYTES : 0),
                                                       w < 2 ? 0 : lane * 64, w < 2 ? 0 : (w - 2) * 4096, SC1);
  u32x4 xa, xb0, xb1;
  asm volatile("" : "=v"(xa), "=v"(xb0), "=v"(xb1));
  if (w < 2) {
    const rsrc_t rx = mk_rsrc((void*)px, 1024);
    xa = LD128(rx, kvv, 0, 0);
    xb0 = LD128(rx, (lane >> sh) << (sh + 1), s * 128, 0);
    xb1 = LD128(rx, ((lane >> sh) << (sh + 1)) + 16, s * 128, 0);
  }
  u32x4 kt[16], vt[16];
  SB();
#pragma unroll
  for (int i = 0; i < NB; ++i) { kt[i] = LD128(ra, va, sa + i * da, KV_AUX); SB(); }
  if (w >= 2) asm volatile("s_waitcnt lgkmcnt(0)\n s_barrier" ::: "memory");
#pragma unroll
  for (int i = NB; i < 8; ++i) { kt[i] = LD128(ra, va, sa + i * da, KV_AUX); SB(); }
#pragma unroll
  for (int i = 0; i < 8; ++i) { kt[8 + i] = LD128(rb, vb, sb + i * db, KV_AUX); SB(); }
  const f32x4 z4 = {0.f, 0.f, 0.f, 0.f};
#define LANE_VARS() int ln_ = lane; asm volatile("" : "+v"(ln_)); const int r = ln_ >> 4, c = ln_ & 15, r8 = ln_ >> 3, c8 = ln_ & 7; (void)r; (void)c; (void)r8; (void)c8;
#define MASK8() u32x4 m8; _Pragma("unroll") for (int i = 0; i < 4; ++i) m8[i] = ((c8 >> 1) == i) ? ((c8 & 1) ? 0xffff0000u : 0x0000ffffu) : 0u; const bool valid8 = (r >> 1) == (c >> 3); (void)valid8;
#define MASK16() u32x4 mk; _Pragma("unroll") for (int i = 0; i < 4; ++i) mk[i] = ((c >> 1) == i) ? ((c & 1) ? 0xffff0000u : 0x0000ffffu) : 0u;

  if (w < 2) {
    asm volatile("s_barrier" ::: "memory");
    LANE_VARS(); MASK8(); MASK16();
    asm volatile("" : "+v"(etag));
    const unsigned tag = etag + 1u;
    float mb, rden; ln_stats(xa, mb, rden);
    const int pairoff = (4 * c + 2 * (r & 1) + (r >> 1)) * 16;
    if (w == 0) {
      f32x4 qa = z4;
#pragma unroll
      for (int i = 0; i < 16; ++i) {
        const unsigned u = i < 8 ? xb0[i >> 1] : xb1[(i - 8) >> 1];
        qa = mfma(diag(xn_pk((i & 1) ? bhi(u) : blo(u), mb, rden), mk), kt[i], qa);
      }
      ST128(x32_sum2(qa[0], qa[2]), x32_sum2(qa[1], qa[3]), tag, rs, WS_Q + (h * 8 + s) * 1024 + pairoff);
      STAMP(1);
      __builtin_amdgcn_s_sleep(QSLEEP);
      u32x4 g[8];
      poll128<8, 8>(g, rs, WS_Q + h * 8192 + lane * 16, 1024, tag);
      float q0 = 0.f, q1 = 0.f;
#pragma unroll
      for (int i = 0; i < 8; ++i) { q0 += __uint_as_float(g[i][0]); q1 += __uint_as_float(g[i][2]); }
      L.qb[lane] = pkbf(q0, q1);
      if (lane == 0) L.tag = tag;
      __hip_atomic_store(&L.qready, 1u, __ATOMIC_RELEASE, __HIP_MEMORY_SCOPE_WORKGROUP);
      STAMP(2);
    } else {
      f32x4 ha = z4;
#pragma unroll
      for (int i = 0; i < 8; ++i) ha = mfma(diag(xn_pk((i & 1) ? bhi(xb0[i >> 1]) : blo(xb0[i >> 1]), mb, rden), m8), kt[i], ha);
#pragma unroll
      for (int j = 0; j < 4; ++j) { float v = valid8 ? ha[j] : 0.f; ha[j] = v + dppf<ROR8>(v); }
      {
        const float a0 = x32_sum2(ha[0], ha[2]), a1 = x32_sum2(ha[1], ha[3]);
        if (c < 6) ST128(a0, a1, tag, rs, WS_H + (h * 8 + s) * 512 + pairoff);
      }
      __builtin_amdgcn_s_sleep(HSLEEP);
      u32x2 g[6];
#pragma unroll
      for (int i = 0; i < 6; ++i) g[i] = LD64(rs, WS_H + h * 4096 + c8 * 512 + r8 * 8 + i * 64, 0, SC1);
      _Pragma("nounroll") for (unsigned spins = 0; spins < SPIN_CAP; ++spins) {
        bool ok = true;
#pragma unroll
        for (int i = 0; i < 6; ++i) ok &= g[i][1] == tag;
        if (__all(ok)) break;
        asm volatile("s_sleep 8" ::: "memory");
        if (!ok) {
#pragma unroll
          for (int i = 0; i < 6; ++i) g[i] = LD64(rs, WS_H + h * 4096 + c8 * 512 + r8 * 8 + i * 64, 0, SC1);
        }
      }
      f32x4 za = z4;
#pragma unroll
      for (int i = 0; i < 6; ++i) {
        float hv = silu(rbf(oct_sum(__uint_as_float(g[i][0]))));
        hv = i < nch ? hv : 0.f;
        za = mfma(diag(pkbf(hv, hv), m8), kt[8 + i], za);
      }
#pragma unroll
      for (int j = 0; j < 4; ++j) { float v = valid8 ? za[j] : 0.f; za[j] = v + dppf<ROR8>(v); }
      {
        const float a0 = x32_sum2(za[0], za[2]), a1 = x32_sum2(za[1], za[3]);
        if (c < 8) ST128(a0, a1, tag, rs, WS_Z + (s * 32 + h) * 512 + pairoff);
      }
      STAMP(10);
    }
    SB();
#pragma unroll
    for (int t = 0; t < 16; ++t) { kt[t] = LD128(rk, kvv + (t & 3) * 1024, kvs + (t >> 2) * 4096, KV_AUX); SB(); }
  }

  int kvs2 = kvs, kvv2 = kvv;
  asm volatile("" : "+s"(kvs2), "+v"(kvv2));
#pragma unroll
  for (int t = 0; t < 8; ++t) { vt[t] = LD128(rv, kvv2 + (t & 3) * 1024, kvs2 + (t >> 2) * 4096, KV_AUX); SB(); }
  _Pragma("nounroll") for (unsigned spins = 0; __hip_atomic_load(&L.qready, __ATOMIC_RELAXED, __HIP_MEMORY_SCOPE_WORKGROUP) == 0u && spins < (SPIN_CAP << 6); ++spins)
    __builtin_amdgcn_s_sleep(2);
  MEMFENCE();
  STAMPW(3, 2);
  const u32x4 qp = *(const u32x4*)&L.qb[4 * (lane & 15)];
  const unsigned tag = L.tag;
  float xs[16];
#pragma unroll
  for (int t = 0; t < 16; ++t) {
    float d = dot2(kt[t][0], qp[0], 0.f);
    d = dot2(kt[t][1], qp[1], d); d = dot2(kt[t][2], qp[2], d); d = dot2(kt[t][3], qp[3], d);
    xs[t] = d;
    if (t < 8) { asm volatile("" : "+v"(xs[t])); SB(); vt[t + 8] = LD128(rv, kvv2 + (t & 3) * 1024, kvs2 + ((t + 8) >> 2) * 4096, KV_AUX); SB(); }
  }
  asm volatile("" :: "v"(etag));
  LANE_VARS();
#define TR_STEP(HALF, CTRL, BIT) _Pragma("unroll") for (int i = 0; i < (HALF); ++i) { \
    const float t1 = xs[i] + dppf<CTRL>(xs[i]), t2 = xs[i + (HALF)] + dppf<CTRL>(xs[i + (HALF)]); xs[i] = (c & (BIT)) ? t2 : t1; }
  TR_STEP(8, ROR8, 8) TR_STEP(4, XOR7, 4) TR_STEP(2, XOR2, 2) TR_STEP(1, XOR1, 1)
#undef TR_STEP
  const float sc = rbf(rbf(xs[0]) * 0.08838834764831845f) * LOG2E;
  const float wm = wave_max(sc);
  const float p = __builtin_amdgcn_exp2f(sc - wm);
  {
    const float wsu = wave_sum(p);
    if (lane == 0) {
      L.wmax[w] = wm; L.wsum[w] = wsu;
      __hip_atomic_fetch_add(&L.cnt, 1u, __ATOMIC_RELEASE, __HIP_MEMORY_SCOPE_WORKGROUP);
    }
  }
  const unsigned pp = pkbf(p, p);
  MASK16();
  MASK8();
  const bool fin = w == 5 && (h & 7) == s;
  int a_part = w * 512 + (8 * c + 4 * r) * 4;
  int a_mrg = r * 2048 + (32 * w + 2 * c) * 4;
  int a_od = 32 * w + 2 * c + (r >> 1);
  int a_yw = (2 * w + (r >> 1)) * 256 + (8 * c8 + 4 * (r & 1)) * 4;
  int a_yr = r8 * 1024 + (16 * w + 2 * c8) * 4;
  int a_row = (8 * w + r8) * 4;
  int o_f = (w == 4 && lane < 8) ? WS_F + h * 128 + c8 * 8 : OOB, o_o = WS_O + h * 8192 + c8 * 1024 + (8 * w + r8) * 8;
  int o_pub = (r & 1) ? OOB : WS_O + (h * 8 + s) * 1024 + a_od * 8;
  int o_y = lane < 8 ? WS_Y + (s * 32 + h) * 512 + w * 128 + lane * 16 : OOB;
  int o_fin = s * 16384 + (h >> 3) * 128 + r8 * 512 + c8 * 16;
  int o_out = lane < 8 ? 128 * s + 32 * (h >> 3) + 4 * lane : OOB;
  asm volatile("" : "+v"(a_part), "+v"(a_mrg), "+v"(a_od), "+v"(a_yw), "+v"(a_yr), "+v"(a_row), "+v"(o_f), "+v"(o_o), "+v"(o_pub), "+v"(o_y), "+v"(o_fin), "+v"(o_out));
  asm volatile("" : "+v"(m8[0]), "+v"(m8[1]), "+v"(m8[2]), "+v"(m8[3]));
  const unsigned long long mine = __ballot(c8 == s), vmask = __ballot(valid8);
  char* const lds = (char*)&L;
  f32x4 oa = z4;
#define PV(T) oa = mfma(diag(dppu<0x150 + (T)>(pp), mk), vt[T], oa);
  PV(0) PV(1) PV(2) PV(3) PV(4) PV(5) PV(6) PV(7) PV(8) PV(9) PV(10) PV(11)
  SB();
  const u32x4 wov = LD128(mk_rsrc((void*)po, 4u << 20), r8 * 1024 + c8 * 16, (h * DH + 8 * w) * 1024 + s * 128, KV_AUX);
  SB();
  PV(12) PV(13) PV(14) PV(15)
#undef PV
  if (lane < 32) *(f32x4*)(lds + offsetof(Lds, part) + a_part) = oa;
  STAMPW(5, 15);
  float g0 = 0.f, g1 = 0.f;
  unsigned xo = 0;
  f32x4 fw = z4;
  if (w < 4) {
    _Pragma("nounroll") for (unsigned spins = 0; LDS_LD(&L.cnt) != 16u && spins < (SPIN_CAP << 6); ++spins) __builtin_amdgcn_s_sleep(2);
    MEMFENCE();
    const float wmc = L.wmax[c];
    const f32x4 wm4 = *(const f32x4*)&L.wmax[4 * r];
    const float M = row_max(wmc);
    const float l = row_sum(L.wsum[c] * __builtin_amdgcn_exp2f(wmc - M));
    if (tid == 0) {
      const float lse = M + __builtin_amdgcn_logf(l);
      if (!LATEFLAG) ST64(lse, tag, rs, WS_F + h * 128 + s * 8);
      L.lse = lse;
    }
    STAMPW(4, 0);
    const float rl = __builtin_amdgcn_rcpf(l);
#pragma unroll
    for (int i = 0; i < 4; ++i) fw[i] = __builtin_amdgcn_exp2f(wm4[i] - M) * rl;
  } else if (fin) {
    xo = __builtin_amdgcn_raw_buffer_load_b32(mk_rsrc((void*)px, 1024), o_out, 0, 0);
    u32x4 gz[4];
    poll128<4, 8>(gz, rs, WS_Z + o_fin, 4096, tag);
    float v0 = (__uint_as_float(gz[0][0]) + __uint_as_float(gz[1][0])) + (__uint_as_float(gz[2][0]) + __uint_as_float(gz[3][0]));
    float v1 = (__uint_as_float(gz[0][2]) + __uint_as_float(gz[1][2])) + (__uint_as_float(gz[2][2]) + __uint_as_float(gz[3][2]));
    v0 += dppf<ROR8>(v0); v1 += dppf<ROR8>(v1);
    g0 = rbf(silu(rbf(xrow_sum(v0)))); g1 = rbf(silu(rbf(xrow_sum(v1))));
  }
#define HOP(i, nx)
  asm volatile("s_waitcnt lgkmcnt(0)\n s_barrier" ::: "memory");
  HOP(0, "fz_hop1");
  STAMPW(6, 6);
  if (w < 4) {
    const float* src = (const float*)(lds + offsetof(Lds, part) + a_mrg);
    const f32x2 v0 = *(const f32x2*)src, v1 = *(const f32x2*)(src + 128), v2 = *(const f32x2*)(src + 256), v3 = *(const f32x2*)(src + 384);
    const float a0 = (fw[0] * v0[0] + fw[1] * v1[0]) + (fw[2] * v2[0] + fw[3] * v3[0]);
    const float a1 = (fw[0] * v0[1] + fw[1] * v1[1]) + (fw[2] * v2[1] + fw[3] * v3[1]);
    const float a = x16_sum(x32_sum2(a0, a1));
    ST64(a, tag, rs, o_pub);
    if (LATEFLAG && tid == 0) ST64(L.lse, tag, rs, WS_F + h * 128 + s * 8);
    L.own[a_od] = a;
    STAMPW(12, 0);
  }
  u32x2 gf = LD64(rs, o_f, 0, SC1);
  u32x2 go = NOPASS1 ? u32x2{0u, ~tag} : LD64(rs, o_o, 0, SC1);
  asm volatile("s_waitcnt lgkmcnt(0)\n s_barrier" ::: "memory");
  HOP(1, "fz_hop2");
  const bool me = (mine >> lane) & 1;
  const float ownv = *(const float*)(lds + offsetof(Lds, own) + a_row);
  if (w == 4) {
    gf = me ? u32x2{__float_as_uint(L.lse), tag} : gf;
    _Pragma("nounroll") for (unsigned spins = 0; spins < SPIN_CAP; ++spins) {
      if (!(__ballot(gf[1] != tag) & 0xff)) break;
      __builtin_amdgcn_s_sleep(2);
      MEMFENCE();
      if (gf[1] != tag) gf = LD64(rs, o_f, 0, SC1);
    }
    const float ls = __uint_as_float(gf[0]);
    const float f = __builtin_amdgcn_exp2f(ls - oct_max(ls));
    const float wv = f * __builtin_amdgcn_rcpf(oct_sum(f));
    if (lane < 8) L.wgt[lane] = wv;
    __hip_atomic_store(&L.go, 1u, __ATOMIC_RELEASE, __HIP_MEMORY_SCOPE_WORKGROUP);
  }
  _Pragma("nounroll") for (unsigned spins = 0; LDS_LD(&L.go) == 0u && spins < (SPIN_CAP << 6); ++spins) __builtin_amdgcn_s_sleep(1);
  MEMFENCE();
  HOP(2, "fz_hop3");
  const float wg = L.wgt[lane & 7];
  go = me ? u32x2{__float_as_uint(ownv), tag} : go;
  _Pragma("nounroll") for (unsigned spins = 0; spins < SPIN_CAP; ++spins) {
    if (!__any(go[1] != tag)) break;
    if (OSLEEP && spins) __builtin_amdgcn_s_sleep(OSLEEP);
    MEMFENCE();
    if (go[1] != tag) go = LD64(rs, o_o, 0, SC1);
  }
  HOP(3, "fz_hop4");
  STAMPW(7, 6);
  {
    const float o = oct_sum(wg * __uint_as_float(go[0]));
    const f32x4 y = mfma(diag(pkbf(o, o), m8), wov, z4);
    if ((vmask >> lane) & 1) *(f32x4*)(lds + offsetof(Lds, ypart) + a_yw) = y;
  }
  asm volatile("s_waitcnt lgkmcnt(0)\n s_barrier" ::: "memory");
  HOP(4, "fz_hop5");
  STAMPW(8, 6);
  if (w < 4) {
    const float* src = (const float*)(lds + offsetof(Lds, ypart) + a_yr);
    f32x2 a = (*(const f32x2*)src + *(const f32x2*)(src + 64)) + (*(const f32x2*)(src + 128) + *(const f32x2*)(src + 192));
    a[0] += dppf<ROR8>(a[0]); a[1] += dppf<ROR8>(a[1]);
    a[0] = xrow_sum(a[0]); a[1] = xrow_sum(a[1]);
    ST128(a[0], a[1], tag, rs, o_y);
  } else if (fin) {
    HOP(5, "fz_hop6");
    u32x4 gy[4];
    poll128<4, 0>(gy, rs, WS_Y + o_fin, 4096, tag);
    HOP(6, "fz_hop7");
    STAMP(9);
    float v0 = (__uint_as_float(gy[0][0]) + __uint_as_float(gy[1][0])) + (__uint_as_float(gy[2][0]) + __uint_as_float(gy[3][0]));
    float v1 = (__uint_as_float(gy[0][2]) + __uint_as_float(gy[1][2])) + (__uint_as_float(gy[2][2]) + __uint_as_float(gy[3][2]));
    v0 += dppf<ROR8>(v0); v1 += dppf<ROR8>(v1);
    v0 = rbf(xrow_sum(v0)); v1 = rbf(xrow_sum(v1));
    const float o0 = blo(xo) + rbf(v0 * g0), o1 = bhi(xo) + rbf(v1 * g1);
    __builtin_amdgcn_raw_buffer_store_b32(pkbf(o0, o1), mk_rsrc((void*)pout, 1024), o_out, 0, 0);
    HOP(7, "fz_ret");
    if (b == 0 && lane == 0) __builtin_amdgcn_raw_buffer_store_b32(tag, rs, WS_EPOCH, 0, SC1);
    STAMP(11);
  }
}

extern "C" void kernel_launch(void* const* d_in, const int* in_sizes, int n_in, void* d_out, int out_size, void* d_ws, size_t ws_size, hipStream_t stream) {
  fused<<<256, 1024, 0, stream>>>((const u16*)d_in[0], (const u16*)d_in[1], (const u16*)d_in[2], (const u16*)d_in[3], (const u16*)d_in[4],
                                  (const u16*)d_in[6], (const u16*)d_in[7], (u16*)d_out, (unsigned char*)d_ws);
}
```

```cpp
#include <hip/hip_runtime.h>
#include <stdint.h>
#include <stddef.h>

typedef unsigned short u16;
typedef unsigned u32x4 __attribute__((ext_vector_type(4)));
typedef unsigned u32x2 __attribute__((ext_vector_type(2)));
typedef float f32x4 __attribute__((ext_vector_type(4)));
typedef float f32x2 __attribute__((ext_vector_type(2)));
typedef __bf16 bf16x8 __attribute__((ext_vector_type(8)));
typedef __bf16 bf16x2 __attribute__((ext_vector_type(2)));
typedef __amdgpu_buffer_rsrc_t rsrc_t;
#define GAS __attribute__((address_space(1)))

#define KV_AUX 2
#define STAMPS 1
#define PROBE_K 99
#define PROBE_M 3
#define QSLEEP 32
#define HSLEEP 64
#define HOPS 1
#define OSLEEP 0
#define LATEFLAG 0
#define NOPASS1 0
#define NB 2
#define PF 1
#define PF_BYTES 10880

constexpr int D = 512, SK = 8192, DH = 128, FF = 1376;
constexpr float LOG2E = 1.4426950408889634f;
constexpr int SC1 = 16;
constexpr unsigned WS_EPOCH = 0;
constexpr unsigned WS_Q = 4096;
constexpr unsigned WS_H = WS_Q + 256 * 1024;
constexpr unsigned WS_F = WS_H + 256 * 512;
constexpr unsigned WS_O = WS_F + 32 * 128;
constexpr unsigned WS_Y = WS_O + 256 * 1024;
constexpr unsigned WS_Z = WS_Y + 256 * 512;
constexpr unsigned WS_END = WS_Z + 256 * 512;
constexpr unsigned WS_STAMP = 2u << 20;
constexpr unsigned SPIN_CAP = 1u << 18;

__device__ __forceinline__ float rbf(float f) { return (float)(__bf16)f; }
__device__ __forceinline__ float blo(unsigned u) { return __uint_as_float(u << 16); }
__device__ __forceinline__ float bhi(unsigned u) { return __uint_as_float(u & 0xffff0000u); }
__device__ __forceinline__ unsigned pkbf(float a, float b) { bf16x2 v = {(__bf16)a, (__bf16)b}; return __builtin_bit_cast(unsigned, v); }
__device__ __forceinline__ float dot2(unsigned a, unsigned b, float acc) {
  return __builtin_amdgcn_fdot2_f32_bf16(__builtin_bit_cast(bf16x2, a), __builtin_bit_cast(bf16x2, b), acc, false);
}
template <int CTRL> __device__ __forceinline__ float dppf(float x) {
  return __builtin_bit_cast(float, __builtin_amdgcn_mov_dpp(__builtin_bit_cast(int, x), CTRL, 0xf, 0xf, true));
}
template <int CTRL> __device__ __forceinline__ unsigned dppu(unsigned x) {
  return (unsigned)__builtin_amdgcn_mov_dpp((int)x, CTRL, 0xf, 0xf, true);
}
constexpr int XOR1 = 0xB1, XOR2 = 0x4E, XOR7 = 0x141, ROR8 = 0x128, ROR4 = 0x124, ROR2 = 0x122;
__device__ __forceinline__ float x16_sum(float x) {
  auto s = __builtin_amdgcn_permlane16_swap(__float_as_uint(x), __float_as_uint(x), false, false);
  return __uint_as_float(s[0]) + __uint_as_float(s[1]);
}
__device__ __forceinline__ float x32_sum2(float a, float b) {
  auto t = __builtin_amdgcn_permlane32_swap(__float_as_uint(a), __float_as_uint(b), false, false);
  return __uint_as_float(t[0]) + __uint_as_float(t[1]);
}
__device__ __forceinline__ float x32_sum(float x) { return x32_sum2(x, x); }
__device__ __forceinline__ float xrow_sum(float x) { return x32_sum(x16_sum(x)); }
__device__ __forceinline__ float fmx(float a, float b) { return __builtin_amdgcn_fmed3f(a, b, __builtin_inff()); }
__device__ __forceinline__ float xrow_max(float x) {
  auto s = __builtin_amdgcn_permlane16_swap(__float_as_uint(x), __float_as_uint(x), false, false);
  x = fmx(__uint_as_float(s[0]), __uint_as_float(s[1]));
  auto t = __builtin_amdgcn_permlane32_swap(__float_as_uint(x), __float_as_uint(x), false, false);
  return fmx(__uint_as_float(t[0]), __uint_as_float(t[1]));
}
__device__ __forceinline__ float oct_sum(float x) { x += dppf<XOR1>(x); x += dppf<XOR2>(x); x += dppf<XOR7>(x); return x; }
__device__ __forceinline__ float oct_max(float x) { x = fmx(x, dppf<XOR1>(x)); x = fmx(x, dppf<XOR2>(x)); x = fmx(x, dppf<XOR7>(x)); return x; }
__device__ __forceinline__ float row_sum(float x) { x = oct_sum(x); x += dppf<ROR8>(x); return x; }
__device__ __forceinline__ float row_max(float x) { x = oct_max(x); x = fmx(x, dppf<ROR8>(x)); return x; }
__device__ __forceinline__ float wave_sum(float x) { return xrow_sum(row_sum(x)); }
__device__ __forceinline__ float wave_max(float x) { return xrow_max(row_max(x)); }
__device__ __forceinline__ f32x4 mfma(u32x4 a, u32x4 b, f32x4 c) {
  return __builtin_amdgcn_mfma_f32_16x16x32_bf16(__builtin_bit_cast(bf16x8, a), __builtin_bit_cast(bf16x8, b), c, 0, 0, 0);
}
__device__ __forceinline__ u32x4 diag(unsigned pk, const u32x4& m) { return u32x4{pk & m[0], pk & m[1], pk & m[2], pk & m[3]}; }
__device__ __forceinline__ float silu(float z) { return z * __builtin_amdgcn_rcpf(1.f + __builtin_amdgcn_exp2f(-z * LOG2E)); }
__device__ __forceinline__ rsrc_t mk_rsrc(const void* p, unsigned bytes) { return __builtin_amdgcn_make_buffer_rsrc((void*)p, 0, bytes, 0x00020000); }
#define LD128(rs, vo, so, aux) __builtin_amdgcn_raw_buffer_load_b128(rs, vo, so, aux)
#define LD64(rs, vo, so, aux) __builtin_amdgcn_raw_buffer_load_b64(rs, vo, so, aux)
#define ST128(a, b, tag, rs, vo) __builtin_amdgcn_raw_buffer_store_b128((u32x4{__float_as_uint(a), tag, __float_as_uint(b), tag}), rs, vo, 0, SC1)
#define ST64(a, tag, rs, vo) __builtin_amdgcn_raw_buffer_store_b64((u32x2{__float_as_uint(a), tag}), rs, vo, 0, SC1)
#define LDS_LD(p) __hip_atomic_load(p, __ATOMIC_RELAXED, __HIP_MEMORY_SCOPE_WORKGROUP)
#define SB() __builtin_amdgcn_sched_barrier(0)
#define MEMFENCE() asm volatile("" ::: "memory")
#define STAMP(i) do { if ((i) == PROBE_K && lane == 0) { unsigned long long t_ = __builtin_amdgcn_s_memrealtime(); __hip_atomic_store((GAS unsigned long long*)(ws + WS_STAMP) + blockIdx.x * 16 + (i), t_, __ATOMIC_RELAXED, __HIP_MEMORY_SCOPE_AGENT); } } while (0)
#define STAMPW(i, wv) do { if (w == (wv)) STAMP(i); } while (0)

constexpr int OOB = 0x40000000;
template <int N, int SLEEP> __device__ __forceinline__ void poll128(u32x4 (&g)[N], rsrc_t rs, int off, int stride, unsigned tag) {
#pragma unroll
  for (int i = 0; i < N; ++i) g[i] = LD128(rs, off + i * stride, 0, SC1);
  _Pragma("nounroll") for (unsigned spins = 0; spins < SPIN_CAP; ++spins) {
    bool bad[N], any = false;
#pragma unroll
    for (int i = 0; i < N; ++i) { bad[i] = (g[i][1] != tag) | (g[i][3] != tag); any |= bad[i]; }
    if (!__any(any)) break;
    if (SLEEP) __builtin_amdgcn_s_sleep(SLEEP);
    MEMFENCE();
    u32x4 n[N];
#pragma unroll
    for (int i = 0; i < N; ++i) n[i] = LD128(rs, bad[i] ? off + i * stride : OOB, 0, SC1);
    SB();
#pragma unroll
    for (int i = 0; i < N; ++i) g[i] = bad[i] ? n[i] : g[i];
  }
}

struct Lds {
  float part[16][128];
  float ypart[32][64];
  float wmax[16];
  float wsum[16];
  unsigned qb[64];
  float own[128];
  float wgt[8];
  float lse;
  unsigned tag, qready, cnt, go;
};

__device__ __forceinline__ void ln_stats(const u32x4 xa, float& mb, float& rden) {
  float xv[8];
#pragma unroll
  for (int j = 0; j < 4; ++j) { xv[2 * j] = blo(xa[j]); xv[2 * j + 1] = bhi(xa[j]); }
  float sm = ((xv[0] + xv[1]) + (xv[2] + xv[3])) + ((xv[4] + xv[5]) + (xv[6] + xv[7]));
  const float mean = wave_sum(sm) * (1.f / D);
  float sq = 0.f;
#pragma unroll
  for (int j = 0; j < 8; ++j) { const float d = xv[j] - mean; sq = fmaf(d, d, sq); }
  const float var = rbf(wave_sum(sq) * (1.f / D));
  mb = rbf(mean);
  rden = __builtin_amdgcn_rcpf(rbf(__builtin_amdgcn_sqrtf(rbf(var + 1e-5f))));
}
__device__ __forceinline__ unsigned xn_pk(float xi, float mb, float rden) { const float v = rbf(xi - mb) * rden; return pkbf(v, v); }

__global__ void __launch_bounds__(1024) fused(const u16* __restrict__ x, const u16* __restrict__ kc, const u16* __restrict__ vc,
                                               const u16* __restrict__ wq, const u16* __restrict__ wo, const u16* __restrict__ w1,
                                               const u16* __restrict__ w2, u16* __restrict__ out, unsigned char* ws) {
  __shared__ __attribute__((aligned(16))) Lds L;
  const unsigned long long tstart = __builtin_amdgcn_s_memrealtime();
  unsigned long long px = (unsigned long long)x, pk = (unsigned long long)kc, pv = (unsigned long long)vc, pq = (unsigned long long)wq,
                     po = (unsigned long long)wo, p1 = (unsigned long long)w1, p2 = (unsigned long long)w2, pout = (unsigned long long)out,
                     pws = (unsigned long long)ws;
  const int b = blockIdx.x;
  const int h = (b & 7) * 4 + (b >> 6), s = (b >> 3) & 7;
  const int tid = threadIdx.x, lane = tid & 63, w = __builtin_amdgcn_readfirstlane(tid >> 6);
  if (tid == 128) { L.qready = 0u; L.cnt = 0u; L.go = 0u; }
  const int kvs = (h * SK + s * 1024 + w * 64) * 256, kvv = lane * 16;
  const int nch = 5 + (h < 12), ch0 = 5 * h + min(h, 12);
  int sa0 = s * (64 * 8192) + h * 256, sa1 = s * (64 * FF * 2) + ch0 * 16, sb1 = ch0 * 8192 + s * 128, nb1 = (ch0 + nch) * 8192;
  asm volatile("" : "+s"(sa0), "+s"(sa1), "+s"(sb1), "+s"(nb1));
  const bool r0 = w == 0, r1 = w == 1;
  const int sh = r1 ? 3 : 4, msk = r1 ? 7 : 15, lim = r1 ? nch - 1 : 15;
  const int SA = r0 ? 16 * 8192 : r1 ? 8 * FF * 2 : 256, SBv = r0 ? 16 * 8192 : r1 ? 1024 : 256;
  const int va = (lane >> sh) * SA + min(lane & msk, lim) * 16, vb = (lane >> sh) * SBv + (lane & msk) * 16;
  const int sa = r0 ? sa0 : r1 ? sa1 : kvs, da = r0 ? 8192 : r1 ? FF * 2 : 1024;
  const int sb = r1 ? sb1 : sa + 8 * da, db = w < 2 ? 8192 : 1024;
  unsigned long long pc;
  asm volatile("s_getpc_b64 %0" : "=s"(pc));
  asm volatile("" : "+s"(px), "+s"(pk), "+s"(pv), "+s"(pq), "+s"(po), "+s"(p1), "+s"(p2), "+s"(pout), "+s"(pws));
  const rsrc_t rs = mk_rsrc((void*)pws, WS_END);
  const rsrc_t rk = mk_rsrc((void*)pk, 64u << 20), rv = mk_rsrc((void*)pv, 64u << 20);
  const rsrc_t ra = mk_rsrc((void*)(r0 ? pq : r1 ? p1 : pk), r0 ? 4u << 20 : r1 ? 512 * FF * 2 : 64u << 20);
  const rsrc_t rb = mk_rsrc((void*)(r0 ? pq : r1 ? p2 : pk), r0 ? 4u << 20 : r1 ? nb1 : 64u << 20);
  unsigned etag = __builtin_amdgcn_raw_buffer_load_b32(mk_rsrc((void*)(w < 2 ? pws : pc & ~0xffull), w < 2 ? 4 : PF ? PF_BYTES : 0),
                                                       w < 2 ? 0 : lane * 64, w < 2 ? 0 : (w - 2) * 4096, SC1);
  u32x4 xa, xb0, xb1;
  asm volatile("" : "=v"(xa), "=v"(xb0), "=v"(xb1));
  if (w < 2) {
    const rsrc_t rx = mk_rsrc((void*)px, 1024);
    xa = LD128(rx, kvv, 0, 0);
    xb0 = LD128(rx, (lane >> sh) << (sh + 1), s * 128, 0);
    xb1 = LD128(rx, ((lane >> sh) << (sh + 1)) + 16, s * 128, 0);
  }
  u32x4 kt[16], vt[16];
  SB();
#pragma unroll
  for (int i = 0; i < NB; ++i) { kt[i] = LD128(ra, va, sa + i * da, KV_AUX); SB(); }
  if (w >= 2) asm volatile("s_waitcnt lgkmcnt(0)\n s_barrier" ::: "memory");
#pragma unroll
  for (int i = NB; i < 8; ++i) { kt[i] = LD128(ra, va, sa + i * da, KV_AUX); SB(); }
#pragma unroll
  for (int i = 0; i < 8; ++i) { kt[8 + i] = LD128(rb, vb, sb + i * db, KV_AUX); SB(); }
  if (tid == 0) __hip_atomic_store((GAS unsigned long long*)(ws + WS_STAMP) + blockIdx.x * 16, tstart, __ATOMIC_RELAXED, __HIP_MEMORY_SCOPE_AGENT);
  const f32x4 z4 = {0.f, 0.f, 0.f, 0.f};
#define LANE_VARS() int ln_ = lane; asm volatile("" : "+v"(ln_)); const int r = ln_ >> 4, c = ln_ & 15, r8 = ln_ >> 3, c8 = ln_ & 7; (void)r; (void)c; (void)r8; (void)c8;
#define MASK8() u32x4 m8; _Pragma("unroll") for (int i = 0; i < 4; ++i) m8[i] = ((c8 >> 1) == i) ? ((c8 & 1) ? 0xffff0000u : 0x0000ffffu) : 0u; const bool valid8 = (r >> 1) == (c >> 3); (void)valid8;
#define MASK16() u32x4 mk; _Pragma("unroll") for (int i = 0; i < 4; ++i) mk[i] = ((c >> 1) == i) ? ((c & 1) ? 0xffff0000u : 0x0000ffffu) : 0u;

  if (w < 2) {
    asm volatile("s_barrier" ::: "memory");
    LANE_VARS(); MASK8(); MASK16();
    asm volatile("" : "+v"(etag));
    const unsigned tag = etag + 1u;
    float mb, rden; ln_stats(xa, mb, rden);
    const int pairoff = (4 * c + 2 * (r & 1) + (r >> 1)) * 16;
    if (w == 0) {
      f32x4 qa = z4;
#pragma unroll
      for (int i = 0; i < 16; ++i) {
        const unsigned u = i < 8 ? xb0[i >> 1] : xb1[(i - 8) >> 1];
        qa = mfma(diag(xn_pk((i & 1) ? bhi(u) : blo(u), mb, rden), mk), kt[i], qa);
      }
      ST128(x32_sum2(qa[0], qa[2]), x32_sum2(qa[1], qa[3]), tag, rs, WS_Q + (h * 8 + s) * 1024 + pairoff);
      STAMP(1);
      __builtin_amdgcn_s_sleep(QSLEEP);
      u32x4 g[8];
      poll128<8, 8>(g, rs, WS_Q + h * 8192 + lane * 16, 1024, tag);
      float q0 = 0.f, q1 = 0.f;
#pragma unroll
      for (int i = 0; i < 8; ++i) { q0 += __uint_as_float(g[i][0]); q1 += __uint_as_float(g[i][2]); }
      L.qb[lane] = pkbf(q0, q1);
      if (lane == 0) L.tag = tag;
      __hip_atomic_store(&L.qready, 1u, __ATOMIC_RELEASE, __HIP_MEMORY_SCOPE_WORKGROUP);
      STAMP(2);
    } else {
      f32x4 ha = z4;
#pragma unroll
      for (int i = 0; i < 8; ++i) ha = mfma(diag(xn_pk((i & 1) ? bhi(xb0[i >> 1]) : blo(xb0[i >> 1]), mb, rden), m8), kt[i], ha);
#pragma unroll
      for (int j = 0; j < 4; ++j) { float v = valid8 ? ha[j] : 0.f; ha[j] = v + dppf<ROR8>(v); }
      {
        const float a0 = x32_sum2(ha[0], ha[2]), a1 = x32_sum2(ha[1], ha[3]);
        if (c < 6) ST128(a0, a1, tag, rs, WS_H + (h * 8 + s) * 512 + pairoff);
      }
      __builtin_amdgcn_s_sleep(HSLEEP);
      u32x2 g[6];
#pragma unroll
      for (int i = 0; i < 6; ++i) g[i] = LD64(rs, WS_H + h * 4096 + c8 * 512 + r8 * 8 + i * 64, 0, SC1);
      _Pragma("nounroll") for (unsigned spins = 0; spins < SPIN_CAP; ++spins) {
        bool ok = true;
#pragma unroll
        for (int i = 0; i < 6; ++i) ok &= g[i][1] == tag;
        if (__all(ok)) break;
        asm volatile("s_sleep 8" ::: "memory");
        if (!ok) {
#pragma unroll
          for (int i = 0; i < 6; ++i) g[i] = LD64(rs, WS_H + h * 4096 + c8 * 512 + r8 * 8 + i * 64, 0, SC1);
        }
      }
      f32x4 za = z4;
#pragma unroll
      for (int i = 0; i < 6; ++i) {
        float hv = silu(rbf(oct_sum(__uint_as_float(g[i][0]))));
        hv = i < nch ? hv : 0.f;
        za = mfma(diag(pkbf(hv, hv), m8), kt[8 + i], za);
      }
#pragma unroll
      for (int j = 0; j < 4; ++j) { float v = valid8 ? za[j] : 0.f; za[j] = v + dppf<ROR8>(v); }
      {
        const float a0 = x32_sum2(za[0], za[2]), a1 = x32_sum2(za[1], za[3]);
        if (c < 8) ST128(a0, a1, tag, rs, WS_Z + (s * 32 + h) * 512 + pairoff);
      }
      STAMP(10);
    }
    SB();
#pragma unroll
    for (int t = 0; t < 16; ++t) { kt[t] = LD128(rk, kvv + (t & 3) * 1024, kvs + (t >> 2) * 4096, KV_AUX); SB(); }
  }

  int kvs2 = kvs, kvv2 = kvv;
  asm volatile("" : "+s"(kvs2), "+v"(kvv2));
#pragma unroll
  for (int t = 0; t < 8; ++t) { vt[t] = LD128(rv, kvv2 + (t & 3) * 1024, kvs2 + (t >> 2) * 4096, KV_AUX); SB(); }
  _Pragma("nounroll") for (unsigned spins = 0; __hip_atomic_load(&L.qready, __ATOMIC_RELAXED, __HIP_MEMORY_SCOPE_WORKGROUP) == 0u && spins < (SPIN_CAP << 6); ++spins)
    __builtin_amdgcn_s_sleep(2);
  MEMFENCE();
  STAMPW(3, 2);
  const u32x4 qp = *(const u32x4*)&L.qb[4 * (lane & 15)];
  const unsigned tag = L.tag;
  float xs[16];
#pragma unroll
  for (int t = 0; t < 16; ++t) {
    float d = dot2(kt[t][0], qp[0], 0.f);
    d = dot2(kt[t][1], qp[1], d); d = dot2(kt[t][2], qp[2], d); d = dot2(kt[t][3], qp[3], d);
    xs[t] = d;
    if (t < 8) { asm volatile("" : "+v"(xs[t])); SB(); vt[t + 8] = LD128(rv, kvv2 + (t & 3) * 1024, kvs2 + ((t + 8) >> 2) * 4096, KV_AUX); SB(); }
  }
  asm volatile("" :: "v"(etag));
  LANE_VARS();
#define TR_STEP(HALF, CTRL, BIT) _Pragma("unroll") for (int i = 0; i < (HALF); ++i) { \
    const float t1 = xs[i] + dppf<CTRL>(xs[i]), t2 = xs[i + (HALF)] + dppf<CTRL>(xs[i + (HALF)]); xs[i] = (c & (BIT)) ? t2 : t1; }
  TR_STEP(8, ROR8, 8) TR_STEP(4, XOR7, 4) TR_STEP(2, XOR2, 2) TR_STEP(1, XOR1, 1)
#undef TR_STEP
  const float sc = rbf(rbf(xs[0]) * 0.08838834764831845f) * LOG2E;
  const float wm = wave_max(sc);
  const float p = __builtin_amdgcn_exp2f(sc - wm);
  {
    const float wsu = wave_sum(p);
    if (lane == 0) {
      L.wmax[w] = wm; L.wsum[w] = wsu;
      __hip_atomic_fetch_add(&L.cnt, 1u, __ATOMIC_RELEASE, __HIP_MEMORY_SCOPE_WORKGROUP);
    }
  }
  const unsigned pp = pkbf(p, p);
  MASK16();
  MASK8();
  const bool fin = w == 5 && (h & 7) == s;
  int a_part = w * 512 + (8 * c + 4 * r) * 4;
  int a_mrg = r * 2048 + (32 * w + 2 * c) * 4;
  int a_od = 32 * w + 2 * c + (r >> 1);
  int a_yw = (2 * w + (r >> 1)) * 256 + (8 * c8 + 4 * (r & 1)) * 4;
  int a_yr = r8 * 1024 + (16 * w + 2 * c8) * 4;
  int a_row = (8 * w + r8) * 4;
  int o_f = (w == 4 && lane < 8) ? WS_F + h * 128 + c8 * 8 : OOB, o_o = WS_O + h * 8192 + c8 * 1024 + (8 * w + r8) * 8;
  int o_pub = (r & 1) ? OOB : WS_O + (h * 8 + s) * 1024 + a_od * 8;
  int o_y = lane < 8 ? WS_Y + (s * 32 + h) * 512 + w * 128 + lane * 16 : OOB;
  int o_fin = s * 16384 + (h >> 3) * 128 + r8 * 512 + c8 * 16;
  int o_out = lane < 8 ? 128 * s + 32 * (h >> 3) + 4 * lane : OOB;
  asm volatile("" : "+v"(a_part), "+v"(a_mrg), "+v"(a_od), "+v"(a_yw), "+v"(a_yr), "+v"(a_row), "+v"(o_f), "+v"(o_o), "+v"(o_pub), "+v"(o_y), "+v"(o_fin), "+v"(o_out));
  asm volatile("" : "+v"(m8[0]), "+v"(m8[1]), "+v"(m8[2]), "+v"(m8[3]));
  const unsigned long long mine = __ballot(c8 == s), vmask = __ballot(valid8);
  char* const lds = (char*)&L;
  f32x4 oa = z4;
#define PV(T) oa = mfma(diag(dppu<0x150 + (T)>(pp), mk), vt[T], oa);
  PV(0) PV(1) PV(2) PV(3) PV(4) PV(5) PV(6) PV(7) PV(8) PV(9) PV(10) PV(11)
  SB();
  const u32x4 wov = LD128(mk_rsrc((void*)po, 4u << 20), r8 * 1024 + c8 * 16, (h * DH + 8 * w) * 1024 + s * 128, KV_AUX);
  SB();
  PV(12) PV(13) PV(14) PV(15)
#undef PV
  if (lane < 32) *(f32x4*)(lds + offsetof(Lds, part) + a_part) = oa;
  STAMPW(5, 15);
  float g0 = 0.f, g1 = 0.f;
  unsigned xo = 0;
  f32x4 fw = z4;
  if (w < 4) {
    _Pragma("nounroll") for (unsigned spins = 0; LDS_LD(&L.cnt) != 16u && spins < (SPIN_CAP << 6); ++spins) __builtin_amdgcn_s_sleep(2);
    MEMFENCE();
    const float wmc = L.wmax[c];
    const f32x4 wm4 = *(const f32x4*)&L.wmax[4 * r];
    const float M = row_max(wmc);
    const float l = row_sum(L.wsum[c] * __builtin_amdgcn_exp2f(wmc - M));
    if (tid == 0) {
      const float lse = M + __builtin_amdgcn_logf(l);
      if (!LATEFLAG) ST64(lse, tag, rs, WS_F + h * 128 + s * 8);
      L.lse = lse;
    }
    STAMPW(4, 0);
    const float rl = __builtin_amdgcn_rcpf(l);
#pragma unroll
    for (int i = 0; i < 4; ++i) fw[i] = __builtin_amdgcn_exp2f(wm4[i] - M) * rl;
  } else if (fin) {
    xo = __builtin_amdgcn_raw_buffer_load_b32(mk_rsrc((void*)px, 1024), o_out, 0, 0);
    u32x4 gz[4];
    poll128<4, 8>(gz, rs, WS_Z + o_fin, 4096, tag);
    float v0 = (__uint_as_float(gz[0][0]) + __uint_as_float(gz[1][0])) + (__uint_as_float(gz[2][0]) + __uint_as_float(gz[3][0]));
    float v1 = (__uint_as_float(gz[0][2]) + __uint_as_float(gz[1][2])) + (__uint_as_float(gz[2][2]) + __uint_as_float(gz[3][2]));
    v0 += dppf<ROR8>(v0); v1 += dppf<ROR8>(v1);
    g0 = rbf(silu(rbf(xrow_sum(v0)))); g1 = rbf(silu(rbf(xrow_sum(v1))));
  }
  else if (w == 6) {
    unsigned long long sv;
    asm volatile("s_mov_b64 %0, exec\n s_mov_b64 exec, 0\n s_branch fz_hop0\n fz_ret:\n s_mov_b64 exec, %0" : "=s"(sv) :: "memory");
  }
#define HOP(i, nx) asm volatile("fz_hop" #i ": s_cbranch_execz " nx)
  asm volatile("s_waitcnt lgkmcnt(0)\n s_barrier" ::: "memory");
  HOP(0, "fz_hop1");
  STAMPW(6, 6);
  if (w < 4) {
    const float* src = (const float*)(lds + offsetof(Lds, part) + a_mrg);
    const f32x2 v0 = *(const f32x2*)src, v1 = *(const f32x2*)(src + 128), v2 = *(const f32x2*)(src + 256), v3 = *(const f32x2*)(src + 384);
    const float a0 = (fw[0] * v0[0] + fw[1] * v1[0]) + (fw[2] * v2[0] + fw[3] * v3[0]);
    const float a1 = (fw[0] * v0[1] + fw[1] * v1[1]) + (fw[2] * v2[1] + fw[3] * v3[1]);
    const float a = x16_sum(x32_sum2(a0, a1));
    ST64(a, tag, rs, o_pub);
    if (LATEFLAG && tid == 0) ST64(L.lse, tag, rs, WS_F + h * 128 + s * 8);
    L.own[a_od] = a;
    STAMPW(12, 0);
  }
  u32x2 gf = LD64(rs, o_f, 0, SC1);
  u32x2 go = NOPASS1 ? u32x2{0u, ~tag} : LD64(rs, o_o, 0, SC1);
  asm volatile("s_waitcnt lgkmcnt(0)\n s_barrier" ::: "memory");
  HOP(1, "fz_hop2");
  const bool me = (mine >> lane) & 1;
  const float ownv = *(const float*)(lds + offsetof(Lds, own) + a_row);
  if (w == 4) {
    gf = me ? u32x2{__float_as_uint(L.lse), tag} : gf;
    _Pragma("nounroll") for (unsigned spins = 0; spins < SPIN_CAP; ++spins) {
      if (!(__ballot(gf[1] != tag) & 0xff)) break;
      __builtin_amdgcn_s_sleep(2);
      MEMFENCE();
      if (gf[1] != tag) gf = LD64(rs, o_f, 0, SC1);
    }
    const float ls = __uint_as_float(gf[0]);
    const float f = __builtin_amdgcn_exp2f(ls - oct_max(ls));
    const float wv = f * __builtin_amdgcn_rcpf(oct_sum(f));
    if (lane < 8) L.wgt[lane] = wv;
    __hip_atomic_store(&L.go, 1u, __ATOMIC_RELEASE, __HIP_MEMORY_SCOPE_WORKGROUP);
  }
  _Pragma("nounroll") for (unsigned spins = 0; LDS_LD(&L.go) == 0u && spins < (SPIN_CAP << 6); ++spins) __builtin_amdgcn_s_sleep(1);
  MEMFENCE();
  HOP(2, "fz_hop3");
  const float wg = L.wgt[lane & 7];
  go = me ? u32x2{__float_as_uint(ownv), tag} : go;
  _Pragma("nounroll") for (unsigned spins = 0; spins < SPIN_CAP; ++spins) {
    if (!__any(go[1] != tag)) break;
    if (OSLEEP && spins) __builtin_amdgcn_s_sleep(OSLEEP);
    MEMFENCE();
    if (go[1] != tag) go = LD64(rs, o_o, 0, SC1);
  }
  HOP(3, "fz_hop4");
  STAMPW(7, 6);
  {
    const float o = oct_sum(wg * __uint_as_float(go[0]));
    const f32x4 y = mfma(diag(pkbf(o, o), m8), wov, z4);
    if ((vmask >> lane) & 1) *(f32x4*)(lds + offsetof(Lds, ypart) + a_yw) = y;
  }
  asm volatile("s_waitcnt lgkmcnt(0)\n s_barrier" ::: "memory");
  HOP(4, "fz_hop5");
  STAMPW(8, 6);
  if (w < 4) {
    const float* src = (const float*)(lds + offsetof(Lds, ypart) + a_yr);
    f32x2 a = (*(const f32x2*)src + *(const f32x2*)(src + 64)) + (*(const f32x2*)(src + 128) + *(const f32x2*)(src + 192));
    a[0] += dppf<ROR8>(a[0]); a[1] += dppf<ROR8>(a[1]);
    a[0] = xrow_sum(a[0]); a[1] = xrow_sum(a[1]);
    ST128(a[0], a[1], tag, rs, o_y);
  } else if (fin) {
    HOP(5, "fz_hop6");
    u32x4 gy[4];
    poll128<4, 0>(gy, rs, WS_Y + o_fin, 4096, tag);
    HOP(6, "fz_hop7");
    STAMP(9);
    float v0 = (__uint_as_float(gy[0][0]) + __uint_as_float(gy[1][0])) + (__uint_as_float(gy[2][0]) + __uint_as_float(gy[3][0]));
    float v1 = (__uint_as_float(gy[0][2]) + __uint_as_float(gy[1][2])) + (__uint_as_float(gy[2][2]) + __uint_as_float(gy[3][2]));
    v0 += dppf<ROR8>(v0); v1 += dppf<ROR8>(v1);
    v0 = rbf(xrow_sum(v0)); v1 = rbf(xrow_sum(v1));
    const float o0 = blo(xo) + rbf(v0 * g0), o1 = bhi(xo) + rbf(v1 * g1);
    __builtin_amdgcn_raw_buffer_store_b32(pkbf(o0, o1), mk_rsrc((void*)pout, 1024), o_out, 0, 0);
    HOP(7, "fz_ret");
    if (b == 0 && lane == 0) __builtin_amdgcn_raw_buffer_store_b32(tag, rs, WS_EPOCH, 0, SC1);
    STAMP(11);
    if (b == 0) {
      const unsigned long long te = __builtin_amdgcn_s_memrealtime();
      int st = 0, mx = -0x7fffffff, mn = 0x7fffffff, sum = 0;
      for (int i = 0; i < 4; ++i) {
        const int d0 = (int)(te - __hip_atomic_load((GAS unsigned long long*)(ws + WS_STAMP) + (lane + 64 * i) * 16, __ATOMIC_RELAXED, __HIP_MEMORY_SCOPE_AGENT));
        const int dk = PROBE_K == 99 ? 0 : (int)(te - __hip_atomic_load((GAS unsigned long long*)(ws + WS_STAMP) + (lane + 64 * i) * 16 + (PROBE_K & 15), __ATOMIC_RELAXED, __HIP_MEMORY_SCOPE_AGENT));
        st = max(st, d0); mx = max(mx, dk); mn = min(mn, dk); sum += dk;
      }
      for (int o = 32; o > 0; o >>= 1) { st = max(st, __shfl_xor(st, o)); mn = min(mn, __shfl_xor(mn, o)); mx = max(mx, __shfl_xor(mx, o)); sum += __shfl_xor(sum, o); }
      int v = PROBE_M == 0 ? st - mn : PROBE_M == 1 ? st - sum / 256 : PROBE_M == 2 ? st - mx : 0;
      int dl = 4 * __builtin_amdgcn_readfirstlane(v);
      dl = min(max(dl, 0), 100000) + 200;
      while ((int)(__builtin_amdgcn_s_memrealtime() - te) < dl) __builtin_amdgcn_s_sleep(1);
    }
  }
}

extern "C" void kernel_launch(void* const* d_in, const int* in_sizes, int n_in, void* d_out, int out_size, void* d_ws, size_t ws_size, hipStream_t stream) {
  fused<<<256, 1024, 0, stream>>>((const u16*)d_in[0], (const u16*)d_in[1], (const u16*)d_in[2], (const u16*)d_in[3], (const u16*)d_in[4],
                                  (const u16*)d_in[6], (const u16*)d_in[7], (u16*)d_out, (unsigned char*)d_ws);
}
```

```cpp
#include <hip/hip_runtime.h>
#include <stdint.h>
#include <stddef.h>

typedef unsigned short u16;
typedef unsigned u32x4 __attribute__((ext_vector_type(4)));
typedef unsigned u32x2 __attribute__((ext_vector_type(2)));
typedef float f32x4 __attribute__((ext_vector_type(4)));
typedef float f32x2 __attribute__((ext_vector_type(2)));
typedef __bf16 bf16x8 __attribute__((ext_vector_type(8)));
typedef __bf16 bf16x2 __attribute__((ext_vector_type(2)));
typedef __amdgpu_buffer_rsrc_t rsrc_t;
#define GAS __attribute__((address_space(1)))

#define KV_AUX 2
#define STAMPS 0
#define QSLEEP 16
#define HSLEEP 32
#define HOPS 1
#define DK 67
#define DV 67
#define D0 100
#define DL1 8
#define DL2 8
#define P2 0
#define NB 2
#define PF_BYTES 8192

constexpr int D = 512, SK = 8192, DH = 128, FF = 1376;
constexpr float LOG2E = 1.4426950408889634f;
constexpr int SC1 = 16;
constexpr unsigned WS_EPOCH = 0;
constexpr unsigned WS_Q = 4096;
constexpr unsigned WS_H = WS_Q + 256 * 1024;
constexpr unsigned WS_F = WS_H + 256 * 512;
constexpr unsigned WS_O = WS_F + 32 * 128;
constexpr unsigned WS_Y = WS_O + 256 * 1024;
constexpr unsigned WS_Z = WS_Y + 256 * 512;
constexpr unsigned WS_END = WS_Z + 256 * 512;
constexpr unsigned WS_STAMP = 2u << 20;
constexpr unsigned SPIN_CAP = 1u << 18;

__device__ __forceinline__ float rbf(float f) { return (float)(__bf16)f; }
__device__ __forceinline__ float blo(unsigned u) { return __uint_as_float(u << 16); }
__device__ __forceinline__ float bhi(unsigned u) { return __uint_as_float(u & 0xffff0000u); }
__device__ __forceinline__ unsigned pkbf(float a, float b) { bf16x2 v = {(__bf16)a, (__bf16)b}; return __builtin_bit_cast(unsigned, v); }
__device__ __forceinline__ float dot2(unsigned a, unsigned b, float acc) {
  return __builtin_amdgcn_fdot2_f32_bf16(__builtin_bit_cast(bf16x2, a), __builtin_bit_cast(bf16x2, b), acc, false);
}
template <int CTRL> __device__ __forceinline__ float dppf(float x) {
  return __builtin_bit_cast(float, __builtin_amdgcn_mov_dpp(__builtin_bit_cast(int, x), CTRL, 0xf, 0xf, true));
}
template <int CTRL> __device__ __forceinline__ unsigned dppu(unsigned x) {
  return (unsigned)__builtin_amdgcn_mov_dpp((int)x, CTRL, 0xf, 0xf, true);
}
constexpr int XOR1 = 0xB1, XOR2 = 0x4E, XOR7 = 0x141, ROR8 = 0x128, ROR4 = 0x124, ROR2 = 0x122;
__device__ __forceinline__ float x16_sum(float x) {
  auto s = __builtin_amdgcn_permlane16_swap(__float_as_uint(x), __float_as_uint(x), false, false);
  return __uint_as_float(s[0]) + __uint_as_float(s[1]);
}
__device__ __forceinline__ float x32_sum2(float a, float b) {
  auto t = __builtin_amdgcn_permlane32_swap(__float_as_uint(a), __float_as_uint(b), false, false);
  return __uint_as_float(t[0]) + __uint_as_float(t[1]);
}
__device__ __forceinline__ float x32_sum(float x) { return x32_sum2(x, x); }
__device__ __forceinline__ float xrow_sum(float x) { return x32_sum(x16_sum(x)); }
__device__ __forceinline__ float fmx(float a, float b) { return __builtin_amdgcn_fmed3f(a, b, __builtin_inff()); }
__device__ __forceinline__ float xrow_max(float x) {
  auto s = __builtin_amdgcn_permlane16_swap(__float_as_uint(x), __float_as_uint(x), false, false);
  x = fmx(__uint_as_float(s[0]), __uint_as_float(s[1]));
  auto t = __builtin_amdgcn_permlane32_swap(__float_as_uint(x), __float_as_uint(x), false, false);
  return fmx(__uint_as_float(t[0]), __uint_as_float(t[1]));
}
__device__ __forceinline__ float oct_sum(float x) { x += dppf<XOR1>(x); x += dppf<XOR2>(x); x += dppf<XOR7>(x); return x; }
__device__ __forceinline__ float oct_max(float x) { x = fmx(x, dppf<XOR1>(x)); x = fmx(x, dppf<XOR2>(x)); x = fmx(x, dppf<XOR7>(x)); return x; }
__device__ __forceinline__ float row_sum(float x) { x = oct_sum(x); x += dppf<ROR8>(x); return x; }
__device__ __forceinline__ float row_max(float x) { x = oct_max(x); x = fmx(x, dppf<ROR8>(x)); return x; }
__device__ __forceinline__ float wave_sum(float x) { return xrow_sum(row_sum(x)); }
__device__ __forceinline__ float wave_max(float x) { return xrow_max(row_max(x)); }
__device__ __forceinline__ f32x4 mfma(u32x4 a, u32x4 b, f32x4 c) {
  return __builtin_amdgcn_mfma_f32_16x16x32_bf16(__builtin_bit_cast(bf16x8, a), __builtin_bit_cast(bf16x8, b), c, 0, 0, 0);
}
__device__ __forceinline__ u32x4 diag(unsigned pk, const u32x4& m) { return u32x4{pk & m[0], pk & m[1], pk & m[2], pk & m[3]}; }
__device__ __forceinline__ float silu(float z) { return z * __builtin_amdgcn_rcpf(1.f + __builtin_amdgcn_exp2f(-z * LOG2E)); }
__device__ __forceinline__ rsrc_t mk_rsrc(const void* p, unsigned bytes) { return __builtin_amdgcn_make_buffer_rsrc((void*)p, 0, bytes, 0x00020000); }
#define LD128(rs, vo, so, aux) __builtin_amdgcn_raw_buffer_load_b128(rs, vo, so, aux)
#define LD64(rs, vo, so, aux) __builtin_amdgcn_raw_buffer_load_b64(rs, vo, so, aux)
#define ST128(a, b, tag, rs, vo) __builtin_amdgcn_raw_buffer_store_b128((u32x4{__float_as_uint(a), tag, __float_as_uint(b), tag}), rs, vo, 0, SC1)
#define ST64(a, tag, rs, vo) __builtin_amdgcn_raw_buffer_store_b64((u32x2{__float_as_uint(a), tag}), rs, vo, 0, SC1)
#define LDS_LD(p) __hip_atomic_load(p, __ATOMIC_RELAXED, __HIP_MEMORY_SCOPE_WORKGROUP)
#define PACE(STEP) asm volatile("1: s_memrealtime s[90:91]\n s_waitcnt lgkmcnt(0)\n s_sub_u32 s90, s90, %0\n s_cmp_lt_i32 s90, 0\n s_cbranch_scc0 2f\n s_sleep 2\n s_branch 1b\n 2: s_add_u32 %0, %0, %1" : "+s"(dl) : "i"(STEP) : "scc", "s90", "s91")
#define SB() __builtin_amdgcn_sched_barrier(0)
#define MEMFENCE() asm volatile("" ::: "memory")
#define STAMP(i) do {} while (0)
#define STAMPW(i, wv) do {} while (0)

constexpr int OOB = 0x40000000;
template <int N, int SLEEP> __device__ __forceinline__ void poll128(u32x4 (&g)[N], rsrc_t rs, int off, int stride, unsigned tag) {
#pragma unroll
  for (int i = 0; i < N; ++i) g[i] = LD128(rs, off + i * stride, 0, SC1);
  _Pragma("nounroll") for (unsigned spins = 0; spins < SPIN_CAP; ++spins) {
    bool bad[N], any = false;
#pragma unroll
    for (int i = 0; i < N; ++i) { bad[i] = (g[i][1] != tag) | (g[i][3] != tag); any |= bad[i]; }
    if (!__any(any)) break;
    if (SLEEP) __builtin_amdgcn_s_sleep(SLEEP);
    MEMFENCE();
    u32x4 n[N];
#pragma unroll
    for (int i = 0; i < N; ++i) n[i] = LD128(rs, bad[i] ? off + i * stride : OOB, 0, SC1);
    SB();
#pragma unroll
    for (int i = 0; i < N; ++i) g[i] = bad[i] ? n[i] : g[i];
  }
}

struct Lds {
  float part[16][128];
  float ypart[16][64];
  float wmax[16];
  float wsum[16];
  unsigned qb[64];
  float own[128];
  unsigned tag, qready, cnt;
};

__device__ __forceinline__ void ln_stats(const u32x4 xa, float& mb, float& rden) {
  float xv[8];
#pragma unroll
  for (int j = 0; j < 4; ++j) { xv[2 * j] = blo(xa[j]); xv[2 * j + 1] = bhi(xa[j]); }
  float sm = ((xv[0] + xv[1]) + (xv[2] + xv[3])) + ((xv[4] + xv[5]) + (xv[6] + xv[7]));
  const float mean = wave_sum(sm) * (1.f / D);
  float sq = 0.f;
#pragma unroll
  for (int j = 0; j < 8; ++j) { const float d = xv[j] - mean; sq = fmaf(d, d, sq); }
  const float var = rbf(wave_sum(sq) * (1.f / D));
  mb = rbf(mean);
  rden = __builtin_amdgcn_rcpf(rbf(__builtin_amdgcn_sqrtf(rbf(var + 1e-5f))));
}
__device__ __forceinline__ unsigned xn_pk(float xi, float mb, float rden) { const float v = rbf(xi - mb) * rden; return pkbf(v, v); }

__global__ void __launch_bounds__(1024) fused(const u16* __restrict__ x, const u16* __restrict__ kc, const u16* __restrict__ vc,
                                               const u16* __restrict__ wq, const u16* __restrict__ wo, const u16* __restrict__ w1,
                                               const u16* __restrict__ w2, u16* __restrict__ out, unsigned char* ws) {
  __shared__ __attribute__((aligned(16))) Lds L;
  const unsigned long long tstart = __builtin_amdgcn_s_memrealtime();
  unsigned long long px = (unsigned long long)x, pk = (unsigned long long)kc, pv = (unsigned long long)vc, pq = (unsigned long long)wq,
                     po = (unsigned long long)wo, p1 = (unsigned long long)w1, p2 = (unsigned long long)w2, pout = (unsigned long long)out,
                     pws = (unsigned long long)ws;
  const int b = blockIdx.x;
  const int h = (b & 7) * 4 + (b >> 6), s = (b >> 3) & 7;
  const int tid = threadIdx.x, lane = tid & 63, w = __builtin_amdgcn_readfirstlane(tid >> 6);
  int kvs = (h * SK + s * 1024 + w * 64) * 256, kvv = lane * 16;
  asm volatile("" : "+s"(kvs), "+v"(kvv));
  asm volatile("" : "+s"(px), "+s"(pk), "+s"(pv), "+s"(pq), "+s"(po), "+s"(p1), "+s"(p2), "+s"(pout), "+s"(pws));
  const rsrc_t rk = mk_rsrc((void*)pk, 64u << 20);
  const rsrc_t rs = mk_rsrc((void*)pws, WS_END);
  const f32x4 z4 = {0.f, 0.f, 0.f, 0.f};
  unsigned dl = (unsigned)tstart + D0 + (w * DK) / 16;
#define LANE_VARS() int ln_ = lane; asm volatile("" : "+v"(ln_)); const int r = ln_ >> 4, c = ln_ & 15, r8 = ln_ >> 3, c8 = ln_ & 7; (void)r; (void)c; (void)r8; (void)c8;
#define MASK8() u32x4 m8; _Pragma("unroll") for (int i = 0; i < 4; ++i) m8[i] = ((c8 >> 1) == i) ? ((c8 & 1) ? 0xffff0000u : 0x0000ffffu) : 0u; const bool valid8 = (r >> 1) == (c >> 3); (void)valid8;
#define MASK16() u32x4 mk; _Pragma("unroll") for (int i = 0; i < 4; ++i) mk[i] = ((c >> 1) == i) ? ((c & 1) ? 0xffff0000u : 0x0000ffffu) : 0u;
#define LDK(t) kt[t] = LD128(rk, kvv + ((t) & 3) * 1024, kvs + ((t) >> 2) * 4096, KV_AUX)
  u32x4 kt[16], vt[16];
#pragma unroll
  for (int t = 0; t < NB; ++t) { LDK(t); SB(); }
  unsigned pfv;
  {
    unsigned long long pc; unsigned len;
    asm volatile("s_getpc_b64 %0\n fz_pc: s_mov_b32 %1, fz_last-fz_pc" : "=s"(pc), "=s"(len));
    pfv = __builtin_amdgcn_raw_buffer_load_b32(mk_rsrc((void*)pc, len), lane * 64, (15 - w) * 4096, 0);
  }
  if (tid == 128) { L.qready = 0u; L.cnt = 0u; }
  const int nch = 5 + (h < 12), ch0 = 5 * h + min(h, 12);
  unsigned stag = 0;
  if (w >= 2) {
    asm volatile("s_waitcnt lgkmcnt(0)\n s_barrier" ::: "memory");
  } else {
    LANE_VARS();
    const rsrc_t rx = mk_rsrc((void*)px, 1024);
    stag = __builtin_amdgcn_raw_buffer_load_b32(rs, WS_EPOCH, 0, SC1);
    const u32x4 xa = LD128(rx, kvv, 0, 0);
    const int xoff = w == 0 ? r * 32 : r8 * 16;
    const u32x4 xb0 = LD128(rx, xoff, s * 128, 0), xb1 = LD128(rx, xoff + 16, s * 128, 0);
    u32x4 wv[16];
    if (w == 0) {
      const rsrc_t rq = mk_rsrc((void*)pq, 4u << 20);
#pragma unroll
      for (int i = 0; i < 16; ++i) { wv[i] = LD128(rq, r * (16 * 8192) + c * 16, (64 * s + i) * 8192 + h * 256, KV_AUX); SB(); }
    } else {
      const rsrc_t r1 = mk_rsrc((void*)p1, 512 * FF * 2);
#pragma unroll
      for (int i = 0; i < 8; ++i) { wv[i] = LD128(r1, r8 * (8 * FF * 2) + min(c8, nch - 1) * 16, (64 * s + i) * (FF * 2) + ch0 * 16, KV_AUX); SB(); }
#pragma unroll
      for (int i = 8; i < 16; ++i) asm volatile("" : "=v"(wv[i]));
    }
    asm volatile("s_barrier" ::: "memory");
    MASK8(); MASK16();
    asm volatile("" : "+v"(stag));
    stag += 1u;
    float mb, rden; ln_stats(xa, mb, rden);
    const int pairoff = (4 * c + 2 * (r & 1) + (r >> 1)) * 16;
    const u32x4 ma = w == 0 ? mk : m8;
    f32x4 qa = z4;
#pragma unroll
    for (int i = 0; i < 8; ++i) qa = mfma(diag(xn_pk((i & 1) ? bhi(xb0[i >> 1]) : blo(xb0[i >> 1]), mb, rden), ma), wv[i], qa);
    if (w == 0) {
#pragma unroll
      for (int i = 0; i < 8; ++i) qa = mfma(diag(xn_pk((i & 1) ? bhi(xb1[i >> 1]) : blo(xb1[i >> 1]), mb, rden), mk), wv[8 + i], qa);
    } else {
#pragma unroll
      for (int j = 0; j < 4; ++j) { float v = valid8 ? qa[j] : 0.f; qa[j] = v + dppf<ROR8>(v); }
    }
    ST128(x32_sum2(qa[0], qa[2]), x32_sum2(qa[1], qa[3]), stag, rs,
          w == 0 ? WS_Q + (h * 8 + s) * 1024 + pairoff : c < 6 ? WS_H + (h * 8 + s) * 512 + pairoff : OOB);
    STAMPW(1, 0);
    dl -= 100000;
  }
  asm volatile("" : "+s"(kvs), "+v"(kvv));
#pragma unroll
  for (int t = NB; t < 16; ++t) { PACE(DK); LDK(t); SB(); }
  if (w < 2) {
    dl += 100000;
    LANE_VARS();
    const unsigned tag = stag;
    if (w == 0) {
      __builtin_amdgcn_s_sleep(QSLEEP);
      u32x4 g[8];
      _Pragma("nounroll") for (unsigned spins = 0; spins < SPIN_CAP; ++spins) {
        bool ok = true;
#pragma unroll
        for (int i = 0; i < 8; ++i) g[i] = LD128(rs, WS_Q + h * 8192 + kvv + i * 1024, 0, SC1);
        SB();
#pragma unroll
        for (int i = 0; i < 8; ++i) ok &= (g[i][1] == tag) & (g[i][3] == tag);
        if (__all(ok)) break;
        asm volatile("s_sleep 8" ::: "memory");
      }
      float q0 = 0.f, q1 = 0.f;
#pragma unroll
      for (int i = 0; i < 8; ++i) { q0 += __uint_as_float(g[i][0]); q1 += __uint_as_float(g[i][2]); }
      L.qb[lane] = pkbf(q0, q1);
      if (lane == 0) L.tag = tag;
      __hip_atomic_store(&L.qready, 1u, __ATOMIC_RELEASE, __HIP_MEMORY_SCOPE_WORKGROUP);
      STAMP(2);
    } else {
      MASK8();
      const rsrc_t r2 = mk_rsrc((void*)p2, (ch0 + nch) * 8192);
      u32x4 wv[6];
#pragma unroll
      for (int i = 0; i < 6; ++i) { wv[i] = LD128(r2, r8 * 1024 + c8 * 16, (ch0 + i) * 8192 + s * 128, KV_AUX); SB(); }
      __builtin_amdgcn_s_sleep(HSLEEP);
      u32x2 g[6];
      _Pragma("nounroll") for (unsigned spins = 0; spins < SPIN_CAP; ++spins) {
        bool ok = true;
#pragma unroll
        for (int i = 0; i < 6; ++i) g[i] = LD64(rs, WS_H + h * 4096 + c8 * 512 + r8 * 8 + i * 64, 0, SC1);
        SB();
#pragma unroll
        for (int i = 0; i < 6; ++i) ok &= g[i][1] == tag;
        if (__all(ok)) break;
        asm volatile("s_sleep 8" ::: "memory");
      }
      f32x4 za = z4;
#pragma unroll
      for (int i = 0; i < 6; ++i) {
        float hv = silu(rbf(oct_sum(__uint_as_float(g[i][0]))));
        hv = i < nch ? hv : 0.f;
        za = mfma(diag(pkbf(hv, hv), m8), wv[i], za);
      }
#pragma unroll
      for (int j = 0; j < 4; ++j) { float v = valid8 ? za[j] : 0.f; za[j] = v + dppf<ROR8>(v); }
      ST128(x32_sum2(za[0], za[2]), x32_sum2(za[1], za[3]), tag, rs, c < 8 ? WS_Z + (s * 32 + h) * 512 + (4 * c + 2 * (r & 1) + (r >> 1)) * 16 : OOB);
      STAMP(10);
    }
  }

  asm volatile("" : "+s"(kvs), "+v"(kvv));
  const rsrc_t rv = mk_rsrc((void*)pv, 64u << 20);
#define LDV(t) do { PACE(DV); vt[t] = LD128(rv, kvv + ((t) & 3) * 1024, kvs + ((t) >> 2) * 4096, KV_AUX); SB(); } while (0)
  LDV(0); LDV(1);
  _Pragma("nounroll") for (unsigned spins = 0; LDS_LD(&L.qready) == 0u && spins < (SPIN_CAP << 6); ++spins) __builtin_amdgcn_s_sleep(2);
  MEMFENCE();
  STAMPW(3, 2);
  const u32x4 qp = *(const u32x4*)&L.qb[4 * (lane & 15)];
  const unsigned tag = L.tag;
  float xs[16];
#pragma unroll
  for (int t = 0; t < 16; ++t) {
    float d = dot2(kt[t][0], qp[0], 0.f);
    d = dot2(kt[t][1], qp[1], d); d = dot2(kt[t][2], qp[2], d); d = dot2(kt[t][3], qp[3], d);
    xs[t] = d;
  }
  asm volatile("" :: "v"(pfv));
  LANE_VARS();
#define TR_STEP(HALF, CTRL, BIT) _Pragma("unroll") for (int i = 0; i < (HALF); ++i) { \
    const float t1 = xs[i] + dppf<CTRL>(xs[i]), t2 = xs[i + (HALF)] + dppf<CTRL>(xs[i + (HALF)]); xs[i] = (c & (BIT)) ? t2 : t1; }
  TR_STEP(8, ROR8, 8) TR_STEP(4, XOR7, 4) TR_STEP(2, XOR2, 2) TR_STEP(1, XOR1, 1)
#undef TR_STEP
  asm volatile("" : "+v"(xs[0]));
  LDV(2);
  const float sc = rbf(rbf(xs[0]) * 0.08838834764831845f) * LOG2E;
  const float wm = wave_max(sc);
  const float p = __builtin_amdgcn_exp2f(sc - wm);
  {
    const float wsu = wave_sum(p);
    if (lane == 0) {
      L.wmax[w] = wm; L.wsum[w] = wsu;
      __hip_atomic_fetch_add(&L.cnt, 1u, __ATOMIC_RELEASE, __HIP_MEMORY_SCOPE_WORKGROUP);
    }
  }
  unsigned pp = pkbf(p, p);
  asm volatile("" : "+v"(pp));
  LDV(3);
  MASK16();
  MASK8();
  const int wc = w & 7;
  const bool fin = w == 5 && (h & 7) == s;
  int a_part = w * 512 + (8 * c + 4 * r) * 4;
  int a_mrg = r * 2048 + (32 * w + 2 * c) * 4;
  int a_od = (32 * w + 2 * c + (r >> 1)) * 4;
  int a_yw = (2 * wc + (r >> 1)) * 256 + (8 * c8 + 4 * (r & 1)) * 4;
  int a_yr = r8 * 512 + (16 * w + 2 * c8) * 4;
  int a_row = (16 * wc + 2 * r8) * 4;
  int o_o = c8 == s ? OOB : WS_O + h * 8192 + c8 * 1024 + wc * 128 + r8 * 16;
  int o_pub = (r & 1) ? OOB : WS_O + (h * 8 + s) * 1024 + a_od * 2;
  int o_y = lane < 8 ? WS_Y + (s * 32 + h) * 512 + w * 128 + lane * 16 : OOB;
  int o_fin = s * 16384 + (h >> 3) * 128 + r8 * 512 + c8 * 16;
  int o_out = lane < 8 ? 128 * s + 32 * (h >> 3) + 4 * lane : OOB;
  int o_wo = r8 * 2048 + c8 * 16;
  asm volatile("" : "+v"(a_part), "+v"(a_mrg), "+v"(a_od), "+v"(a_yw), "+v"(a_yr), "+v"(a_row), "+v"(o_o), "+v"(o_pub), "+v"(o_y), "+v"(o_fin), "+v"(o_out), "+v"(o_wo));
  asm volatile("" : "+v"(m8[0]), "+v"(m8[1]), "+v"(m8[2]), "+v"(m8[3]), "+v"(mk[0]), "+v"(mk[1]), "+v"(mk[2]), "+v"(mk[3]));
  const bool me = c8 == s;
  char* const lds = (char*)&L;
  f32x4 oa = z4;
#define PV(T) do { oa = mfma(diag(dppu<0x150 + (T)>(pp), mk), vt[T], oa); asm volatile("" : "+v"(oa)); } while (0)
  LDV(4);
  LDV(5);
  LDV(6); PV(0);
  LDV(7); PV(1);
  float g0 = 0.f, g1 = 0.f, M = 0.f;
  unsigned xo = 0;
  f32x4 fw = z4;
  if (w < 4) {
    _Pragma("nounroll") for (unsigned spins = 0; LDS_LD(&L.cnt) != 16u && spins < (SPIN_CAP << 6); ++spins) __builtin_amdgcn_s_sleep(2);
    MEMFENCE();
    fw = *(const f32x4*)&L.wmax[4 * r];
    if (w == 0) {
      const float wmc = L.wmax[c];
      M = row_max(wmc);
      const float lse = M + __builtin_amdgcn_logf(row_sum(L.wsum[c] * __builtin_amdgcn_exp2f(wmc - M)));
      ST64(lse, tag, rs, lane == 0 ? WS_F + h * 128 + s * 8 : OOB);
      STAMP(4);
    }
  } else if (fin) {
    xo = __builtin_amdgcn_raw_buffer_load_b32(mk_rsrc((void*)px, 1024), o_out, 0, 0);
    u32x4 gz[4];
    poll128<4, 8>(gz, rs, WS_Z + o_fin, 4096, tag);
    float v0 = (__uint_as_float(gz[0][0]) + __uint_as_float(gz[1][0])) + (__uint_as_float(gz[2][0]) + __uint_as_float(gz[3][0]));
    float v1 = (__uint_as_float(gz[0][2]) + __uint_as_float(gz[1][2])) + (__uint_as_float(gz[2][2]) + __uint_as_float(gz[3][2]));
    v0 += dppf<ROR8>(v0); v1 += dppf<ROR8>(v1);
    g0 = rbf(silu(rbf(xrow_sum(v0)))); g1 = rbf(silu(rbf(xrow_sum(v1))));
  } else if (w == 7) {
    const int off = lane < 8 ? WS_O + (h * 8 + s) * 1024 + lane * 128 : lane < 12 ? WS_Y + (s * 32 + h) * 512 + (lane - 8) * 128 : OOB;
    xo = __builtin_amdgcn_raw_buffer_load_b32(rs, off, 0, SC1);
  }
  else if (w == 6) {
    unsigned long long sv;
    asm volatile("s_mov_b64 %0, exec\n s_mov_b64 exec, 0\n s_branch fz_hop0\n fz_ret:\n s_mov_b64 exec, %0" : "=s"(sv) :: "memory");
  }
#define HOP(i, nx) asm volatile("fz_hop" #i ": s_cbranch_execz " nx)
  LDV(8); PV(2);
  LDV(9); PV(3);
  LDV(10); PV(4);
  LDV(11); PV(5);
  const int o_f = w < 4 ? WS_F + h * 128 + c8 * 8 : OOB;
  u32x2 gf = LD64(rs, o_f, 0, SC1);
  SB();
  LDV(12); PV(6);
  LDV(13); PV(7);
  if (w < 4) {
    _Pragma("nounroll") for (unsigned spins = 0; __any(gf[1] != tag) && spins < SPIN_CAP; ++spins) {
      __builtin_amdgcn_s_sleep(4);
      MEMFENCE();
      gf = LD64(rs, o_f, 0, SC1);
    }
    const float ls = __uint_as_float(gf[0]);
    const float MM = oct_max(ls);
    const float lh = MM + __builtin_amdgcn_logf(oct_sum(__builtin_amdgcn_exp2f(ls - MM)));
#pragma unroll
    for (int i = 0; i < 4; ++i) fw[i] = __builtin_amdgcn_exp2f(fw[i] - lh);
    STAMPW(13, 0);
  }
  LDV(14); PV(8);
  LDV(15);
  const rsrc_t rwo = mk_rsrc((void*)po, w >= 8 ? 4u << 20 : 0);
  const u32x4 wov0 = LD128(rwo, o_wo, (h * DH + 16 * wc) * 1024 + s * 128, KV_AUX);
  const u32x4 wov1 = LD128(rwo, o_wo + 1024, (h * DH + 16 * wc) * 1024 + s * 128, KV_AUX);
  SB();
  PV(9); PV(10); PV(11); PV(12); PV(13); PV(14); PV(15);
#undef PV
  if (lane < 32) *(f32x4*)(lds + offsetof(Lds, part) + a_part) = oa;
  STAMPW(5, 15);
  asm volatile("s_waitcnt lgkmcnt(0)\n s_barrier" ::: "memory");
  HOP(0, "fz_hop1");
  STAMPW(6, 6);
  u32x4 go;
  if (w < 4) {
    const float* src = (const float*)(lds + offsetof(Lds, part) + a_mrg);
    const f32x2 v0 = *(const f32x2*)src, v1 = *(const f32x2*)(src + 128), v2 = *(const f32x2*)(src + 256), v3 = *(const f32x2*)(src + 384);
    const f32x2 av = (fw[0] * v0 + fw[1] * v1) + (fw[2] * v2 + fw[3] * v3);
    const float a = x16_sum(x32_sum2(av[0], av[1]));
    ST64(a, tag, rs, o_pub);
    *(float*)(lds + offsetof(Lds, own) + a_od) = a;
    STAMPW(12, 0);
  }
  asm volatile("s_waitcnt lgkmcnt(0)\n s_barrier" ::: "memory");
  HOP(1, "fz_hop2");
  if (w >= 8) {
    if (DL1) __builtin_amdgcn_s_sleep(DL1);
    go = LD128(rs, o_o, 0, SC1);
    const f32x2 ownv = *(const f32x2*)(lds + offsetof(Lds, own) + a_row);
    go = me ? u32x4{__float_as_uint(ownv[0]), tag, __float_as_uint(ownv[1]), tag} : go;
    _Pragma("nounroll") for (unsigned spins = 0; spins < SPIN_CAP; ++spins) {
      const bool bad = (go[1] != tag) | (go[3] != tag);
      if (!__any(bad)) break;
      MEMFENCE();
      if (bad) go = LD128(rs, o_o, 0, SC1);
    }
    HOP(2, "fz_hop3");
    STAMPW(7, 8);
    const float o0 = oct_sum(__uint_as_float(go[0])), o1 = oct_sum(__uint_as_float(go[2]));
    f32x4 y = mfma(diag(pkbf(o0, o0), m8), wov0, z4);
    y = mfma(diag(pkbf(o1, o1), m8), wov1, y);
    if (valid8) *(f32x4*)(lds + offsetof(Lds, ypart) + a_yw) = y;
  }
  asm volatile("s_waitcnt lgkmcnt(0)\n s_barrier" ::: "memory");
  HOP(3, "fz_hop4");
  STAMPW(8, 6);
  if (w < 4) {
    HOP(4, "fz_hop5");
    const float* src = (const float*)(lds + offsetof(Lds, ypart) + a_yr);
    f32x2 a = *(const f32x2*)src + *(const f32x2*)(src + 64);
    a[0] += dppf<ROR8>(a[0]); a[1] += dppf<ROR8>(a[1]);
    a[0] = xrow_sum(a[0]); a[1] = xrow_sum(a[1]);
    ST128(a[0], a[1], tag, rs, o_y);
  } else if (fin) {
    HOP(5, "fz_hop6");
    u32x4 gy[4];
    if (DL2) __builtin_amdgcn_s_sleep(DL2);
    poll128<4, 0>(gy, rs, WS_Y + o_fin, 4096, tag);
    HOP(6, "fz_hop7");
    STAMP(9);
    float v0 = (__uint_as_float(gy[0][0]) + __uint_as_float(gy[1][0])) + (__uint_as_float(gy[2][0]) + __uint_as_float(gy[3][0]));
    float v1 = (__uint_as_float(gy[0][2]) + __uint_as_float(gy[1][2])) + (__uint_as_float(gy[2][2]) + __uint_as_float(gy[3][2]));
    v0 += dppf<ROR8>(v0); v1 += dppf<ROR8>(v1);
    v0 = rbf(xrow_sum(v0)); v1 = rbf(xrow_sum(v1));
    const float o0 = blo(xo) + rbf(v0 * g0), o1 = bhi(xo) + rbf(v1 * g1);
    __builtin_amdgcn_raw_buffer_store_b32(pkbf(o0, o1), mk_rsrc((void*)pout, 1024), o_out, 0, 0);
    HOP(7, "fz_ret");
    asm volatile("fz_last:");
    if (b == 0 && lane == 0) __builtin_amdgcn_raw_buffer_store_b32(tag, rs, WS_EPOCH, 0, SC1);
    STAMP(11);
  }
}

extern "C" void kernel_launch(void* const* d_in, const int* in_sizes, int n_in, void* d_out, int out_size, void* d_ws, size_t ws_size, hipStream_t stream) {
  fused<<<256, 1024, 0, stream>>>((const u16*)d_in[0], (const u16*)d_in[1], (const u16*)d_in[2], (const u16*)d_in[3], (const u16*)d_in[4],
                                  (const u16*)d_in[6], (const u16*)d_in[7], (u16*)d_out, (unsigned char*)d_ws);
}
```

```cpp
#include <hip/hip_runtime.h>
#include <stdint.h>
#include <stddef.h>

typedef unsigned short u16;
typedef unsigned u32x4 __attribute__((ext_vector_type(4)));
typedef unsigned u32x2 __attribute__((ext_vector_type(2)));
typedef float f32x4 __attribute__((ext_vector_type(4)));
typedef float f32x2 __attribute__((ext_vector_type(2)));
typedef __bf16 bf16x8 __attribute__((ext_vector_type(8)));
typedef __bf16 bf16x2 __attribute__((ext_vector_type(2)));
typedef __amdgpu_buffer_rsrc_t rsrc_t;
#define GAS __attribute__((address_space(1)))

#define KV_AUX 2
#define STAMPS 0
#define QSLEEP 16
#define HSLEEP 32
#define HOPS 1
#define DK 63
#define DV 63
#define D0 100
#define DL1 8
#define DL2 8
#define P2 0
#define TOUCH 1
#define PRIO 1
#define ANCH 1
#define D1 0
#define STG 0
#define TEND 2060
#define WOD 30
#define NB 2
#define PF_BYTES 8192

constexpr int D = 512, SK = 8192, DH = 128, FF = 1376;
constexpr float LOG2E = 1.4426950408889634f;
constexpr int SC1 = 16;
constexpr unsigned WS_EPOCH = 0;
constexpr unsigned WS_Q = 4096;
constexpr unsigned WS_H = WS_Q + 256 * 1024;
constexpr unsigned WS_F = WS_H + 256 * 512;
constexpr unsigned WS_O = WS_F + 32 * 128;
constexpr unsigned WS_Y = WS_O + 256 * 1024;
constexpr unsigned WS_Z = WS_Y + 256 * 512;
constexpr unsigned WS_END = WS_Z + 256 * 512;
constexpr unsigned WS_STAMP = 2u << 20;
constexpr unsigned SPIN_CAP = 1u << 18;

__device__ __forceinline__ float rbf(float f) { return (float)(__bf16)f; }
__device__ __forceinline__ float blo(unsigned u) { return __uint_as_float(u << 16); }
__device__ __forceinline__ float bhi(unsigned u) { return __uint_as_float(u & 0xffff0000u); }
__device__ __forceinline__ unsigned pkbf(float a, float b) { bf16x2 v = {(__bf16)a, (__bf16)b}; return __builtin_bit_cast(unsigned, v); }
__device__ __forceinline__ float dot2(unsigned a, unsigned b, float acc) {
  return __builtin_amdgcn_fdot2_f32_bf16(__builtin_bit_cast(bf16x2, a), __builtin_bit_cast(bf16x2, b), acc, false);
}
template <int CTRL> __device__ __forceinline__ float dppf(float x) {
  return __builtin_bit_cast(float, __builtin_amdgcn_mov_dpp(__builtin_bit_cast(int, x), CTRL, 0xf, 0xf, true));
}
template <int CTRL> __device__ __forceinline__ unsigned dppu(unsigned x) {
  return (unsigned)__builtin_amdgcn_mov_dpp((int)x, CTRL, 0xf, 0xf, true);
}
constexpr int XOR1 = 0xB1, XOR2 = 0x4E, XOR7 = 0x141, ROR8 = 0x128, ROR4 = 0x124, ROR2 = 0x122;
__device__ __forceinline__ float x16_sum(float x) {
  auto s = __builtin_amdgcn_permlane16_swap(__float_as_uint(x), __float_as_uint(x), false, false);
  return __uint_as_float(s[0]) + __uint_as_float(s[1]);
}
__device__ __forceinline__ float x32_sum2(float a, float b) {
  auto t = __builtin_amdgcn_permlane32_swap(__float_as_uint(a), __float_as_uint(b), false, false);
  return __uint_as_float(t[0]) + __uint_as_float(t[1]);
}
__device__ __forceinline__ float x32_sum(float x) { return x32_sum2(x, x); }
__device__ __forceinline__ float xrow_sum(float x) { return x32_sum(x16_sum(x)); }
__device__ __forceinline__ float fmx(float a, float b) { return __builtin_amdgcn_fmed3f(a, b, __builtin_inff()); }
__device__ __forceinline__ float xrow_max(float x) {
  auto s = __builtin_amdgcn_permlane16_swap(__float_as_uint(x), __float_as_uint(x), false, false);
  x = fmx(__uint_as_float(s[0]), __uint_as_float(s[1]));
  auto t = __builtin_amdgcn_permlane32_swap(__float_as_uint(x), __float_as_uint(x), false, false);
  return fmx(__uint_as_float(t[0]), __uint_as_float(t[1]));
}
__device__ __forceinline__ float oct_sum(float x) { x += dppf<XOR1>(x); x += dppf<XOR2>(x); x += dppf<XOR7>(x); return x; }
__device__ __forceinline__ float oct_max(float x) { x = fmx(x, dppf<XOR1>(x)); x = fmx(x, dppf<XOR2>(x)); x = fmx(x, dppf<XOR7>(x)); return x; }
__device__ __forceinline__ float row_sum(float x) { x = oct_sum(x); x += dppf<ROR8>(x); return x; }
__device__ __forceinline__ float row_max(float x) { x = oct_max(x); x = fmx(x, dppf<ROR8>(x)); return x; }
__device__ __forceinline__ float wave_sum(float x) { return xrow_sum(row_sum(x)); }
__device__ __forceinline__ float wave_max(float x) { return xrow_max(row_max(x)); }
__device__ __forceinline__ f32x4 mfma(u32x4 a, u32x4 b, f32x4 c) {
  return __builtin_amdgcn_mfma_f32_16x16x32_bf16(__builtin_bit_cast(bf16x8, a), __builtin_bit_cast(bf16x8, b), c, 0, 0, 0);
}
__device__ __forceinline__ u32x4 diag(unsigned pk, const u32x4& m) { return u32x4{pk & m[0], pk & m[1], pk & m[2], pk & m[3]}; }
__device__ __forceinline__ float silu(float z) { return z * __builtin_amdgcn_rcpf(1.f + __builtin_amdgcn_exp2f(-z * LOG2E)); }
__device__ __forceinline__ rsrc_t mk_rsrc(const void* p, unsigned bytes) { return __builtin_amdgcn_make_buffer_rsrc((void*)p, 0, bytes, 0x00020000); }
#define LD128(rs, vo, so, aux) __builtin_amdgcn_raw_buffer_load_b128(rs, vo, so, aux)
#define LD64(rs, vo, so, aux) __builtin_amdgcn_raw_buffer_load_b64(rs, vo, so, aux)
#define ST128(a, b, tag, rs, vo) __builtin_amdgcn_raw_buffer_store_b128((u32x4{__float_as_uint(a), tag, __float_as_uint(b), tag}), rs, vo, 0, SC1)
#define ST64(a, tag, rs, vo) __builtin_amdgcn_raw_buffer_store_b64((u32x2{__float_as_uint(a), tag}), rs, vo, 0, SC1)
#define LDS_LD(p) __hip_atomic_load(p, __ATOMIC_RELAXED, __HIP_MEMORY_SCOPE_WORKGROUP)
#define PACE(STEP) asm volatile("1: s_memrealtime s[90:91]\n s_waitcnt lgkmcnt(0)\n s_sub_u32 s90, s90, %0\n s_cmp_lt_i32 s90, 0\n s_cbranch_scc0 2f\n s_sleep 2\n s_branch 1b\n 2: s_add_u32 %0, %0, %1" : "+s"(dl) : "s"(stp) : "scc", "s90", "s91")
#define SB() __builtin_amdgcn_sched_barrier(0)
#define MEMFENCE() asm volatile("" ::: "memory")
#define STAMP(i) do {} while (0)
#define STAMPW(i, wv) do {} while (0)

constexpr int OOB = 0x40000000;
template <int N, int SLEEP> __device__ __forceinline__ void poll128(u32x4 (&g)[N], rsrc_t rs, int off, int stride, unsigned tag) {
#pragma unroll
  for (int i = 0; i < N; ++i) g[i] = LD128(rs, off + i * stride, 0, SC1);
  _Pragma("nounroll") for (unsigned spins = 0; spins < SPIN_CAP; ++spins) {
    bool bad[N], any = false;
#pragma unroll
    for (int i = 0; i < N; ++i) { bad[i] = (g[i][1] != tag) | (g[i][3] != tag); any |= bad[i]; }
    if (!__any(any)) break;
    if (SLEEP) __builtin_amdgcn_s_sleep(SLEEP);
    MEMFENCE();
    u32x4 n[N];
#pragma unroll
    for (int i = 0; i < N; ++i) n[i] = LD128(rs, bad[i] ? off + i * stride : OOB, 0, SC1);
    SB();
#pragma unroll
    for (int i = 0; i < N; ++i) g[i] = bad[i] ? n[i] : g[i];
  }
}

struct Lds {
  float part[16][128];
  float ypart[16][64];
  float wmax[16];
  float wsum[16];
  unsigned qb[64];
  float own[128];
  unsigned tag, qready, cnt;
};

__device__ __forceinline__ void ln_stats(const u32x4 xa, float& mb, float& rden) {
  float xv[8];
#pragma unroll
  for (int j = 0; j < 4; ++j) { xv[2 * j] = blo(xa[j]); xv[2 * j + 1] = bhi(xa[j]); }
  float sm = ((xv[0] + xv[1]) + (xv[2] + xv[3])) + ((xv[4] + xv[5]) + (xv[6] + xv[7]));
  const float mean = wave_sum(sm) * (1.f / D);
  float sq = 0.f;
#pragma unroll
  for (int j = 0; j < 8; ++j) { const float d = xv[j] - mean; sq = fmaf(d, d, sq); }
  const float var = rbf(wave_sum(sq) * (1.f / D));
  mb = rbf(mean);
  rden = __builtin_amdgcn_rcpf(rbf(__builtin_amdgcn_sqrtf(rbf(var + 1e-5f))));
}
__device__ __forceinline__ unsigned xn_pk(float xi, float mb, float rden) { const float v = rbf(xi - mb) * rden; return pkbf(v, v); }

__global__ void __launch_bounds__(1024) fused(const u16* __restrict__ kc, const u16* __restrict__ vc, const u16* __restrict__ x,
                                               const u16* __restrict__ wq, const u16* __restrict__ w1, unsigned char* ws, const u16* __restrict__ wo,
                                               const u16* __restrict__ w2, u16* __restrict__ out) {
  __shared__ __attribute__((aligned(16))) Lds L;
  const unsigned long long tstart = __builtin_amdgcn_s_memrealtime();
  unsigned long long px = (unsigned long long)x, pk = (unsigned long long)kc, pv = (unsigned long long)vc, pq = (unsigned long long)wq,
                     po = (unsigned long long)wo, p1 = (unsigned long long)w1, p2 = (unsigned long long)w2, pout = (unsigned long long)out,
                     pws = (unsigned long long)ws;
  const int b = blockIdx.x;
  const int h = (b & 7) * 4 + (b >> 6), s = (b >> 3) & 7;
  const int tid = threadIdx.x, lane = tid & 63, w = __builtin_amdgcn_readfirstlane(tid >> 6);
  int kvs = (h * SK + s * 1024 + w * 64) * 256, kvv = lane * 16;
  asm volatile("" : "+s"(kvs), "+v"(kvv));
  asm volatile("" : "+s"(px), "+s"(pk), "+s"(pv), "+s"(pq), "+s"(po), "+s"(p1), "+s"(pws));
  const rsrc_t rk = mk_rsrc((void*)pk, 64u << 20);
  const rsrc_t rs = mk_rsrc((void*)pws, WS_END);
  const f32x4 z4 = {0.f, 0.f, 0.f, 0.f};
  unsigned dl, stp;
#define ANCHOR() do { const unsigned te_ = (unsigned)tstart + TEND - (15 - w) * 4; const int span_ = (int)(te_ - (unsigned)__builtin_amdgcn_s_memrealtime()) - D1; \
    stp = (unsigned)max(span_, 0) * 2260u >> 16; dl = te_ - 29 * stp; } while (0)
#define LANE_VARS() int ln_ = lane; asm volatile("" : "+v"(ln_)); const int r = ln_ >> 4, c = ln_ & 15, r8 = ln_ >> 3, c8 = ln_ & 7; (void)r; (void)c; (void)r8; (void)c8;
#define MASK8() u32x4 m8; _Pragma("unroll") for (int i = 0; i < 4; ++i) m8[i] = ((c8 >> 1) == i) ? ((c8 & 1) ? 0xffff0000u : 0x0000ffffu) : 0u; const bool valid8 = (r >> 1) == (c >> 3); (void)valid8;
#define MASK16() u32x4 mk; _Pragma("unroll") for (int i = 0; i < 4; ++i) mk[i] = ((c >> 1) == i) ? ((c & 1) ? 0xffff0000u : 0x0000ffffu) : 0u;
#define LDK(t) kt[t] = LD128(rk, kvv + ((t) & 3) * 1024, kvs + ((t) >> 2) * 4096, KV_AUX)
  u32x4 kt[16], vt[16];
  const rsrc_t rv = mk_rsrc((void*)pv, 64u << 20);
  if (PRIO) __builtin_amdgcn_s_setprio(3);
  LDK(0); SB();
  vt[0] = LD128(rv, kvv, kvs, KV_AUX); SB();
  asm volatile("" : "+s"(p2), "+s"(pout));
  unsigned pfv;
  {
    unsigned long long pc; unsigned len;
    asm volatile("s_getpc_b64 %0\n fz_pc: s_mov_b32 %1, fz_last-fz_pc" : "=s"(pc), "=s"(len));
    pfv = __builtin_amdgcn_raw_buffer_load_b32(mk_rsrc((void*)pc, len), lane * 64, (15 - w) * 4096, 0);
  }
  if (TOUCH && w == 8 && lane < 3) {
    const unsigned long long pt = lane == 0 ? po + h * (DH * 1024) : lane == 1 ? p2 + h * (43 * 1024) : pout;
    pfv = *(const GAS unsigned*)pt;
  }
  if (tid == 128) { L.qready = 0u; L.cnt = 0u; }
  const int nch = 5 + (h < 12), ch0 = 5 * h + min(h, 12);
  unsigned stag = 0;
  if (w >= 2) {
    asm volatile("s_waitcnt lgkmcnt(0)\n s_barrier" ::: "memory");
    asm volatile("" :: "v"(kt[0]));
    ANCHOR();
  } else {
    LANE_VARS();
    const rsrc_t rx = mk_rsrc((void*)px, 1024);
    stag = __builtin_amdgcn_raw_buffer_load_b32(rs, WS_EPOCH, 0, SC1);
    const u32x4 xa = LD128(rx, kvv, 0, 0);
    const int xoff = w == 0 ? r * 32 : r8 * 16;
    const u32x4 xb0 = LD128(rx, xoff, s * 128, 0), xb1 = LD128(rx, xoff + 16, s * 128, 0);
    u32x4 wv[16];
    if (w == 0) {
      const rsrc_t rq = mk_rsrc((void*)pq, 4u << 20);
#pragma unroll
      for (int i = 0; i < 16; ++i) { wv[i] = LD128(rq, r * (16 * 8192) + c * 16, (64 * s + i) * 8192 + h * 256, KV_AUX); SB(); }
    } else {
      const rsrc_t r1 = mk_rsrc((void*)p1, 512 * FF * 2);
#pragma unroll
      for (int i = 0; i < 8; ++i) { wv[i] = LD128(r1, r8 * (8 * FF * 2) + min(c8, nch - 1) * 16, (64 * s + i) * (FF * 2) + ch0 * 16, KV_AUX); SB(); }
#pragma unroll
      for (int i = 8; i < 16; ++i) asm volatile("" : "=v"(wv[i]));
    }
    asm volatile("s_barrier" ::: "memory");
    MASK8(); MASK16();
    asm volatile("" : "+v"(stag));
    ANCHOR();
    stag += 1u;
    float mb, rden; ln_stats(xa, mb, rden);
    const int pairoff = (4 * c + 2 * (r & 1) + (r >> 1)) * 16;
    const u32x4 ma = w == 0 ? mk : m8;
    f32x4 qa = z4;
#pragma unroll
    for (int i = 0; i < 8; ++i) qa = mfma(diag(xn_pk((i & 1) ? bhi(xb0[i >> 1]) : blo(xb0[i >> 1]), mb, rden), ma), wv[i], qa);
    if (w == 0) {
#pragma unroll
      for (int i = 0; i < 8; ++i) qa = mfma(diag(xn_pk((i & 1) ? bhi(xb1[i >> 1]) : blo(xb1[i >> 1]), mb, rden), mk), wv[8 + i], qa);
    } else {
#pragma unroll
      for (int j = 0; j < 4; ++j) { float v = valid8 ? qa[j] : 0.f; qa[j] = v + dppf<ROR8>(v); }
    }
    ST128(x32_sum2(qa[0], qa[2]), x32_sum2(qa[1], qa[3]), stag, rs,
          w == 0 ? WS_Q + (h * 8 + s) * 1024 + pairoff : c < 6 ? WS_H + (h * 8 + s) * 512 + pairoff : OOB);
    STAMPW(1, 0);
    dl -= 100000;
  }
  asm volatile("" : "+s"(kvs), "+v"(kvv));
#pragma unroll
  for (int t = 1; t < 16; ++t) { PACE(DK); LDK(t); SB(); }
  if (w < 2) {
    dl += 100000;
    LANE_VARS();
    const unsigned tag = stag;
    if (w == 0) {
      __builtin_amdgcn_s_sleep(QSLEEP);
      u32x4 g[8];
      _Pragma("nounroll") for (unsigned spins = 0; spins < SPIN_CAP; ++spins) {
        bool ok = true;
#pragma unroll
        for (int i = 0; i < 8; ++i) g[i] = LD128(rs, WS_Q + h * 8192 + kvv + i * 1024, 0, SC1);
        SB();
#pragma unroll
        for (int i = 0; i < 8; ++i) ok &= (g[i][1] == tag) & (g[i][3] == tag);
        if (__all(ok)) break;
        asm volatile("s_sleep 8" ::: "memory");
      }
      float q0 = 0.f, q1 = 0.f;
#pragma unroll
      for (int i = 0; i < 8; ++i) { q0 += __uint_as_float(g[i][0]); q1 += __uint_as_float(g[i][2]); }
      L.qb[lane] = pkbf(q0, q1);
      if (lane == 0) L.tag = tag;
      __hip_atomic_store(&L.qready, 1u, __ATOMIC_RELEASE, __HIP_MEMORY_SCOPE_WORKGROUP);
      STAMP(2);
    } else {
      MASK8();
      const rsrc_t r2 = mk_rsrc((void*)p2, (ch0 + nch) * 8192);
      u32x4 wv[6];
#pragma unroll
      for (int i = 0; i < 6; ++i) { wv[i] = LD128(r2, r8 * 1024 + c8 * 16, (ch0 + i) * 8192 + s * 128, KV_AUX); SB(); }
      __builtin_amdgcn_s_sleep(HSLEEP);
      u32x2 g[6];
      _Pragma("nounroll") for (unsigned spins = 0; spins < SPIN_CAP; ++spins) {
        bool ok = true;
#pragma unroll
        for (int i = 0; i < 6; ++i) g[i] = LD64(rs, WS_H + h * 4096 + c8 * 512 + r8 * 8 + i * 64, 0, SC1);
        SB();
#pragma unroll
        for (int i = 0; i < 6; ++i) ok &= g[i][1] == tag;
        if (__all(ok)) break;
        asm volatile("s_sleep 8" ::: "memory");
      }
      f32x4 za = z4;
#pragma unroll
      for (int i = 0; i < 6; ++i) {
        float hv = silu(rbf(oct_sum(__uint_as_float(g[i][0]))));
        hv = i < nch ? hv : 0.f;
        za = mfma(diag(pkbf(hv, hv), m8), wv[i], za);
      }
#pragma unroll
      for (int j = 0; j < 4; ++j) { float v = valid8 ? za[j] : 0.f; za[j] = v + dppf<ROR8>(v); }
      ST128(x32_sum2(za[0], za[2]), x32_sum2(za[1], za[3]), tag, rs, c < 8 ? WS_Z + (s * 32 + h) * 512 + (4 * c + 2 * (r & 1) + (r >> 1)) * 16 : OOB);
      STAMP(10);
    }
  }

  asm volatile("" : "+s"(kvs), "+v"(kvv));
#define LDV(t) do { PACE(DV); vt[t] = LD128(rv, kvv + ((t) & 3) * 1024, kvs + ((t) >> 2) * 4096, KV_AUX); SB(); } while (0)
  if (PRIO) __builtin_amdgcn_s_setprio(2);
  LDV(1); LDV(2); LDV(3); LDV(4);
  _Pragma("nounroll") for (unsigned spins = 0; LDS_LD(&L.qready) == 0u && spins < (SPIN_CAP << 6); ++spins) __builtin_amdgcn_s_sleep(2);
  MEMFENCE();
  STAMPW(3, 2);
  const u32x4 qp = *(const u32x4*)&L.qb[4 * (lane & 15)];
  const unsigned tag = L.tag;
  float xs[16];
#pragma unroll
  for (int t = 0; t < 16; ++t) {
    float d = dot2(kt[t][0], qp[0], 0.f);
    d = dot2(kt[t][1], qp[1], d); d = dot2(kt[t][2], qp[2], d); d = dot2(kt[t][3], qp[3], d);
    xs[t] = d;
  }
  asm volatile("" :: "v"(pfv));
  LANE_VARS();
#define TR_STEP(HALF, CTRL, BIT) _Pragma("unroll") for (int i = 0; i < (HALF); ++i) { \
    const float t1 = xs[i] + dppf<CTRL>(xs[i]), t2 = xs[i + (HALF)] + dppf<CTRL>(xs[i + (HALF)]); xs[i] = (c & (BIT)) ? t2 : t1; }
  TR_STEP(8, ROR8, 8) TR_STEP(4, XOR7, 4) TR_STEP(2, XOR2, 2) TR_STEP(1, XOR1, 1)
#undef TR_STEP
  asm volatile("" : "+v"(xs[0]));
  LDV(5);
  const float sc = rbf(rbf(xs[0]) * 0.08838834764831845f) * LOG2E;
  const float wm = wave_max(sc);
  const float p = __builtin_amdgcn_exp2f(sc - wm);
  {
    const float wsu = wave_sum(p);
    unsigned old = 0;
    if (lane == 0) {
      L.wmax[w] = wm; L.wsum[w] = wsu;
      old = __hip_atomic_fetch_add(&L.cnt, 1u, __ATOMIC_ACQ_REL, __HIP_MEMORY_SCOPE_WORKGROUP);
    }
    if (__builtin_amdgcn_readfirstlane(old) == 15u) {
      const float wmc = L.wmax[c];
      const float M = row_max(wmc);
      const float lse = M + __builtin_amdgcn_logf(row_sum(L.wsum[c] * __builtin_amdgcn_exp2f(wmc - M)));
      ST128(lse, lse, tag, rs, lane == 0 ? WS_F + h * 128 + s * 16 : OOB);
      STAMP(4);
    }
  }
  unsigned pp = pkbf(p, p);
  asm volatile("" : "+v"(pp));
  if (PRIO) __builtin_amdgcn_s_setprio(1);
  LDV(6);
  MASK16();
  MASK8();
  const int wc = w & 7;
  const bool fin = w == 5 && (h & 7) == s;
  int a_part = w * 512 + (8 * c + 4 * r) * 4;
  int a_mrg = r * 2048 + (32 * w + 2 * c) * 4;
  int a_od = (32 * w + 2 * c + (r >> 1)) * 4;
  int a_yw = (2 * wc + (r >> 1)) * 256 + (8 * c8 + 4 * (r & 1)) * 4;
  int a_yr = r8 * 512 + (16 * w + 2 * c8) * 4;
  int a_row = (16 * wc + 2 * r8) * 4;
  int o_o = c8 == s ? OOB : WS_O + h * 8192 + c8 * 1024 + wc * 128 + r8 * 16;
  int o_pub = (r & 1) ? OOB : WS_O + (h * 8 + s) * 1024 + a_od * 2;
  int o_y = lane < 8 ? WS_Y + (s * 32 + h) * 512 + w * 128 + lane * 16 : OOB;
  int o_fin = s * 16384 + (h >> 3) * 128 + r8 * 512 + c8 * 16;
  int o_out = lane < 8 ? 128 * s + 32 * (h >> 3) + 4 * lane : OOB;
  int o_wo = r8 * 2048 + c8 * 16;
  int o_f = w < 4 ? WS_F + h * 128 + c8 * 16 : fin ? WS_Z + o_fin : w != 7 ? OOB
          : lane < 8 ? WS_O + (h * 8 + s) * 1024 + lane * 128 : lane < 12 ? WS_Y + (s * 32 + h) * 512 + (lane - 8) * 128 : OOB;
  asm volatile("" : "+v"(a_part), "+v"(a_mrg), "+v"(a_od), "+v"(a_yw), "+v"(a_yr), "+v"(a_row), "+v"(o_o), "+v"(o_pub), "+v"(o_y), "+v"(o_fin), "+v"(o_out), "+v"(o_wo), "+v"(o_f));
  asm volatile("" : "+v"(m8[0]), "+v"(m8[1]), "+v"(m8[2]), "+v"(m8[3]), "+v"(mk[0]), "+v"(mk[1]), "+v"(mk[2]), "+v"(mk[3]));
  const bool me = c8 == s;
  char* const lds = (char*)&L;
  f32x4 oa = z4;
#define PV(T) do { oa = mfma(diag(dppu<0x150 + (T)>(pp), mk), vt[T], oa); asm volatile("" : "+v"(oa)); } while (0)
  LDV(7); PV(0); PV(1);
  LDV(8); PV(2);
  if (w == 6) {
    unsigned long long sv;
    asm volatile("s_mov_b64 %0, exec\n s_mov_b64 exec, 0\n s_branch fz_hop0\n fz_ret:\n s_mov_b64 exec, %0" : "=s"(sv) :: "memory");
  }
#define HOP(i, nx) asm volatile("fz_hop" #i ": s_cbranch_execz " nx)
  LDV(9); PV(3);
  LDV(10); PV(4);
  if (PRIO) __builtin_amdgcn_s_setprio(0);
  LDV(11); PV(5);
  LDV(12); PV(6);
  LDV(13); PV(7);
  u32x4 gz[4];
  gz[0] = LD128(rs, o_f, 0, SC1);
  {
    const rsrc_t rz = mk_rsrc((void*)pws, fin ? WS_END : 0);
#pragma unroll
    for (int i = 1; i < 4; ++i) gz[i] = LD128(rz, o_f, i * 4096, SC1);
  }
  const unsigned xo = __builtin_amdgcn_raw_buffer_load_b32(mk_rsrc((void*)px, fin ? 1024 : 0), o_out, 0, 0);
  SB();
  LDV(14);
  LDV(15);
  PV(8); PV(9); PV(10); PV(11);
  if (WOD >= 0) { dl = (unsigned)tstart + TEND + WOD; PACE(0); }
  const rsrc_t rwo = mk_rsrc((void*)po, w >= 8 ? 4u << 20 : 0);
  const u32x4 wov0 = LD128(rwo, o_wo, (h * DH + 16 * wc) * 1024 + s * 128, KV_AUX);
  const u32x4 wov1 = LD128(rwo, o_wo + 1024, (h * DH + 16 * wc) * 1024 + s * 128, KV_AUX);
  SB();
  PV(12); PV(13);
  float g0 = 0.f, g1 = 0.f;
  f32x4 fw = z4;
  if (w < 4) {
    u32x2 gf = {gz[0][0], gz[0][1]};
    if (__any(gf[1] != tag)) {
      _Pragma("nounroll") for (unsigned spins = 0; spins < SPIN_CAP; ++spins) {
        MEMFENCE();
        gf = LD64(rs, o_f, 0, SC1);
        if (!__any(gf[1] != tag)) break;
        __builtin_amdgcn_s_sleep(4);
      }
    }
    MEMFENCE();
    fw = *(const f32x4*)&L.wmax[4 * r];
    const float ls = __uint_as_float(gf[0]);
    const float MM = oct_max(ls);
    const float lh = MM + __builtin_amdgcn_logf(oct_sum(__builtin_amdgcn_exp2f(ls - MM)));
#pragma unroll
    for (int i = 0; i < 4; ++i) fw[i] = __builtin_amdgcn_exp2f(fw[i] - lh);
    STAMPW(13, 0);
  } else if (fin) {
    bool bad = false;
#pragma unroll
    for (int i = 0; i < 4; ++i) bad |= (gz[i][1] != tag) | (gz[i][3] != tag);
    _Pragma("nounroll") for (unsigned spins = 0; __any(bad) && spins < SPIN_CAP; ++spins) {
      __builtin_amdgcn_s_sleep(4);
      MEMFENCE();
      bad = false;
#pragma unroll
      for (int i = 0; i < 4; ++i) gz[i] = LD128(rs, o_f, i * 4096, SC1);
#pragma unroll
      for (int i = 0; i < 4; ++i) bad |= (gz[i][1] != tag) | (gz[i][3] != tag);
    }
    float v0 = (__uint_as_float(gz[0][0]) + __uint_as_float(gz[1][0])) + (__uint_as_float(gz[2][0]) + __uint_as_float(gz[3][0]));
    float v1 = (__uint_as_float(gz[0][2]) + __uint_as_float(gz[1][2])) + (__uint_as_float(gz[2][2]) + __uint_as_float(gz[3][2]));
    v0 += dppf<ROR8>(v0); v1 += dppf<ROR8>(v1);
    g0 = rbf(silu(rbf(xrow_sum(v0)))); g1 = rbf(silu(rbf(xrow_sum(v1))));
  }
  asm volatile("" :: "v"(gz[0]));
  PV(14); PV(15);
#undef PV
  if (lane < 32) *(f32x4*)(lds + offsetof(Lds, part) + a_part) = oa;
  STAMPW(5, 15);
  asm volatile("s_waitcnt lgkmcnt(0)\n s_barrier" ::: "memory");
  HOP(0, "fz_hop1");
  STAMPW(6, 6);
  u32x4 go;
  if (w < 4) {
    const float* src = (const float*)(lds + offsetof(Lds, part) + a_mrg);
    const f32x2 v0 = *(const f32x2*)src, v1 = *(const f32x2*)(src + 128), v2 = *(const f32x2*)(src + 256), v3 = *(const f32x2*)(src + 384);
    const f32x2 av = (fw[0] * v0 + fw[1] * v1) + (fw[2] * v2 + fw[3] * v3);
    const float a = x16_sum(x32_sum2(av[0], av[1]));
    ST64(a, tag, rs, o_pub);
    *(float*)(lds + offsetof(Lds, own) + a_od) = a;
    STAMPW(12, 0);
  }
  asm volatile("s_waitcnt lgkmcnt(0)\n s_barrier" ::: "memory");
  HOP(1, "fz_hop2");
  if (w >= 8) {
    f32x2 ownv = *(const f32x2*)(lds + offsetof(Lds, own) + a_row);
    asm volatile("" : "+v"(ownv));
    if (DL1) __builtin_amdgcn_s_sleep(DL1);
    go = LD128(rs, o_o, 0, SC1);
    go = me ? u32x4{__float_as_uint(ownv[0]), tag, __float_as_uint(ownv[1]), tag} : go;
    _Pragma("nounroll") for (unsigned spins = 0; spins < SPIN_CAP; ++spins) {
      const bool bad = (go[1] != tag) | (go[3] != tag);
      if (!__any(bad)) break;
      MEMFENCE();
      if (bad) go = LD128(rs, o_o, 0, SC1);
    }
    HOP(2, "fz_hop3");
    STAMPW(7, 8);
    const float o0 = oct_sum(__uint_as_float(go[0])), o1 = oct_sum(__uint_as_float(go[2]));
    f32x4 y = mfma(diag(pkbf(o0, o0), m8), wov0, z4);
    y = mfma(diag(pkbf(o1, o1), m8), wov1, y);
    if (valid8) *(f32x4*)(lds + offsetof(Lds, ypart) + a_yw) = y;
  }
  asm volatile("s_waitcnt lgkmcnt(0)\n s_barrier" ::: "memory");
  HOP(3, "fz_hop4");
  STAMPW(8, 6);
  if (w < 4) {
    HOP(4, "fz_hop5");
    const float* src = (const float*)(lds + offsetof(Lds, ypart) + a_yr);
    f32x2 a = *(const f32x2*)src + *(const f32x2*)(src + 64);
    a[0] += dppf<ROR8>(a[0]); a[1] += dppf<ROR8>(a[1]);
    a[0] = xrow_sum(a[0]); a[1] = xrow_sum(a[1]);
    ST128(a[0], a[1], tag, rs, o_y);
  } else if (fin) {
    HOP(5, "fz_hop6");
    u32x4 gy[4];
    if (DL2) __builtin_amdgcn_s_sleep(DL2);
    poll128<4, 0>(gy, rs, WS_Y + o_fin, 4096, tag);
    HOP(6, "fz_hop7");
    STAMP(9);
    float v0 = (__uint_as_float(gy[0][0]) + __uint_as_float(gy[1][0])) + (__uint_as_float(gy[2][0]) + __uint_as_float(gy[3][0]));
    float v1 = (__uint_as_float(gy[0][2]) + __uint_as_float(gy[1][2])) + (__uint_as_float(gy[2][2]) + __uint_as_float(gy[3][2]));
    v0 += dppf<ROR8>(v0); v1 += dppf<ROR8>(v1);
    v0 = rbf(xrow_sum(v0)); v1 = rbf(xrow_sum(v1));
    const float o0 = blo(xo) + rbf(v0 * g0), o1 = bhi(xo) + rbf(v1 * g1);
    __builtin_amdgcn_raw_buffer_store_b32(pkbf(o0, o1), mk_rsrc((void*)pout, 1024), o_out, 0, 0);
    HOP(7, "fz_ret");
    asm volatile("fz_last:");
    if (b == 0 && lane == 0) __builtin_amdgcn_raw_buffer_store_b32(tag, rs, WS_EPOCH, 0, SC1);
    STAMP(11);
  }
}

extern "C" void kernel_launch(void* const* d_in, const int* in_sizes, int n_in, void* d_out, int out_size, void* d_ws, size_t ws_size, hipStream_t stream) {
  fused<<<256, 1024, 0, stream>>>((const u16*)d_in[1], (const u16*)d_in[2], (const u16*)d_in[0], (const u16*)d_in[3], (const u16*)d_in[6],
                                  (unsigned char*)d_ws, (const u16*)d_in[4], (const u16*)d_in[7], (u16*)d_out);
}
```

```cpp
#include <hip/hip_runtime.h>
#include <stdint.h>
#include <stddef.h>

typedef unsigned short u16;
typedef unsigned u32x4 __attribute__((ext_vector_type(4)));
typedef unsigned u32x2 __attribute__((ext_vector_type(2)));
typedef float f32x4 __attribute__((ext_vector_type(4)));
typedef float f32x2 __attribute__((ext_vector_type(2)));
typedef __bf16 bf16x8 __attribute__((ext_vector_type(8)));
typedef __bf16 bf16x2 __attribute__((ext_vector_type(2)));
typedef __amdgpu_buffer_rsrc_t rsrc_t;
#define GAS __attribute__((address_space(1)))

#define KV_AUX 2
#define STAMPS 0
#define QSLEEP 16
#define HSLEEP 32
#define HOPS 1
#define DK 63
#define DV 63
#define D0 100
#define DL1 8
#define DL2 8
#define P2 5
#define TOUCH 1
#define PRIO 1
#define ANCH 1
#define D1 0
#define STG 0
#define TREL 1850
#define WOD 30
#define GT 1
#define NK0 2
#define LATEPF 0
#define ROT 3
#define COND_STREAM __builtin_expect(w >= 2, 1)
#define TLD 11
#define OUT_AUX 0
#define LOOK 9
#define NOWO 0
#define TB 12
#define AW 8
#define QOFF 0
#define WARM 1
#define WA "fz_wret"
#define LEAD 17
#define KEPS 5
#define TWAIT ""
#define NB 2
#define PF_BYTES 8192

constexpr int D = 512, SK = 8192, DH = 128, FF = 1376;
constexpr float LOG2E = 1.4426950408889634f;
constexpr int SC1 = 16;
constexpr int OOB = 0x40000000;
constexpr unsigned WS_EPOCH = 0;
constexpr unsigned WS_T = 4096;
constexpr unsigned WS_Q = 8192;
constexpr unsigned WS_H = WS_Q + 256 * 1024;
constexpr unsigned WS_F = WS_H + 256 * 512;
constexpr unsigned WS_O = WS_F + 32 * 128;
constexpr unsigned WS_Y = WS_O + 256 * 1024;
constexpr unsigned WS_Z = WS_Y + 256 * 512;
constexpr unsigned WS_END = WS_Z + 256 * 512;
constexpr unsigned WS_STAMP = 2u << 20;
constexpr unsigned SPIN_CAP = 1u << 18;

__device__ __forceinline__ float rbf(float f) { return (float)(__bf16)f; }
__device__ __forceinline__ float blo(unsigned u) { return __uint_as_float(u << 16); }
__device__ __forceinline__ float bhi(unsigned u) { return __uint_as_float(u & 0xffff0000u); }
__device__ __forceinline__ unsigned pkbf(float a, float b) { bf16x2 v = {(__bf16)a, (__bf16)b}; return __builtin_bit_cast(unsigned, v); }
__device__ __forceinline__ float dot2(unsigned a, unsigned b, float acc) {
  return __builtin_amdgcn_fdot2_f32_bf16(__builtin_bit_cast(bf16x2, a), __builtin_bit_cast(bf16x2, b), acc, false);
}
template <int CTRL> __device__ __forceinline__ float dppf(float x) {
  return __builtin_bit_cast(float, __builtin_amdgcn_mov_dpp(__builtin_bit_cast(int, x), CTRL, 0xf, 0xf, true));
}
template <int CTRL> __device__ __forceinline__ unsigned dppu(unsigned x) {
  return (unsigned)__builtin_amdgcn_mov_dpp((int)x, CTRL, 0xf, 0xf, true);
}
constexpr int XOR1 = 0xB1, XOR2 = 0x4E, XOR7 = 0x141, ROR8 = 0x128, ROR4 = 0x124, ROR2 = 0x122;
__device__ __forceinline__ float x16_sum(float x) {
  auto s = __builtin_amdgcn_permlane16_swap(__float_as_uint(x), __float_as_uint(x), false, false);
  return __uint_as_float(s[0]) + __uint_as_float(s[1]);
}
__device__ __forceinline__ float x32_sum2(float a, float b) {
  auto t = __builtin_amdgcn_permlane32_swap(__float_as_uint(a), __float_as_uint(b), false, false);
  return __uint_as_float(t[0]) + __uint_as_float(t[1]);
}
__device__ __forceinline__ float x32_sum(float x) { return x32_sum2(x, x); }
__device__ __forceinline__ float xrow_sum(float x) { return x32_sum(x16_sum(x)); }
__device__ __forceinline__ float fmx(float a, float b) { return __builtin_amdgcn_fmed3f(a, b, __builtin_inff()); }
__device__ __forceinline__ float xrow_max(float x) {
  auto s = __builtin_amdgcn_permlane16_swap(__float_as_uint(x), __float_as_uint(x), false, false);
  x = fmx(__uint_as_float(s[0]), __uint_as_float(s[1]));
  auto t = __builtin_amdgcn_permlane32_swap(__float_as_uint(x), __float_as_uint(x), false, false);
  return fmx(__uint_as_float(t[0]), __uint_as_float(t[1]));
}
__device__ __forceinline__ float oct_sum(float x) { x += dppf<XOR1>(x); x += dppf<XOR2>(x); x += dppf<XOR7>(x); return x; }
__device__ __forceinline__ float oct_max(float x) { x = fmx(x, dppf<XOR1>(x)); x = fmx(x, dppf<XOR2>(x)); x = fmx(x, dppf<XOR7>(x)); return x; }
__device__ __forceinline__ float row_sum(float x) { x = oct_sum(x); x += dppf<ROR8>(x); return x; }
__device__ __forceinline__ float row_max(float x) { x = oct_max(x); x = fmx(x, dppf<ROR8>(x)); return x; }
__device__ __forceinline__ float col_sum(float a, float b) { float v = x16_sum(x32_sum2(a, b)); return v + dppf<ROR8>(v); }
__device__ __forceinline__ float wave_sum(float x) { return xrow_sum(row_sum(x)); }
__device__ __forceinline__ float wave_max(float x) { return xrow_max(row_max(x)); }
__device__ __forceinline__ f32x4 mfma(u32x4 a, u32x4 b, f32x4 c) {
  return __builtin_amdgcn_mfma_f32_16x16x32_bf16(__builtin_bit_cast(bf16x8, a), __builtin_bit_cast(bf16x8, b), c, 0, 0, 0);
}
__device__ __forceinline__ u32x4 diag(unsigned pk, const u32x4& m) { return u32x4{pk & m[0], pk & m[1], pk & m[2], pk & m[3]}; }
__device__ __forceinline__ float silu(float z) { return z * __builtin_amdgcn_rcpf(1.f + __builtin_amdgcn_exp2f(-z * LOG2E)); }
__device__ __forceinline__ rsrc_t mk_rsrc(const void* p, unsigned bytes) { return __builtin_amdgcn_make_buffer_rsrc((void*)p, 0, bytes, 0x00020000); }
#define LD128(rs, vo, so, aux) __builtin_amdgcn_raw_buffer_load_b128(rs, vo, so, aux)
#define LD64(rs, vo, so, aux) __builtin_amdgcn_raw_buffer_load_b64(rs, vo, so, aux)
#define ST128(a, b, tag, rs, vo) __builtin_amdgcn_raw_buffer_store_b128((u32x4{__float_as_uint(a), tag, __float_as_uint(b), tag}), rs, vo, 0, SC1)
#define ST64(a, tag, rs, vo) __builtin_amdgcn_raw_buffer_store_b64((u32x2{__float_as_uint(a), tag}), rs, vo, 0, SC1)
#define LDS_LD(p) __hip_atomic_load(p, __ATOMIC_RELAXED, __HIP_MEMORY_SCOPE_WORKGROUP)
#define PACE(STEP) asm volatile("1: s_memrealtime s[90:91]\n s_waitcnt lgkmcnt(0)\n s_sub_u32 s90, s90, %0\n s_cmp_lt_i32 s90, 0\n s_cbranch_scc0 2f\n s_sleep 2\n s_branch 1b\n 2: s_add_u32 %0, %0, %1" : "+s"(dl) : "s"(stp) : "scc", "s90", "s91")
#define SB() __builtin_amdgcn_sched_barrier(0)
#define MEMFENCE() asm volatile("" ::: "memory")
#define STAMP(i) do {} while (0)
#define STAMPW(i, wv) do {} while (0)

template <int N, int SLEEP> __device__ __forceinline__ void poll128(u32x4 (&g)[N], rsrc_t rs, int off, int stride, unsigned tag) {
#pragma unroll
  for (int i = 0; i < N; ++i) g[i] = LD128(rs, off + i * stride, 0, SC1);
  _Pragma("nounroll") for (unsigned spins = 0; spins < SPIN_CAP; ++spins) {
    bool bad[N], any = false;
#pragma unroll
    for (int i = 0; i < N; ++i) { bad[i] = (g[i][1] != tag) | (g[i][3] != tag); any |= bad[i]; }
    if (!__any(any)) break;
    if (SLEEP) __builtin_amdgcn_s_sleep(SLEEP);
    MEMFENCE();
    u32x4 n[N];
#pragma unroll
    for (int i = 0; i < N; ++i) n[i] = LD128(rs, bad[i] ? off + i * stride : OOB, 0, SC1);
    SB();
#pragma unroll
    for (int i = 0; i < N; ++i) g[i] = bad[i] ? n[i] : g[i];
  }
}

struct Lds {
  float part[16][128];
  float ypart[16][64];
  float wmax[16];
  float wsum[16];
  unsigned qb[64];
  float own[128];
  unsigned tag, qready, cnt, tadj, tanc;
};

__device__ __forceinline__ void ln_stats(const u32x4 xa, float& mb, float& rden) {
  float xv[8];
#pragma unroll
  for (int j = 0; j < 4; ++j) { xv[2 * j] = blo(xa[j]); xv[2 * j + 1] = bhi(xa[j]); }
  float sm = ((xv[0] + xv[1]) + (xv[2] + xv[3])) + ((xv[4] + xv[5]) + (xv[6] + xv[7]));
  const float mean = wave_sum(sm) * (1.f / D);
  float sq = 0.f;
#pragma unroll
  for (int j = 0; j < 8; ++j) { const float d = xv[j] - mean; sq = fmaf(d, d, sq); }
  const float var = rbf(wave_sum(sq) * (1.f / D));
  mb = rbf(mean);
  rden = __builtin_amdgcn_rcpf(rbf(__builtin_amdgcn_sqrtf(rbf(var + 1e-5f))));
}
__device__ __forceinline__ unsigned xn_pk(float xi, float mb, float rden) { const float v = rbf(xi - mb) * rden; return pkbf(v, v); }

__global__ void __launch_bounds__(1024) fused(const u16* __restrict__ kc, const u16* __restrict__ vc, const u16* __restrict__ x,
                                               const u16* __restrict__ wq, const u16* __restrict__ w1, unsigned char* ws, const u16* __restrict__ wo,
                                               const u16* __restrict__ w2, u16* __restrict__ out) {
  __shared__ __attribute__((aligned(16))) Lds L;
  const int b = blockIdx.x;
  const int h = (b & 7) * 4 + (b >> 6), s = (b >> 3) & 7;
  const int tid = threadIdx.x, lane = tid & 63, w = __builtin_amdgcn_readfirstlane(tid >> 6);
  typedef unsigned long long u64x8 __attribute__((ext_vector_type(8)));
  u64x8 ka; unsigned long long pout;
  asm volatile("s_load_dwordx16 %0, %2, 0x0\n s_load_dwordx2 %1, %2, 0x40\n"
               " s_mov_b64 exec, 0\n s_getpc_b64 s[90:91]\n s_lshl_b32 s92, %3, 2\n s_add_u32 s90, s90, s92\n s_addc_u32 s91, s91, 0\n s_add_u32 s90, s90, 24\n s_addc_u32 s91, s91, 0\n s_setpc_b64 s[90:91]\n"
               " s_branch fz_w0\n s_branch fz_w1\n s_branch fz_w2\n s_branch fz_w3\n s_branch fz_w4\n s_branch fz_w5\n s_branch fz_w6\n s_branch fz_w7\n"
               " s_branch fz_w8\n s_branch fz_w9\n s_branch fz_w10\n s_branch fz_w11\n s_branch fz_w12\n s_branch fz_w13\n s_branch fz_w14\n s_branch fz_w15\n"
               "fz_wret: s_mov_b64 exec, -1\n"
               " s_waitcnt lgkmcnt(0)"
               : "=&s"(ka), "=&s"(pout) : "s"(__builtin_amdgcn_kernarg_segment_ptr()), "s"(w) : "s90", "s91", "s92", "scc");
#define PAD(i) asm volatile("fz_w%c0: s_cbranch_execz fz_wret" :: "i"(i))
  const unsigned long long pk = ka[0], pv = ka[1], px = ka[2], pq = ka[3], p1 = ka[4], pws = ka[5], po = ka[6], p2 = ka[7];
  int kvs = (h * SK + s * 1024 + w * 4) * 256, kvv = lane * 16;
  const rsrc_t rk = mk_rsrc((void*)pk, 64u << 20);
  const rsrc_t rs = mk_rsrc((void*)pws, WS_END);
  const f32x4 z4 = {0.f, 0.f, 0.f, 0.f};
  unsigned dl, stp;
  unsigned te, tanc;
#define ANCHOR() do { const unsigned sp_ = TREL - (15 - w) * 4 - (w < 2 ? 0 : (w - AW) * QOFF); te = tanc + sp_; stp = sp_ * (65536u * (100 + KEPS) / 100 / (31 - NK0)) >> 16; dl = tanc; } while (0)
#define REANCHOR(R) do { const unsigned tr_ = __builtin_amdgcn_readfirstlane(LDS_LD(&L.tadj)); te = tr_ ? tr_ + TREL - (15 - w) * 4 : te; const int span_ = (int)(te - dl); \
    stp = (unsigned)max(span_, 0) * (65536u / ((R) - 1)) >> 16; dl = te - ((R) - 1) * stp; } while (0)
#define LANE_VARS() int ln_ = lane; asm volatile("" : "+v"(ln_)); const int r = ln_ >> 4, c = ln_ & 15, r8 = ln_ >> 3, c8 = ln_ & 7; (void)r; (void)c; (void)r8; (void)c8;
#define MASK8() u32x4 m8; _Pragma("unroll") for (int i = 0; i < 4; ++i) m8[i] = ((c8 >> 1) == i) ? ((c8 & 1) ? 0xffff0000u : 0x0000ffffu) : 0u; const bool valid8 = (r >> 1) == (c >> 3); (void)valid8;
#define MASK16() u32x4 mk; _Pragma("unroll") for (int i = 0; i < 4; ++i) mk[i] = ((c >> 1) == i) ? ((c & 1) ? 0xffff0000u : 0x0000ffffu) : 0u;
#define TOFF(t) (kvs + (t) * 16384)
#define LDK(t) kt[t] = LD128(rk, kvv, TOFF(t), KV_AUX)
  u32x4 kt[16], vt[16];
  const rsrc_t rv = mk_rsrc((void*)pv, 64u << 20);
  if (PRIO) __builtin_amdgcn_s_setprio(3);
  LDK(0); SB();
#pragma unroll
  for (int t = 1; t < NK0; ++t) { LDK(t); SB(); }
  PAD(7);
  if (tid == 128) { L.qready = 0u; L.cnt = 0u; L.tadj = 0u; L.tanc = 0u; }
  asm volatile("s_waitcnt lgkmcnt(0)\n s_barrier" ::: "memory");
  unsigned pfv;
#define PFTOUCH() do { unsigned long long pc; unsigned len; \
    asm volatile("s_getpc_b64 %0\n fz_pc: s_mov_b32 %1, fz_last-fz_pc\n s_max_i32 %1, %1, fz_last0-fz_pc\n s_max_i32 %1, %1, fz_last1-fz_pc" : "=s"(pc), "=s"(len) :: "scc"); \
    pfv = __builtin_amdgcn_raw_buffer_load_b32(mk_rsrc((void*)pc, len), lane * 64, (15 - w) * 4096, 0); \
    SB(); } while (0)
  if (!LATEPF) PFTOUCH();
  const int nch = 5 + (h < 12), ch0 = 5 * h + min(h, 12);
  unsigned stag = 0;
  if (__builtin_expect(w < 2, 0)) {
    PAD(0);
    const bool r0 = w == 0;
    int sa0 = s * (64 * 8192) + h * 256, sa1 = s * (64 * FF * 2) + ch0 * 16;
    asm volatile("" : "+s"(sa0), "+s"(sa1));
    const int sa = r0 ? sa0 : sa1, da = r0 ? 8192 : FF * 2;
    {
      unsigned long long ta0 = pv + kvs, ta1 = po + h * (DH * 1024), tc1 = p2 + ch0 * 8192;
      asm volatile("" : "+s"(ta0), "+s"(ta1), "+s"(tc1));
      asm volatile("s_load_dword s92, %0, 0x0\n s_load_dword s93, %1, 0x0\n s_load_dword s94, %2, 0x0\n s_load_dword s95, %3, 0x0"
                   :: "s"((r0 ? pq : p1) + sa), "s"(r0 ? pws : px), "s"(r0 ? ta0 : ta1), "s"(r0 ? pout : tc1) : "s92", "s93", "s94", "s95");
    }
    const rsrc_t rx = mk_rsrc((void*)px, 1024);
    const rsrc_t ra = mk_rsrc((void*)(r0 ? pq : p1), r0 ? 4u << 20 : 512 * FF * 2), rb = mk_rsrc((void*)pq, r0 ? 4u << 20 : 0);
    LANE_VARS();
    const int hi_ = r0 ? r : r8;
    const int va = hi_ * (r0 ? 16 * 8192 : 8 * FF * 2) + min(r0 ? c : c8, r0 ? 15 : nch - 1) * 16, xoff = hi_ * (r0 ? 32 : 16);
    asm volatile("s_waitcnt lgkmcnt(0)" ::: "memory");
    stag = __builtin_amdgcn_raw_buffer_load_b32(rs, WS_EPOCH, 0, SC1);
    const u32x4 xa = LD128(rx, kvv, 0, 0);
    const u32x4 xb0 = LD128(rx, xoff, s * 128, 0), xb1 = LD128(rx, xoff + 16, s * 128, 0);
    u32x4 wv[16];
    SB();
    PAD(1);
#pragma unroll
    for (int i = 0; i < 16; ++i) { wv[i] = LD128(i < 8 ? ra : rb, va, sa + i * da, KV_AUX); SB(); }
    SB();
    PAD(2);
    MASK8();
    u32x4 ma;
#pragma unroll
    for (int i = 0; i < 4; ++i) ma[i] = (r0 && c >= 8) ? 0u : m8[i];
    const int pairoff = (4 * c + 2 * (r & 1) + (r >> 1)) * 16;
    int o_a = (r0 ? WS_Q + (h * 8 + s) * 1024 : WS_H + (h * 8 + s) * 512) + pairoff;
    o_a = (r0 || c < 6) ? o_a : OOB;
    asm volatile("" : "+v"(stag), "+v"(o_a), "+v"(ma));
    stag += 1u;
    float mb, rden; ln_stats(xa, mb, rden);
    asm volatile("" : "+v"(mb), "+v"(rden));
    SB();
    PAD(3);
    f32x4 qa = z4;
#pragma unroll
    for (int i = 0; i < 16; ++i) {
      const unsigned u = i < 8 ? xb0[i >> 1] : xb1[(i - 8) >> 1];
      qa = mfma(diag(xn_pk((i & 1) ? bhi(u) : blo(u), mb, rden), ma), wv[i], qa);
      if (i % 5 == 3) { asm volatile("" : "+v"(qa)); SB(); PAD(4 + i / 5); }
    }
#pragma unroll
    for (int j = 0; j < 4; ++j) { const float v = valid8 ? qa[j] : 0.f; const float t_ = v + dppf<ROR8>(v); qa[j] = r0 ? qa[j] : t_; }
    ST128(x32_sum2(qa[0], qa[2]), x32_sum2(qa[1], qa[3]), stag, rs, o_a);
    STAMPW(1, 0);
    _Pragma("nounroll") for (unsigned spins = 0; LDS_LD(&L.tanc) == 0u && spins < SPIN_CAP; ++spins) __builtin_amdgcn_s_sleep(1);
    __builtin_amdgcn_raw_buffer_store_b64(u32x2{LDS_LD(&L.tanc), stag}, rs, ((b >> 3) == LEAD && w == 1 && lane < 32) ? WS_T + lane * 128 + (b & 7) * 8 : OOB, 0, SC1);
  }
  PAD(8);
  asm volatile("" :: "v"(kt[0]));
  {
    const unsigned now = (unsigned)__builtin_amdgcn_s_memrealtime() | 1u;
    if (w == AW) L.tanc = now;
    tanc = w < 2 ? __builtin_amdgcn_readfirstlane(LDS_LD(&L.tanc)) : now;
    ANCHOR();
  }
  STAMPW(15, 9); STAMPW(14, 2);
  asm volatile("" : "+s"(kvs), "+v"(kvv));
#pragma unroll
  for (int t = NK0; t < TB; ++t) { if (((t - NK0) & 1) == 0) PAD(9 + (t - NK0) / 2); PACE(DK); LDK(t); SB(); }
  if (__builtin_expect(w < 2, 0)) {
    LANE_VARS(); MASK8();
    const unsigned tag = stag;
    if (w == 0) {
      u32x4 g[8];
      _Pragma("nounroll") for (unsigned spins = 0; spins < SPIN_CAP; ++spins) {
        bool ok = true;
#pragma unroll
        for (int i = 0; i < 8; ++i) g[i] = LD128(rs, WS_Q + h * 8192 + kvv + i * 1024, 0, SC1);
        SB();
#pragma unroll
        for (int i = 0; i < 8; ++i) ok &= (g[i][1] == tag) & (g[i][3] == tag);
        if (__all(ok)) break;
        asm volatile("s_sleep 8" ::: "memory");
      }
      float q0 = 0.f, q1 = 0.f;
#pragma unroll
      for (int i = 0; i < 8; ++i) { q0 += __uint_as_float(g[i][0]); q1 += __uint_as_float(g[i][2]); }
      L.qb[lane] = pkbf(q0, q1);
      if (lane == 0) L.tag = tag;
      __hip_atomic_store(&L.qready, 1u, __ATOMIC_RELEASE, __HIP_MEMORY_SCOPE_WORKGROUP);
      STAMP(2);
      asm volatile("fz_last0:");
    } else {
      const rsrc_t r2 = mk_rsrc((void*)p2, (ch0 + nch) * 8192);
      u32x4 wv[6];
#pragma unroll
      for (int i = 0; i < 6; ++i) { wv[i] = LD128(r2, r8 * 1024 + c8 * 16, (ch0 + i) * 8192 + s * 128, KV_AUX); SB(); }
      u32x2 g[6], gt;
      _Pragma("nounroll") for (unsigned spins = 0; spins < SPIN_CAP; ++spins) {
        gt = LD64(rs, WS_T + h * 128 + c8 * 8, 0, SC1);
#pragma unroll
        for (int i = 0; i < 6; ++i) g[i] = LD64(rs, WS_H + h * 4096 + c8 * 512 + r8 * 8 + i * 64, 0, SC1);
        SB();
        bool ok = gt[1] == tag;
#pragma unroll
        for (int i = 0; i < 6; ++i) ok &= g[i][1] == tag;
        if (__all(ok)) break;
        asm volatile("s_sleep 8" ::: "memory");
      }
      L.tadj = (tanc + (int)fminf(fmaxf(oct_sum((float)(int)(gt[0] - tanc)) * 0.125f, -300.f), 300.f)) | 1u;
      f32x4 za = z4;
#pragma unroll
      for (int i = 0; i < 6; ++i) {
        float hv = silu(rbf(oct_sum(__uint_as_float(g[i][0]))));
        hv = i < nch ? hv : 0.f;
        za = mfma(diag(pkbf(hv, hv), m8), wv[i], za);
      }
#pragma unroll
      for (int j = 0; j < 4; ++j) { float v = valid8 ? za[j] : 0.f; za[j] = v + dppf<ROR8>(v); }
      ST128(x32_sum2(za[0], za[2]), x32_sum2(za[1], za[3]), tag, rs, c < 8 ? WS_Z + (s * 32 + h) * 512 + (4 * c + 2 * (r & 1) + (r >> 1)) * 16 : OOB);
      STAMP(10);
      asm volatile("fz_last1:");
    }
  }

  asm volatile("" : "+s"(kvs), "+v"(kvv));
#pragma unroll
  for (int t = TB; t < 16; ++t) { if (((t - NK0) & 1) == 0) PAD(9 + (t - NK0) / 2); PACE(DK); LDK(t); SB(); }
  asm volatile("" : "+s"(kvs), "+v"(kvv));
#define LDV(t) do { PACE(DV); vt[t] = LD128(rv, kvv, TOFF(t), KV_AUX); SB(); } while (0)
  if (PRIO) __builtin_amdgcn_s_setprio(2);
  if (GT) REANCHOR(16);
  LDV(0); LDV(1); LDV(2); LDV(3);
  _Pragma("nounroll") for (unsigned spins = 0; LDS_LD(&L.qready) == 0u && spins < (SPIN_CAP << 6); ++spins) __builtin_amdgcn_s_sleep(2);
  MEMFENCE();
  STAMPW(3, 2);
  const u32x4 qp = *(const u32x4*)&L.qb[4 * (lane & 15)];
  const unsigned tag = L.tag;
  float xs[16];
#pragma unroll
  for (int t = 0; t < 16; ++t) {
    float d = dot2(kt[t][0], qp[0], 0.f);
    d = dot2(kt[t][1], qp[1], d); d = dot2(kt[t][2], qp[2], d); d = dot2(kt[t][3], qp[3], d);
    xs[t] = d;
  }
  asm volatile("" :: "v"(pfv));
  LANE_VARS();
#define TR_STEP(HALF, CTRL, BIT) _Pragma("unroll") for (int i = 0; i < (HALF); ++i) { \
    const float t1 = xs[i] + dppf<CTRL>(xs[i]), t2 = xs[i + (HALF)] + dppf<CTRL>(xs[i + (HALF)]); xs[i] = (c & (BIT)) ? t2 : t1; }
  TR_STEP(8, ROR8, 8) TR_STEP(4, XOR7, 4) TR_STEP(2, XOR2, 2) TR_STEP(1, XOR1, 1)
#undef TR_STEP
  asm volatile("" : "+v"(xs[0]));
  if (GT) REANCHOR(12);
  LDV(4);
  const float sc = rbf(rbf(xs[0]) * 0.08838834764831845f) * LOG2E;
  const float wm = wave_max(sc);
  const float p = __builtin_amdgcn_exp2f(sc - wm);
  {
    const float wsu = wave_sum(p);
    unsigned old = 0;
    if (lane == 0) {
      L.wmax[w] = wm; L.wsum[w] = wsu;
      old = __hip_atomic_fetch_add(&L.cnt, 1u, __ATOMIC_ACQ_REL, __HIP_MEMORY_SCOPE_WORKGROUP);
    }
    if (__builtin_amdgcn_readfirstlane(old) == 15u) {
      const float wmc = L.wmax[c];
      const float M = row_max(wmc);
      const float lse = M + __builtin_amdgcn_logf(row_sum(L.wsum[c] * __builtin_amdgcn_exp2f(wmc - M)));
      ST128(lse, lse, tag, rs, lane == 0 ? WS_F + h * 128 + s * 16 : OOB);
      STAMP(4);
    }
  }
  unsigned pp = pkbf(p, p);
  asm volatile("" : "+v"(pp));
  if (PRIO) __builtin_amdgcn_s_setprio(1);
  LDV(5);
  MASK16();
  MASK8();
  const int wc = w & 7;
  const bool fin = w == 5 && (h & 7) == s;
  int a_part = w * 512 + (8 * c + 4 * r) * 4;
  int a_mrg = r * 2048 + (32 * w + 2 * c) * 4;
  int a_od = (32 * w + 2 * c + (r >> 1)) * 4;
  int a_yw = (2 * wc + (r >> 1)) * 256 + (8 * c8 + 4 * (r & 1)) * 4;
  int a_yr = r8 * 512 + (16 * w + 2 * c8) * 4;
  int a_row = (16 * wc + 2 * r8) * 4;
  int o_o = c8 == s ? OOB : WS_O + h * 8192 + c8 * 1024 + wc * 128 + r8 * 16;
  int o_pub = (r & 1) ? OOB : WS_O + (h * 8 + s) * 1024 + a_od * 2;
  int o_y = (lane & 24) == 0 ? WS_Y + (s * 32 + h) * 512 + w * 128 + c8 * 16 + (r >> 1) * 8 : OOB;
  int o_fin = s * 16384 + (h >> 3) * 128 + r8 * 2048 + c8 * 16;
  int o_out = (lane & 24) == 0 ? 128 * s + 32 * (h >> 3) + 4 * c8 + 2 * (r >> 1) : OOB;
  int o_wo = r8 * 2048 + c8 * 16;
  int o_f = w < 4 ? WS_F + h * 128 + c8 * 16 : fin ? WS_Z + o_fin : w != 7 ? OOB
          : lane < 8 ? WS_O + (h * 8 + s) * 1024 + lane * 128 : lane < 12 ? WS_Y + (s * 32 + h) * 512 + (lane - 8) * 128 : OOB;
  asm volatile("" : "+v"(a_part), "+v"(a_mrg), "+v"(a_od), "+v"(a_yw), "+v"(a_yr), "+v"(a_row), "+v"(o_o), "+v"(o_pub), "+v"(o_y), "+v"(o_fin), "+v"(o_out), "+v"(o_wo), "+v"(o_f));
  asm volatile("" : "+v"(m8[0]), "+v"(m8[1]), "+v"(m8[2]), "+v"(m8[3]), "+v"(mk[0]), "+v"(mk[1]), "+v"(mk[2]), "+v"(mk[3]));
  const bool me = c8 == s;
  char* const lds = (char*)&L;
  f32x4 oa = z4;
#define PV(T) do { oa = mfma(diag(dppu<0x150 + (T)>(pp), mk), vt[T], oa); asm volatile("" : "+v"(oa)); } while (0)
  LDV(6); PV(0);
  LDV(7); PV(1);
  LDV(8); PV(2);
  if (w == 6) {
    unsigned long long sv;
    asm volatile("s_mov_b64 %0, exec\n s_mov_b64 exec, 0\n s_branch fz_hop0\n fz_ret:\n s_mov_b64 exec, %0" : "=s"(sv) :: "memory");
  }
#define HOP(i, nx) asm volatile("fz_hop" #i ": s_cbranch_execz " nx)
  LDV(9); PV(3);
  u32x4 gz[4];
  gz[0] = LD128(rs, o_f, 0, SC1);
  {
    const rsrc_t rz = mk_rsrc((void*)pws, fin ? WS_END : 0);
#pragma unroll
    for (int i = 1; i < 4; ++i) gz[i] = LD128(rz, o_f + i * 512, 0, SC1);
  }
  const unsigned xo = __builtin_amdgcn_raw_buffer_load_b16(mk_rsrc((void*)px, fin ? 1024 : 0), o_out, 0, 0);
  SB();
  LDV(10); PV(4);
  if (PRIO) __builtin_amdgcn_s_setprio(0);
  LDV(11); PV(5);
  LDV(12); PV(6);
  const bool bad1 = w < 4 && gz[0][1] != tag;
  const u32x2 gf2 = LD64(rs, bad1 ? o_f : OOB, 0, SC1);
  SB();
  LDV(13); PV(7);
  SB();
  LDV(14);
  LDV(15);
  PV(8); PV(9); PV(10); PV(11); PV(12); PV(13);
  if (w >= 8) { dl = te + (15 - w) * 4 + WOD; PACE(0); }
  const rsrc_t rwo = mk_rsrc((void*)po, (w >= 8 && !NOWO) ? 4u << 20 : 0);
  const u32x4 wov0 = LD128(rwo, o_wo, (h * DH + 16 * wc) * 1024 + s * 128, KV_AUX);
  const u32x4 wov1 = LD128(rwo, o_wo + 1024, (h * DH + 16 * wc) * 1024 + s * 128, KV_AUX);
  SB();
  float g0 = 0.f, g1 = 0.f;
  f32x4 fw = z4;
  if (w < 4) {
    u32x2 gf = bad1 ? gf2 : u32x2{gz[0][0], gz[0][1]};
    if (__any(gf[1] != tag)) {
      _Pragma("nounroll") for (unsigned spins = 0; spins < SPIN_CAP; ++spins) {
        MEMFENCE();
        gf = LD64(rs, o_f, 0, SC1);
        if (!__any(gf[1] != tag)) break;
        __builtin_amdgcn_s_sleep(4);
      }
    }
    MEMFENCE();
    fw = *(const f32x4*)&L.wmax[4 * r];
    const float ls = __uint_as_float(gf[0]);
    const float MM = oct_max(ls);
    const float lh = MM + __builtin_amdgcn_logf(oct_sum(__builtin_amdgcn_exp2f(ls - MM)));
#pragma unroll
    for (int i = 0; i < 4; ++i) fw[i] = __builtin_amdgcn_exp2f(fw[i] - lh);
    STAMPW(13, 0);
  } else if (fin) {
    bool bad = false;
#pragma unroll
    for (int i = 0; i < 4; ++i) bad |= (gz[i][1] != tag) | (gz[i][3] != tag);
    _Pragma("nounroll") for (unsigned spins = 0; __any(bad) && spins < SPIN_CAP; ++spins) {
      __builtin_amdgcn_s_sleep(4);
      MEMFENCE();
      bad = false;
#pragma unroll
      for (int i = 0; i < 4; ++i) gz[i] = LD128(rs, o_f + i * 512, 0, SC1);
#pragma unroll
      for (int i = 0; i < 4; ++i) bad |= (gz[i][1] != tag) | (gz[i][3] != tag);
    }
    float v0 = (__uint_as_float(gz[0][0]) + __uint_as_float(gz[1][0])) + (__uint_as_float(gz[2][0]) + __uint_as_float(gz[3][0]));
    float v1 = (__uint_as_float(gz[0][2]) + __uint_as_float(gz[1][2])) + (__uint_as_float(gz[2][2]) + __uint_as_float(gz[3][2]));
    g0 = rbf(silu(rbf(col_sum(v0, v1))));
    g1 = blo(xo);
    o_fin += WS_Y;
    asm volatile("" : "+v"(g0), "+v"(g1), "+v"(o_fin));
  }
  asm volatile("" :: "v"(gz[0]));
  PV(14); PV(15);
#undef PV
  if (lane < 32) *(f32x4*)(lds + offsetof(Lds, part) + a_part) = oa;
  STAMPW(5, 15);
  asm volatile("s_waitcnt lgkmcnt(0)\n s_barrier" ::: "memory");
  HOP(0, "fz_hop1");
  STAMPW(6, 6);
  u32x4 go;
  if (w < 4) {
    const float* src = (const float*)(lds + offsetof(Lds, part) + a_mrg);
    const f32x2 v0 = *(const f32x2*)src, v1 = *(const f32x2*)(src + 128), v2 = *(const f32x2*)(src + 256), v3 = *(const f32x2*)(src + 384);
    const f32x2 av = (fw[0] * v0 + fw[1] * v1) + (fw[2] * v2 + fw[3] * v3);
    const float a = x16_sum(x32_sum2(av[0], av[1]));
    ST64(a, tag, rs, o_pub);
    *(float*)(lds + offsetof(Lds, own) + a_od) = a;
    STAMPW(12, 0);
  }
  asm volatile("s_waitcnt lgkmcnt(0)\n s_barrier" ::: "memory");
  HOP(1, "fz_hop2");
  if (w >= 8) {
    f32x2 ownv = *(const f32x2*)(lds + offsetof(Lds, own) + a_row);
    asm volatile("" : "+v"(ownv));
    if (DL1) __builtin_amdgcn_s_sleep(DL1);
    go = LD128(rs, o_o, 0, SC1);
    go = me ? u32x4{__float_as_uint(ownv[0]), tag, __float_as_uint(ownv[1]), tag} : go;
    _Pragma("nounroll") for (unsigned spins = 0; spins < SPIN_CAP; ++spins) {
      const bool bad = (go[1] != tag) | (go[3] != tag);
      if (!__any(bad)) break;
      MEMFENCE();
      if (bad) go = LD128(rs, o_o, 0, SC1);
    }
    HOP(2, "fz_hop3");
    STAMPW(7, 8);
    const float o0 = oct_sum(__uint_as_float(go[0])), o1 = oct_sum(__uint_as_float(go[2]));
    f32x4 y = mfma(diag(pkbf(o0, o0), m8), wov0, z4);
    y = mfma(diag(pkbf(o1, o1), m8), wov1, y);
    if (valid8) *(f32x4*)(lds + offsetof(Lds, ypart) + a_yw) = y;
  }
  asm volatile("s_waitcnt lgkmcnt(0)\n s_barrier" ::: "memory");
  HOP(3, "fz_hop4");
  STAMPW(8, 6);
  if (w < 4) {
    HOP(4, "fz_hop5");
    const float* src = (const float*)(lds + offsetof(Lds, ypart) + a_yr);
    f32x2 a = *(const f32x2*)src + *(const f32x2*)(src + 64);
    ST64(col_sum(a[0], a[1]), tag, rs, o_y);
  } else if (fin) {
    HOP(5, "fz_hop6");
    if (DL2) __builtin_amdgcn_s_sleep(DL2);
    float va, vb, vc2, vd;
    typedef unsigned u32x16 __attribute__((ext_vector_type(16)));
    u32x16 keepA, keepB;
    unsigned cnt_;
    asm volatile("buffer_load_dwordx4 v[64:67], %[off], %[rs], 0 offen offset:0 sc1\n buffer_load_dwordx4 v[68:71], %[off], %[rs], 0 offen offset:512 sc1\n buffer_load_dwordx4 v[72:75], %[off], %[rs], 0 offen offset:1024 sc1\n buffer_load_dwordx4 v[76:79], %[off], %[rs], 0 offen offset:1536 sc1\n s_sleep %[p2]\n s_mov_b32 %[cnt], 262144\n"
      "1: buffer_load_dwordx4 v[80:83], %[off], %[rs], 0 offen offset:0 sc1\n buffer_load_dwordx4 v[84:87], %[off], %[rs], 0 offen offset:512 sc1\n buffer_load_dwordx4 v[88:91], %[off], %[rs], 0 offen offset:1024 sc1\n buffer_load_dwordx4 v[92:95], %[off], %[rs], 0 offen offset:1536 sc1\n s_waitcnt vmcnt(4)\n v_cmpx_eq_u32 v65, %[tag]\n v_cmpx_eq_u32 v67, %[tag]\n v_cmpx_eq_u32 v69, %[tag]\n v_cmpx_eq_u32 v71, %[tag]\n v_cmpx_eq_u32 v73, %[tag]\n v_cmpx_eq_u32 v75, %[tag]\n v_cmpx_eq_u32 v77, %[tag]\n v_cmpx_eq_u32 v79, %[tag]\n s_cmp_eq_u64 exec, -1\n s_mov_b64 exec, -1\n s_cbranch_scc1 2f\n"
      "buffer_load_dwordx4 v[64:67], %[off], %[rs], 0 offen offset:0 sc1\n buffer_load_dwordx4 v[68:71], %[off], %[rs], 0 offen offset:512 sc1\n buffer_load_dwordx4 v[72:75], %[off], %[rs], 0 offen offset:1024 sc1\n buffer_load_dwordx4 v[76:79], %[off], %[rs], 0 offen offset:1536 sc1\n s_waitcnt vmcnt(4)\n v_cmpx_eq_u32 v81, %[tag]\n v_cmpx_eq_u32 v83, %[tag]\n v_cmpx_eq_u32 v85, %[tag]\n v_cmpx_eq_u32 v87, %[tag]\n v_cmpx_eq_u32 v89, %[tag]\n v_cmpx_eq_u32 v91, %[tag]\n v_cmpx_eq_u32 v93, %[tag]\n v_cmpx_eq_u32 v95, %[tag]\n s_cmp_eq_u64 exec, -1\n s_mov_b64 exec, -1\n s_cbranch_scc1 3f\n"
      "s_sub_u32 %[cnt], %[cnt], 1\n s_cmp_lg_u32 %[cnt], 0\n s_cbranch_scc1 1b\n"
      "3: v_add_f32 %[a], v80, v84\n v_add_f32 %[b], v82, v86\n v_add_f32 %[c], v88, v92\n v_add_f32 %[d], v90, v94\n s_branch 4f\n"
      "2: v_add_f32 %[a], v64, v68\n v_add_f32 %[b], v66, v70\n v_add_f32 %[c], v72, v76\n v_add_f32 %[d], v74, v78\n 4:"
      : [a] "=&v"(va), [b] "=&v"(vb), [c] "=&v"(vc2), [d] "=&v"(vd), [cnt] "=&s"(cnt_), "=&{v[64:79]}"(keepA), "=&{v[80:95]}"(keepB)
      : [off] "v"(o_fin), [rs] "s"(rs), [tag] "v"(tag), [p2] "i"(P2) : "scc", "vcc", "memory");
    HOP(6, "fz_hop7");
    STAMP(9);
    const float ov = g1 + rbf(rbf(col_sum(va + vc2, vb + vd)) * g0);
    __builtin_amdgcn_raw_buffer_store_b16((u16)pkbf(ov, ov), mk_rsrc((void*)pout, 1024), o_out, 0, OUT_AUX);
    HOP(7, "fz_ret");
    asm volatile("fz_last:");
    if (b == 0 && lane == 0) __builtin_amdgcn_raw_buffer_store_b32(tag, rs, WS_EPOCH, 0, SC1);
    STAMP(11);
    asm volatile("" :: "v"(keepA), "v"(keepB));
  }
}

extern "C" void kernel_launch(void* const* d_in, const int* in_sizes, int n_in, void* d_out, int out_size, void* d_ws, size_t ws_size, hipStream_t stream) {
  fused<<<256, 1024, 0, stream>>>((const u16*)d_in[1], (const u16*)d_in[2], (const u16*)d_in[0], (const u16*)d_in[3], (const u16*)d_in[6],
                                  (unsigned char*)d_ws, (const u16*)d_in[4], (const u16*)d_in[7], (u16*)d_out);
}
```

```cpp
#include <hip/hip_runtime.h>
#include <stdint.h>
#include <stddef.h>

typedef unsigned short u16;
typedef unsigned u32x4 __attribute__((ext_vector_type(4)));
typedef unsigned u32x2 __attribute__((ext_vector_type(2)));
typedef float f32x4 __attribute__((ext_vector_type(4)));
typedef float f32x2 __attribute__((ext_vector_type(2)));
typedef __bf16 bf16x8 __attribute__((ext_vector_type(8)));
typedef __bf16 bf16x2 __attribute__((ext_vector_type(2)));
typedef __amdgpu_buffer_rsrc_t rsrc_t;
#define GAS __attribute__((address_space(1)))

#define KV_AUX 2
#define STAMPS 0
#define QSLEEP 16
#define HSLEEP 32
#define HOPS 1
#define DK 63
#define DV 63
#define D0 100
#define DL1 8
#define DL2 12
#define P2 5
#define TOUCH 1
#define PRIO 1
#define ANCH 1
#define D1 0
#define STG 0
#define TREL 1820
#define WOD 30
#define GT 1
#define NK0 2
#define LATEPF 0
#define ROT 3
#define COND_STREAM __builtin_expect(w >= 2, 1)
#define TLD 11
#define OUT_AUX 0
#define LOOK 9
#define NOWO 0
#define TB 12
#define AW 8
#define QOFF 0
#define WARM 1
#define WA "fz_wret"
#define LEAD 17
#define KEPS 5
#define TWAIT ""
#define GW 8
#define PAIR 0
#define ROTX h
#define XORR 0
#define RHO 0
#define LAYB 0
#define NB 2
#define PF_BYTES 8192

constexpr int D = 512, SK = 8192, DH = 128, FF = 1376;
constexpr float LOG2E = 1.4426950408889634f;
constexpr int SC1 = 16;
constexpr int OOB = 0x40000000;
constexpr unsigned WS_EPOCH = 0;
constexpr unsigned WS_T = 4096;
constexpr unsigned WS_Q = 8192;
constexpr unsigned WS_H = WS_Q + 256 * 1024;
constexpr unsigned WS_F = WS_H + 256 * 512;
constexpr unsigned WS_O = WS_F + 32 * 128;
constexpr unsigned WS_Y = WS_O + 256 * 1024;
constexpr unsigned WS_Z = WS_Y + 256 * 512;
constexpr unsigned WS_END = WS_Z + 256 * 512;
constexpr unsigned WS_STAMP = 2u << 20;
constexpr unsigned SPIN_CAP = 1u << 18;

__device__ __forceinline__ float rbf(float f) { return (float)(__bf16)f; }
__device__ __forceinline__ float blo(unsigned u) { return __uint_as_float(u << 16); }
__device__ __forceinline__ float bhi(unsigned u) { return __uint_as_float(u & 0xffff0000u); }
__device__ __forceinline__ unsigned pkbf(float a, float b) { bf16x2 v = {(__bf16)a, (__bf16)b}; return __builtin_bit_cast(unsigned, v); }
__device__ __forceinline__ float dot2(unsigned a, unsigned b, float acc) {
  return __builtin_amdgcn_fdot2_f32_bf16(__builtin_bit_cast(bf16x2, a), __builtin_bit_cast(bf16x2, b), acc, false);
}
template <int CTRL> __device__ __forceinline__ float dppf(float x) {
  return __builtin_bit_cast(float, __builtin_amdgcn_mov_dpp(__builtin_bit_cast(int, x), CTRL, 0xf, 0xf, true));
}
template <int CTRL> __device__ __forceinline__ unsigned dppu(unsigned x) {
  return (unsigned)__builtin_amdgcn_mov_dpp((int)x, CTRL, 0xf, 0xf, true);
}
constexpr int XOR1 = 0xB1, XOR2 = 0x4E, XOR7 = 0x141, ROR8 = 0x128, ROR4 = 0x124, ROR2 = 0x122;
__device__ __forceinline__ float x16_sum(float x) {
  auto s = __builtin_amdgcn_permlane16_swap(__float_as_uint(x), __float_as_uint(x), false, false);
  return __uint_as_float(s[0]) + __uint_as_float(s[1]);
}
__device__ __forceinline__ float x32_sum2(float a, float b) {
  auto t = __builtin_amdgcn_permlane32_swap(__float_as_uint(a), __float_as_uint(b), false, false);
  return __uint_as_float(t[0]) + __uint_as_float(t[1]);
}
__device__ __forceinline__ float x32_sum(float x) { return x32_sum2(x, x); }
__device__ __forceinline__ float xrow_sum(float x) { return x32_sum(x16_sum(x)); }
__device__ __forceinline__ float fmx(float a, float b) { return __builtin_amdgcn_fmed3f(a, b, __builtin_inff()); }
__device__ __forceinline__ float xrow_max(float x) {
  auto s = __builtin_amdgcn_permlane16_swap(__float_as_uint(x), __float_as_uint(x), false, false);
  x = fmx(__uint_as_float(s[0]), __uint_as_float(s[1]));
  auto t = __builtin_amdgcn_permlane32_swap(__float_as_uint(x), __float_as_uint(x), false, false);
  return fmx(__uint_as_float(t[0]), __uint_as_float(t[1]));
}
__device__ __forceinline__ float oct_sum(float x) { x += dppf<XOR1>(x); x += dppf<XOR2>(x); x += dppf<XOR7>(x); return x; }
__device__ __forceinline__ float oct_max(float x) { x = fmx(x, dppf<XOR1>(x)); x = fmx(x, dppf<XOR2>(x)); x = fmx(x, dppf<XOR7>(x)); return x; }
__device__ __forceinline__ float row_sum(float x) { x = oct_sum(x); x += dppf<ROR8>(x); return x; }
__device__ __forceinline__ float row_max(float x) { x = oct_max(x); x = fmx(x, dppf<ROR8>(x)); return x; }
__device__ __forceinline__ float col_sum(float a, float b) { float v = x16_sum(x32_sum2(a, b)); return v + dppf<ROR8>(v); }
__device__ __forceinline__ float wave_sum(float x) { return xrow_sum(row_sum(x)); }
__device__ __forceinline__ float wave_max(float x) { return xrow_max(row_max(x)); }
__device__ __forceinline__ f32x4 mfma(u32x4 a, u32x4 b, f32x4 c) {
  return __builtin_amdgcn_mfma_f32_16x16x32_bf16(__builtin_bit_cast(bf16x8, a), __builtin_bit_cast(bf16x8, b), c, 0, 0, 0);
}
__device__ __forceinline__ u32x4 diag(unsigned pk, const u32x4& m) { return u32x4{pk & m[0], pk & m[1], pk & m[2], pk & m[3]}; }
__device__ __forceinline__ float silu(float z) { return z * __builtin_amdgcn_rcpf(1.f + __builtin_amdgcn_exp2f(-z * LOG2E)); }
__device__ __forceinline__ rsrc_t mk_rsrc(const void* p, unsigned bytes) { return __builtin_amdgcn_make_buffer_rsrc((void*)p, 0, bytes, 0x00020000); }
#define LD128(rs, vo, so, aux) __builtin_amdgcn_raw_buffer_load_b128(rs, vo, so, aux)
#define LD64(rs, vo, so, aux) __builtin_amdgcn_raw_buffer_load_b64(rs, vo, so, aux)
#define ST128(a, b, tag, rs, vo) __builtin_amdgcn_raw_buffer_store_b128((u32x4{__float_as_uint(a), tag, __float_as_uint(b), tag}), rs, vo, 0, SC1)
#define ST64(a, tag, rs, vo) __builtin_amdgcn_raw_buffer_store_b64((u32x2{__float_as_uint(a), tag}), rs, vo, 0, SC1)
#define LDS_LD(p) __hip_atomic_load(p, __ATOMIC_RELAXED, __HIP_MEMORY_SCOPE_WORKGROUP)
#define PACE(STEP) asm volatile("1: s_memrealtime s[90:91]\n s_waitcnt lgkmcnt(0)\n s_sub_u32 s90, s90, %0\n s_cmp_lt_i32 s90, 0\n s_cbranch_scc0 2f\n s_sleep 2\n s_branch 1b\n 2: s_add_u32 %0, %0, %1" : "+s"(dl) : "s"(stp) : "scc", "s90", "s91")
#define SB() __builtin_amdgcn_sched_barrier(0)
#define MEMFENCE() asm volatile("" ::: "memory")
#define STAMP(i) do {} while (0)
#define STAMPW(i, wv) do {} while (0)

template <int N, int SLEEP> __device__ __forceinline__ void poll128(u32x4 (&g)[N], rsrc_t rs, int off, int stride, unsigned tag) {
#pragma unroll
  for (int i = 0; i < N; ++i) g[i] = LD128(rs, off + i * stride, 0, SC1);
  _Pragma("nounroll") for (unsigned spins = 0; spins < SPIN_CAP; ++spins) {
    bool bad[N], any = false;
#pragma unroll
    for (int i = 0; i < N; ++i) { bad[i] = (g[i][1] != tag) | (g[i][3] != tag); any |= bad[i]; }
    if (!__any(any)) break;
    if (SLEEP) __builtin_amdgcn_s_sleep(SLEEP);
    MEMFENCE();
    u32x4 n[N];
#pragma unroll
    for (int i = 0; i < N; ++i) n[i] = LD128(rs, bad[i] ? off + i * stride : OOB, 0, SC1);
    SB();
#pragma unroll
    for (int i = 0; i < N; ++i) g[i] = bad[i] ? n[i] : g[i];
  }
}

struct Lds {
  float part[16][128];
  float ypart[16][64];
  float wmax[16];
  float wsum[16];
  unsigned qb[64];
  float own[128];
  unsigned tw[16];
  unsigned qready, cnt, tadj, pad_, tag;
};

__device__ __forceinline__ void ln_stats(const u32x4 xa, float& mb, float& rden) {
  float xv[8];
#pragma unroll
  for (int j = 0; j < 4; ++j) { xv[2 * j] = blo(xa[j]); xv[2 * j + 1] = bhi(xa[j]); }
  float sm = ((xv[0] + xv[1]) + (xv[2] + xv[3])) + ((xv[4] + xv[5]) + (xv[6] + xv[7]));
  const float mean = wave_sum(sm) * (1.f / D);
  float sq = 0.f;
#pragma unroll
  for (int j = 0; j < 8; ++j) { const float d = xv[j] - mean; sq = fmaf(d, d, sq); }
  const float var = rbf(wave_sum(sq) * (1.f / D));
  mb = rbf(mean);
  rden = __builtin_amdgcn_rcpf(rbf(__builtin_amdgcn_sqrtf(rbf(var + 1e-5f))));
}
__device__ __forceinline__ unsigned xn_pk(float xi, float mb, float rden) { const float v = rbf(xi - mb) * rden; return pkbf(v, v); }

__global__ void __launch_bounds__(1024) fused(const u16* __restrict__ kc, const u16* __restrict__ vc, const u16* __restrict__ x,
                                               const u16* __restrict__ wq, const u16* __restrict__ w1, unsigned char* ws, const u16* __restrict__ wo,
                                               const u16* __restrict__ w2, u16* __restrict__ out) {
  __shared__ __attribute__((aligned(16))) Lds L;
  const int b = blockIdx.x;
  const int h = (b & 7) * 4 + (b >> 6), s = (b >> 3) & 7;
  const int tid = threadIdx.x, lane = tid & 63, w = __builtin_amdgcn_readfirstlane(tid >> 6);
  typedef unsigned long long u64x8 __attribute__((ext_vector_type(8)));
  u64x8 ka; unsigned long long pout;
  asm volatile("s_load_dwordx16 %0, %2, 0x0\n s_load_dwordx2 %1, %2, 0x40\n"
               " s_mov_b64 exec, 0\n s_getpc_b64 s[90:91]\n s_lshl_b32 s92, %3, 2\n s_add_u32 s90, s90, s92\n s_addc_u32 s91, s91, 0\n s_add_u32 s90, s90, 24\n s_addc_u32 s91, s91, 0\n s_setpc_b64 s[90:91]\n"
               " s_branch fz_w0\n s_branch fz_w1\n s_branch fz_w2\n s_branch fz_w3\n s_branch fz_w4\n s_branch fz_w5\n s_branch fz_w6\n s_branch fz_w7\n"
               " s_branch fz_w8\n s_branch fz_w9\n s_branch fz_w10\n s_branch fz_w11\n s_branch fz_w12\n s_branch fz_w13\n s_branch fz_w14\n s_branch fz_w15\n"
               "fz_wret: s_mov_b64 exec, -1\n"
               " s_waitcnt lgkmcnt(0)"
               : "=&s"(ka), "=&s"(pout) : "s"(__builtin_amdgcn_kernarg_segment_ptr()), "s"(w) : "s90", "s91", "s92", "scc");
#define PAD(i) asm volatile("fz_w%c0: s_cbranch_execz fz_wret" :: "i"(i))
  const unsigned long long pk = ka[0], pv = ka[1], px = ka[2], pq = ka[3], p1 = ka[4], pws = ka[5], po = ka[6], p2 = ka[7];
  int kvs = h * (SK * 256) + (LAYB == 0 ? s * 16384 + w * 1024 : LAYB == 1 ? w * 8192 + s * 1024 : LAYB == 2 ? (w >> 2) * 32768 + s * 4096 + (w & 3) * 1024 : (w >> 1) * 16384 + s * 2048 + (w & 1) * 1024), kvv = lane * 16;
  const int rot = ROTX;
  const rsrc_t rk = mk_rsrc((void*)pk, 64u << 20);
  const rsrc_t rs = mk_rsrc((void*)pws, WS_END);
  const f32x4 z4 = {0.f, 0.f, 0.f, 0.f};
  unsigned dl, stp;
  unsigned te, tanc = 0;
#define ANCHOR() do { const unsigned sp_ = TREL - (15 - w) * 4 - (w < 2 ? 0 : (w - AW) * QOFF); te = tanc + sp_; stp = sp_ * (65536u * (100 + KEPS) / 100 / (31 - NK0)) >> 16; dl = tanc; } while (0)
#define REANCHOR(R) do { const unsigned tr_ = __builtin_amdgcn_readfirstlane(LDS_LD(&L.tadj)); te = tr_ ? tr_ + TREL - (15 - w) * 4 : te; const int span_ = (int)(te - dl); \
    stp = (unsigned)max(span_, 0) * (65536u / ((R) - 1)) >> 16; dl = te - ((R) - 1) * stp; } while (0)
#define LANE_VARS() int ln_ = lane; asm volatile("" : "+v"(ln_)); const int r = ln_ >> 4, c = ln_ & 15, r8 = ln_ >> 3, c8 = ln_ & 7; (void)r; (void)c; (void)r8; (void)c8;
#define MASK8() u32x4 m8; _Pragma("unroll") for (int i = 0; i < 4; ++i) m8[i] = ((c8 >> 1) == i) ? ((c8 & 1) ? 0xffff0000u : 0x0000ffffu) : 0u; const bool valid8 = (r >> 1) == (c >> 3); (void)valid8;
#define MASK16() u32x4 mk; _Pragma("unroll") for (int i = 0; i < 4; ++i) mk[i] = ((c >> 1) == i) ? ((c & 1) ? 0xffff0000u : 0x0000ffffu) : 0u;
#define TOFF(t) (kvs + (XORR ? (((t) ^ rot) & 15) : (((t) + rot) & 15)) * (16384 * GW))
#define LDK(t) kt[t] = LD128(rk, kvv, TOFF(t), KV_AUX)
  u32x4 kt[16], vt[16];
  const rsrc_t rv = mk_rsrc((void*)pv, 64u << 20);
  if (PRIO) __builtin_amdgcn_s_setprio(3);
  LDK(0); SB();
#pragma unroll
  for (int t = 1; t < NK0; ++t) { LDK(t); SB(); }
  PAD(7);
  if (w == 2 && lane < 20) L.tw[lane] = 0u;
  asm volatile("s_waitcnt lgkmcnt(0)\n s_barrier" ::: "memory");
  unsigned pfv;
#define PFTOUCH() do { unsigned long long pc; unsigned len; \
    asm volatile("s_getpc_b64 %0\n fz_pc: s_mov_b32 %1, fz_last-fz_pc\n s_max_i32 %1, %1, fz_last0-fz_pc\n s_max_i32 %1, %1, fz_last1-fz_pc" : "=s"(pc), "=s"(len) :: "scc"); \
    pfv = __builtin_amdgcn_raw_buffer_load_b32(mk_rsrc((void*)pc, len), lane * 64, (15 - w) * 4096, 0); \
    SB(); } while (0)
  if (!LATEPF) PFTOUCH();
  const int nch = 5 + (h < 12), ch0 = 5 * h + min(h, 12);
  unsigned stag = 0;
  if (__builtin_expect(w < 2, 0)) {
    PAD(0);
    const bool r0 = w == 0;
    int sa0 = s * (64 * 8192) + h * 256, sa1 = s * (64 * FF * 2) + ch0 * 16;
    asm volatile("" : "+s"(sa0), "+s"(sa1));
    const int sa = r0 ? sa0 : sa1, da = r0 ? 8192 : FF * 2;
    {
      unsigned long long ta0 = pv + kvs, ta1 = po + h * (DH * 1024), tc1 = p2 + ch0 * 8192;
      asm volatile("" : "+s"(ta0), "+s"(ta1), "+s"(tc1));
      asm volatile("s_load_dword s92, %0, 0x0\n s_load_dword s93, %1, 0x0\n s_load_dword s94, %2, 0x0\n s_load_dword s95, %3, 0x0"
                   :: "s"((r0 ? pq : p1) + sa), "s"(r0 ? pws : px), "s"(r0 ? ta0 : ta1), "s"(r0 ? pout : tc1) : "s92", "s93", "s94", "s95");
    }
    const rsrc_t rx = mk_rsrc((void*)px, 1024);
    const rsrc_t ra = mk_rsrc((void*)(r0 ? pq : p1), r0 ? 4u << 20 : 512 * FF * 2);
    LANE_VARS();
    const int rho = RHO;
    const int va = c < (r0 ? 16 : nch) ? r * da + c * 16 : OOB;
    asm volatile("s_waitcnt lgkmcnt(0)" ::: "memory");
    stag = __builtin_amdgcn_raw_buffer_load_b32(rs, WS_EPOCH, 0, SC1);
    const u32x4 xa = LD128(rx, kvv, 0, 0);
    const unsigned xe = __builtin_amdgcn_raw_buffer_load_b16(rx, (4 * ((c + rho) & 15) + r) * 2, s * 128, 0);
    u32x4 wv[16];
    SB();
    PAD(1);
#pragma unroll
    for (int i = 0; i < 16; ++i) { wv[i] = LD128(ra, va, sa + ((i + rho) & 15) * 4 * da, KV_AUX); SB(); if (i == 9) PAD(2); }
    SB();
    PAD(3);
    MASK16();
    int o_a = (r0 ? WS_Q + (h * 8 + s) * 1024 : WS_H + (h * 8 + s) * 512) + (4 * c + 2 * (r & 1) + (r >> 1)) * 16;
    o_a = (r0 || c < 6) ? o_a : OOB;
    asm volatile("" : "+v"(stag), "+v"(o_a), "+v"(mk));
    stag += 1u;
    float mb, rden; ln_stats(xa, mb, rden);
    unsigned xp = xn_pk(blo(xe), mb, rden);
    asm volatile("" : "+v"(xp));
    SB();
    PAD(4);
    f32x4 qa = z4;
#define QA(i) qa = mfma(diag(dppu<0x150 + (i)>(xp), mk), wv[i], qa);
    QA(0) QA(1) QA(2) QA(3) QA(4) QA(5)
    asm volatile("" : "+v"(qa)); SB(); PAD(5);
    QA(6) QA(7) QA(8) QA(9) QA(10) QA(11)
    asm volatile("" : "+v"(qa)); SB(); PAD(6);
    QA(12) QA(13) QA(14) QA(15)
#undef QA
    ST128(x32_sum2(qa[0], qa[2]), x32_sum2(qa[1], qa[3]), stag, rs, o_a);
    STAMPW(1, 0);
    {
      const unsigned now = (unsigned)__builtin_amdgcn_s_memrealtime(), tw = LDS_LD(&L.tw[c]);
      const float n = row_sum(tw ? 1.f : 0.f), d = row_sum(tw ? (float)(int)(now - tw) : 0.f);
      tanc = now - __builtin_amdgcn_readfirstlane((unsigned)(d * __builtin_amdgcn_rcpf(fmaxf(n, 1.f))));
      __builtin_amdgcn_raw_buffer_store_b64(u32x2{tanc, stag}, rs, ((b >> 3) == LEAD && w == 1 && lane < 32) ? WS_T + lane * 128 + (b & 7) * 8 : OOB, 0, SC1);
    }
  }
  PAD(8);
  asm volatile("" :: "v"(kt[0]));
  if (w >= 2) {
    tanc = (unsigned)__builtin_amdgcn_s_memrealtime() | 1u;
    L.tw[w] = tanc;
  }
  ANCHOR();
  STAMPW(15, 9); STAMPW(14, 2);
  asm volatile("" : "+s"(kvs), "+v"(kvv));
#pragma unroll
  for (int t = NK0; t < TB; ++t) { if (((t - NK0) & 1) == 0) PAD(9 + (t - NK0) / 2); PACE(DK); LDK(t); SB(); }
  if (__builtin_expect(w < 2, 0)) {
    LANE_VARS(); MASK8();
    const unsigned tag = stag;
    if (w == 0) {
      u32x4 g[8];
      _Pragma("nounroll") for (unsigned spins = 0; spins < SPIN_CAP; ++spins) {
        bool ok = true;
#pragma unroll
        for (int i = 0; i < 8; ++i) g[i] = LD128(rs, WS_Q + h * 8192 + kvv + i * 1024, 0, SC1);
        SB();
#pragma unroll
        for (int i = 0; i < 8; ++i) ok &= (g[i][1] == tag) & (g[i][3] == tag);
        if (__all(ok)) break;
        asm volatile("s_sleep 8" ::: "memory");
      }
      float q0 = 0.f, q1 = 0.f;
#pragma unroll
      for (int i = 0; i < 8; ++i) { q0 += __uint_as_float(g[i][0]); q1 += __uint_as_float(g[i][2]); }
      L.qb[lane] = pkbf(q0, q1);
      if (lane == 0) L.tag = tag;
      __hip_atomic_store(&L.qready, 1u, __ATOMIC_RELEASE, __HIP_MEMORY_SCOPE_WORKGROUP);
      STAMP(2);
      asm volatile("fz_last0:");
    } else {
      const rsrc_t r2 = mk_rsrc((void*)p2, (ch0 + nch) * 8192);
      u32x4 wv[6];
#pragma unroll
      for (int i = 0; i < 6; ++i) { wv[i] = LD128(r2, r8 * 1024 + c8 * 16, (ch0 + i) * 8192 + s * 128, KV_AUX); SB(); }
      u32x2 g[6], gt;
      _Pragma("nounroll") for (unsigned spins = 0; spins < SPIN_CAP; ++spins) {
        gt = LD64(rs, WS_T + h * 128 + c8 * 8, 0, SC1);
#pragma unroll
        for (int i = 0; i < 6; ++i) g[i] = LD64(rs, WS_H + h * 4096 + c8 * 512 + r8 * 8 + i * 64, 0, SC1);
        SB();
        bool ok = gt[1] == tag;
#pragma unroll
        for (int i = 0; i < 6; ++i) ok &= g[i][1] == tag;
        if (__all(ok)) break;
        asm volatile("s_sleep 8" ::: "memory");
      }
      L.tadj = (tanc + (int)fminf(fmaxf(oct_sum((float)(int)(gt[0] - tanc)) * 0.125f, -300.f), 300.f)) | 1u;
      f32x4 za = z4;
#pragma unroll
      for (int i = 0; i < 6; ++i) {
        float hv = silu(rbf(oct_sum(__uint_as_float(g[i][0]))));
        hv = i < nch ? hv : 0.f;
        za = mfma(diag(pkbf(hv, hv), m8), wv[i], za);
      }
#pragma unroll
      for (int j = 0; j < 4; ++j) { float v = valid8 ? za[j] : 0.f; za[j] = v + dppf<ROR8>(v); }
      ST128(x32_sum2(za[0], za[2]), x32_sum2(za[1], za[3]), tag, rs, c < 8 ? WS_Z + (s * 32 + h) * 512 + (4 * c + 2 * (r & 1) + (r >> 1)) * 16 : OOB);
      STAMP(10);
      asm volatile("fz_last1:");
    }
  }

  asm volatile("" : "+s"(kvs), "+v"(kvv));
#pragma unroll
  for (int t = TB; t < 16; ++t) { if (((t - NK0) & 1) == 0) PAD(9 + (t - NK0) / 2); PACE(DK); LDK(t); SB(); }
  asm volatile("" : "+s"(kvs), "+v"(kvv));
#define LDV(t) do { PACE(DV); vt[t] = LD128(rv, kvv, TOFF(t), KV_AUX); SB(); } while (0)
  if (PRIO) __builtin_amdgcn_s_setprio(2);
  if (GT) REANCHOR(16);
  LDV(0); LDV(1); LDV(2); LDV(3);
  _Pragma("nounroll") for (unsigned spins = 0; LDS_LD(&L.qready) == 0u && spins < (SPIN_CAP << 6); ++spins) __builtin_amdgcn_s_sleep(2);
  MEMFENCE();
  STAMPW(3, 2);
  const u32x4 qp = *(const u32x4*)&L.qb[4 * (lane & 15)];
  const unsigned tag = L.tag;
  float xs[16];
#pragma unroll
  for (int t = 0; t < 16; ++t) {
    float d = dot2(kt[t][0], qp[0], 0.f);
    d = dot2(kt[t][1], qp[1], d); d = dot2(kt[t][2], qp[2], d); d = dot2(kt[t][3], qp[3], d);
    xs[t] = d;
  }
  asm volatile("" :: "v"(pfv));
  LANE_VARS();
#define TR_STEP(HALF, CTRL, BIT) _Pragma("unroll") for (int i = 0; i < (HALF); ++i) { \
    const float t1 = xs[i] + dppf<CTRL>(xs[i]), t2 = xs[i + (HALF)] + dppf<CTRL>(xs[i + (HALF)]); xs[i] = (c & (BIT)) ? t2 : t1; }
  TR_STEP(8, ROR8, 8) TR_STEP(4, XOR7, 4) TR_STEP(2, XOR2, 2) TR_STEP(1, XOR1, 1)
#undef TR_STEP
  asm volatile("" : "+v"(xs[0]));
  if (GT) REANCHOR(12);
  LDV(4);
  const float sc = rbf(rbf(xs[0]) * 0.08838834764831845f) * LOG2E;
  const float wm = wave_max(sc);
  const float p = __builtin_amdgcn_exp2f(sc - wm);
  {
    const float wsu = wave_sum(p);
    unsigned old = 0;
    if (lane == 0) {
      L.wmax[w] = wm; L.wsum[w] = wsu;
      old = __hip_atomic_fetch_add(&L.cnt, 1u, __ATOMIC_ACQ_REL, __HIP_MEMORY_SCOPE_WORKGROUP);
    }
    if (__builtin_amdgcn_readfirstlane(old) == 15u) {
      const float wmc = L.wmax[c];
      const float M = row_max(wmc);
      const float lse = M + __builtin_amdgcn_logf(row_sum(L.wsum[c] * __builtin_amdgcn_exp2f(wmc - M)));
      ST128(lse, lse, tag, rs, lane == 0 ? WS_F + h * 128 + s * 16 : OOB);
      STAMP(4);
    }
  }
  unsigned pp = pkbf(p, p);
  asm volatile("" : "+v"(pp));
  if (PRIO) __builtin_amdgcn_s_setprio(1);
  LDV(5);
  MASK16();
  MASK8();
  const int wc = w & 7;
  const bool fin = w == 5 && (h & 7) == s;
  int a_part = w * 512 + (8 * c + 4 * r) * 4;
  int a_mrg = r * 2048 + (32 * w + 2 * c) * 4;
  int a_od = (32 * w + 2 * c + (r >> 1)) * 4;
  int a_yw = (2 * wc + (r >> 1)) * 256 + (8 * c8 + 4 * (r & 1)) * 4;
  int a_yr = r8 * 512 + (16 * w + 2 * c8) * 4;
  int a_row = (16 * wc + 2 * r8) * 4;
  int o_o = c8 == s ? OOB : WS_O + h * 8192 + c8 * 1024 + wc * 128 + r8 * 16;
  int o_pub = (r & 1) ? OOB : WS_O + (h * 8 + s) * 1024 + a_od * 2;
  int o_y = (lane & 24) == 0 ? WS_Y + (s * 32 + h) * 512 + w * 128 + c8 * 16 + (r >> 1) * 8 : OOB;
  int o_fin = s * 16384 + (h >> 3) * 128 + r8 * 2048 + c8 * 16;
  int o_out = (lane & 24) == 0 ? 128 * s + 32 * (h >> 3) + 4 * c8 + 2 * (r >> 1) : OOB;
  int o_wo = r8 * 2048 + c8 * 16;
  int o_f = w < 4 ? WS_F + h * 128 + c8 * 16 : fin ? WS_Z + o_fin : w != 7 ? OOB
          : lane < 8 ? WS_O + (h * 8 + s) * 1024 + lane * 128 : lane < 12 ? WS_Y + (s * 32 + h) * 512 + (lane - 8) * 128 : OOB;
  asm volatile("" : "+v"(a_part), "+v"(a_mrg), "+v"(a_od), "+v"(a_yw), "+v"(a_yr), "+v"(a_row), "+v"(o_o), "+v"(o_pub), "+v"(o_y), "+v"(o_fin), "+v"(o_out), "+v"(o_wo), "+v"(o_f));
  asm volatile("" : "+v"(m8[0]), "+v"(m8[1]), "+v"(m8[2]), "+v"(m8[3]), "+v"(mk[0]), "+v"(mk[1]), "+v"(mk[2]), "+v"(mk[3]));
  const bool me = c8 == s;
  char* const lds = (char*)&L;
  f32x4 oa = z4;
#define PV(T) do { oa = mfma(diag(dppu<0x150 + (T)>(pp), mk), vt[T], oa); asm volatile("" : "+v"(oa)); } while (0)
  LDV(6); PV(0);
  LDV(7); PV(1);
  LDV(8); PV(2);
  if (w == 6) {
    unsigned long long sv;
    asm volatile("s_mov_b64 %0, exec\n s_mov_b64 exec, 0\n s_branch fz_hop0\n fz_ret:\n s_mov_b64 exec, %0" : "=s"(sv) :: "memory");
  }
#define HOP(i, nx) asm volatile("fz_hop" #i ": s_cbranch_execz " nx)
  LDV(9); PV(3);
  u32x4 gz[4];
  gz[0] = LD128(rs, o_f, 0, SC1);
  {
    const rsrc_t rz = mk_rsrc((void*)pws, fin ? WS_END : 0);
#pragma unroll
    for (int i = 1; i < 4; ++i) gz[i] = LD128(rz, o_f + i * 512, 0, SC1);
  }
  const unsigned xo = __builtin_amdgcn_raw_buffer_load_b16(mk_rsrc((void*)px, fin ? 1024 : 0), o_out, 0, 0);
  SB();
  LDV(10); PV(4);
  if (PRIO) __builtin_amdgcn_s_setprio(0);
  LDV(11); PV(5);
  LDV(12); PV(6);
  const bool bad1 = w < 4 && gz[0][1] != tag;
  const u32x2 gf2 = LD64(rs, bad1 ? o_f : OOB, 0, SC1);
  SB();
  LDV(13); PV(7);
  SB();
  LDV(14);
  LDV(15);
  PV(8); PV(9); PV(10); PV(11); PV(12); PV(13);
  if (w >= 8) { dl = te + (15 - w) * 4 + WOD; PACE(0); }
  const rsrc_t rwo = mk_rsrc((void*)po, (w >= 8 && !NOWO) ? 4u << 20 : 0);
  const u32x4 wov0 = LD128(rwo, o_wo, (h * DH + 16 * wc) * 1024 + s * 128, KV_AUX);
  const u32x4 wov1 = LD128(rwo, o_wo + 1024, (h * DH + 16 * wc) * 1024 + s * 128, KV_AUX);
  SB();
  float g0 = 0.f, g1 = 0.f;
  f32x4 fw = z4;
  if (w < 4) {
    u32x2 gf = bad1 ? gf2 : u32x2{gz[0][0], gz[0][1]};
    if (__any(gf[1] != tag)) {
      _Pragma("nounroll") for (unsigned spins = 0; spins < SPIN_CAP; ++spins) {
        MEMFENCE();
        gf = LD64(rs, o_f, 0, SC1);
        if (!__any(gf[1] != tag)) break;
        __builtin_amdgcn_s_sleep(4);
      }
    }
    MEMFENCE();
    fw = *(const f32x4*)&L.wmax[4 * r];
    const float ls = __uint_as_float(gf[0]);
    const float MM = oct_max(ls);
    const float lh = MM + __builtin_amdgcn_logf(oct_sum(__builtin_amdgcn_exp2f(ls - MM)));
#pragma unroll
    for (int i = 0; i < 4; ++i) fw[i] = __builtin_amdgcn_exp2f(fw[i] - lh);
    STAMPW(13, 0);
  } else if (fin) {
    bool bad = false;
#pragma unroll
    for (int i = 0; i < 4; ++i) bad |= (gz[i][1] != tag) | (gz[i][3] != tag);
    _Pragma("nounroll") for (unsigned spins = 0; __any(bad) && spins < SPIN_CAP; ++spins) {
      __builtin_amdgcn_s_sleep(4);
      MEMFENCE();
      bad = false;
#pragma unroll
      for (int i = 0; i < 4; ++i) gz[i] = LD128(rs, o_f + i * 512, 0, SC1);
#pragma unroll
      for (int i = 0; i < 4; ++i) bad |= (gz[i][1] != tag) | (gz[i][3] != tag);
    }
    float v0 = (__uint_as_float(gz[0][0]) + __uint_as_float(gz[1][0])) + (__uint_as_float(gz[2][0]) + __uint_as_float(gz[3][0]));
    float v1 = (__uint_as_float(gz[0][2]) + __uint_as_float(gz[1][2])) + (__uint_as_float(gz[2][2]) + __uint_as_float(gz[3][2]));
    g0 = rbf(silu(rbf(col_sum(v0, v1))));
    g1 = blo(xo);
    o_fin += WS_Y;
    asm volatile("" : "+v"(g0), "+v"(g1), "+v"(o_fin));
  }
  asm volatile("" :: "v"(gz[0]));
  PV(14); PV(15);
#undef PV
  if (lane < 32) *(f32x4*)(lds + offsetof(Lds, part) + a_part) = oa;
  STAMPW(5, 15);
  asm volatile("s_waitcnt lgkmcnt(0)\n s_barrier" ::: "memory");
  HOP(0, "fz_hop1");
  STAMPW(6, 6);
  u32x4 go;
  if (w < 4) {
    const float* src = (const float*)(lds + offsetof(Lds, part) + a_mrg);
    const f32x2 v0 = *(const f32x2*)src, v1 = *(const f32x2*)(src + 128), v2 = *(const f32x2*)(src + 256), v3 = *(const f32x2*)(src + 384);
    const f32x2 av = (fw[0] * v0 + fw[1] * v1) + (fw[2] * v2 + fw[3] * v3);
    const float a = x16_sum(x32_sum2(av[0], av[1]));
    ST64(a, tag, rs, o_pub);
    *(float*)(lds + offsetof(Lds, own) + a_od) = a;
    STAMPW(12, 0);
  }
  asm volatile("s_waitcnt lgkmcnt(0)\n s_barrier" ::: "memory");
  HOP(1, "fz_hop2");
  if (w >= 8) {
    f32x2 ownv = *(const f32x2*)(lds + offsetof(Lds, own) + a_row);
    asm volatile("" : "+v"(ownv));
    if (DL1) __builtin_amdgcn_s_sleep(DL1);
    go = LD128(rs, o_o, 0, SC1);
    go = me ? u32x4{__float_as_uint(ownv[0]), tag, __float_as_uint(ownv[1]), tag} : go;
    _Pragma("nounroll") for (unsigned spins = 0; spins < SPIN_CAP; ++spins) {
      const bool bad = (go[1] != tag) | (go[3] != tag);
      if (!__any(bad)) break;
      MEMFENCE();
      if (bad) go = LD128(rs, o_o, 0, SC1);
    }
    HOP(2, "fz_hop3");
    STAMPW(7, 8);
    const float o0 = oct_sum(__uint_as_float(go[0])), o1 = oct_sum(__uint_as_float(go[2]));
    f32x4 y = mfma(diag(pkbf(o0, o0), m8), wov0, z4);
    y = mfma(diag(pkbf(o1, o1), m8), wov1, y);
    if (valid8) *(f32x4*)(lds + offsetof(Lds, ypart) + a_yw) = y;
  }
  asm volatile("s_waitcnt lgkmcnt(0)\n s_barrier" ::: "memory");
  HOP(3, "fz_hop4");
  STAMPW(8, 6);
  if (w < 4) {
    HOP(4, "fz_hop5");
    const float* src = (const float*)(lds + offsetof(Lds, ypart) + a_yr);
    f32x2 a = *(const f32x2*)src + *(const f32x2*)(src + 64);
    ST64(col_sum(a[0], a[1]), tag, rs, o_y);
  } else if (fin) {
    HOP(5, "fz_hop6");
    if (DL2) __builtin_amdgcn_s_sleep(DL2);
    float va, vb, vc2, vd;
    typedef unsigned u32x16 __attribute__((ext_vector_type(16)));
    u32x16 keepA, keepB;
    unsigned cnt_;
    asm volatile("buffer_load_dwordx4 v[64:67], %[off], %[rs], 0 offen offset:0 sc1\n buffer_load_dwordx4 v[68:71], %[off], %[rs], 0 offen offset:512 sc1\n buffer_load_dwordx4 v[72:75], %[off], %[rs], 0 offen offset:1024 sc1\n buffer_load_dwordx4 v[76:79], %[off], %[rs], 0 offen offset:1536 sc1\n s_sleep %[p2]\n s_mov_b32 %[cnt], 262144\n"
      "1: buffer_load_dwordx4 v[80:83], %[off], %[rs], 0 offen offset:0 sc1\n buffer_load_dwordx4 v[84:87], %[off], %[rs], 0 offen offset:512 sc1\n buffer_load_dwordx4 v[88:91], %[off], %[rs], 0 offen offset:1024 sc1\n buffer_load_dwordx4 v[92:95], %[off], %[rs], 0 offen offset:1536 sc1\n s_waitcnt vmcnt(4)\n v_cmpx_eq_u32 v65, %[tag]\n v_cmpx_eq_u32 v67, %[tag]\n v_cmpx_eq_u32 v69, %[tag]\n v_cmpx_eq_u32 v71, %[tag]\n v_cmpx_eq_u32 v73, %[tag]\n v_cmpx_eq_u32 v75, %[tag]\n v_cmpx_eq_u32 v77, %[tag]\n v_cmpx_eq_u32 v79, %[tag]\n s_cmp_eq_u64 exec, -1\n s_mov_b64 exec, -1\n s_cbranch_scc1 2f\n"
      "buffer_load_dwordx4 v[64:67], %[off], %[rs], 0 offen offset:0 sc1\n buffer_load_dwordx4 v[68:71], %[off], %[rs], 0 offen offset:512 sc1\n buffer_load_dwordx4 v[72:75], %[off], %[rs], 0 offen offset:1024 sc1\n buffer_load_dwordx4 v[76:79], %[off], %[rs], 0 offen offset:1536 sc1\n s_waitcnt vmcnt(4)\n v_cmpx_eq_u32 v81, %[tag]\n v_cmpx_eq_u32 v83, %[tag]\n v_cmpx_eq_u32 v85, %[tag]\n v_cmpx_eq_u32 v87, %[tag]\n v_cmpx_eq_u32 v89, %[tag]\n v_cmpx_eq_u32 v91, %[tag]\n v_cmpx_eq_u32 v93, %[tag]\n v_cmpx_eq_u32 v95, %[tag]\n s_cmp_eq_u64 exec, -1\n s_mov_b64 exec, -1\n s_cbranch_scc1 3f\n"
      "s_sub_u32 %[cnt], %[cnt], 1\n s_cmp_lg_u32 %[cnt], 0\n s_cbranch_scc1 1b\n"
      "3: v_add_f32 %[a], v80, v84\n v_add_f32 %[b], v82, v86\n v_add_f32 %[c], v88, v92\n v_add_f32 %[d], v90, v94\n s_branch 4f\n"
      "2: v_add_f32 %[a], v64, v68\n v_add_f32 %[b], v66, v70\n v_add_f32 %[c], v72, v76\n v_add_f32 %[d], v74, v78\n 4:"
      : [a] "=&v"(va), [b] "=&v"(vb), [c] "=&v"(vc2), [d] "=&v"(vd), [cnt] "=&s"(cnt_), "=&{v[64:79]}"(keepA), "=&{v[80:95]}"(keepB)
      : [off] "v"(o_fin), [rs] "s"(rs), [tag] "v"(tag), [p2] "i"(P2) : "scc", "vcc", "memory");
    HOP(6, "fz_hop7");
    STAMP(9);
    const float ov = g1 + rbf(rbf(col_sum(va + vc2, vb + vd)) * g0);
    __builtin_amdgcn_raw_buffer_store_b16((u16)pkbf(ov, ov), mk_rsrc((void*)pout, 1024), o_out, 0, OUT_AUX);
    HOP(7, "fz_ret");
    asm volatile("fz_last:");
    if (b == 0 && lane == 0) __builtin_amdgcn_raw_buffer_store_b32(tag, rs, WS_EPOCH, 0, SC1);
    STAMP(11);
    asm volatile("" :: "v"(keepA), "v"(keepB));
  }
}

extern "C" void kernel_launch(void* const* d_in, const int* in_sizes, int n_in, void* d_out, int out_size, void* d_ws, size_t ws_size, hipStream_t stream) {
  fused<<<256, 1024, 0, stream>>>((const u16*)d_in[1], (const u16*)d_in[2], (const u16*)d_in[0], (const u16*)d_in[3], (const u16*)d_in[6],
                                  (unsigned char*)d_ws, (const u16*)d_in[4], (const u16*)d_in[7], (u16*)d_out);
}
```

```cpp
#include <hip/hip_runtime.h>
#include <stdint.h>
#include <stddef.h>

typedef unsigned short u16;
typedef unsigned u32x4 __attribute__((ext_vector_type(4)));
typedef unsigned u32x2 __attribute__((ext_vector_type(2)));
typedef float f32x4 __attribute__((ext_vector_type(4)));
typedef float f32x2 __attribute__((ext_vector_type(2)));
typedef __bf16 bf16x8 __attribute__((ext_vector_type(8)));
typedef __bf16 bf16x2 __attribute__((ext_vector_type(2)));
typedef __amdgpu_buffer_rsrc_t rsrc_t;
#define GAS __attribute__((address_space(1)))

#define KV_AUX 2
#define STAMPS 1
#define PROBE_K 0
#define PROBE_M 1
#define PROBE_G 10
#define QSLEEP 16
#define HSLEEP 32
#define HOPS 1
#define DK 63
#define DV 63
#define D0 100
#define DL1 8
#define DL2 12
#define P2 5
#define TOUCH 1
#define PRIO 1
#define ANCH 1
#define D1 0
#define STG 0
#define TREL 1820
#define WOD 30
#define GT 1
#define NK0 2
#define LATEPF 0
#define ROT 3
#define COND_STREAM __builtin_expect(w >= 2, 1)
#define TLD 11
#define OUT_AUX 0
#define LOOK 9
#define NOWO 0
#define TB 12
#define AW 8
#define QOFF 0
#define WARM 1
#define WA "fz_wret"
#define LEAD 17
#define KEPS 5
#define TWAIT ""
#define GW 8
#define PAIR 0
#define ROTX h
#define XORR 0
#define RHO 0
#define LAYB 0
#define NB 2
#define PF_BYTES 8192

constexpr int D = 512, SK = 8192, DH = 128, FF = 1376;
constexpr float LOG2E = 1.4426950408889634f;
constexpr int SC1 = 16;
constexpr int OOB = 0x40000000;
constexpr unsigned WS_EPOCH = 0;
constexpr unsigned WS_T = 4096;
constexpr unsigned WS_Q = 8192;
constexpr unsigned WS_H = WS_Q + 256 * 1024;
constexpr unsigned WS_F = WS_H + 256 * 512;
constexpr unsigned WS_O = WS_F + 32 * 128;
constexpr unsigned WS_Y = WS_O + 256 * 1024;
constexpr unsigned WS_Z = WS_Y + 256 * 512;
constexpr unsigned WS_END = WS_Z + 256 * 512;
constexpr unsigned WS_STAMP = 2u << 20;
constexpr unsigned SPIN_CAP = 1u << 18;

__device__ __forceinline__ float rbf(float f) { return (float)(__bf16)f; }
__device__ __forceinline__ float blo(unsigned u) { return __uint_as_float(u << 16); }
__device__ __forceinline__ float bhi(unsigned u) { return __uint_as_float(u & 0xffff0000u); }
__device__ __forceinline__ unsigned pkbf(float a, float b) { bf16x2 v = {(__bf16)a, (__bf16)b}; return __builtin_bit_cast(unsigned, v); }
__device__ __forceinline__ float dot2(unsigned a, unsigned b, float acc) {
  return __builtin_amdgcn_fdot2_f32_bf16(__builtin_bit_cast(bf16x2, a), __builtin_bit_cast(bf16x2, b), acc, false);
}
template <int CTRL> __device__ __forceinline__ float dppf(float x) {
  return __builtin_bit_cast(float, __builtin_amdgcn_mov_dpp(__builtin_bit_cast(int, x), CTRL, 0xf, 0xf, true));
}
template <int CTRL> __device__ __forceinline__ unsigned dppu(unsigned x) {
  return (unsigned)__builtin_amdgcn_mov_dpp((int)x, CTRL, 0xf, 0xf, true);
}
constexpr int XOR1 = 0xB1, XOR2 = 0x4E, XOR7 = 0x141, ROR8 = 0x128, ROR4 = 0x124, ROR2 = 0x122;
__device__ __forceinline__ float x16_sum(float x) {
  auto s = __builtin_amdgcn_permlane16_swap(__float_as_uint(x), __float_as_uint(x), false, false);
  return __uint_as_float(s[0]) + __uint_as_float(s[1]);
}
__device__ __forceinline__ float x32_sum2(float a, float b) {
  auto t = __builtin_amdgcn_permlane32_swap(__float_as_uint(a), __float_as_uint(b), false, false);
  return __uint_as_float(t[0]) + __uint_as_float(t[1]);
}
__device__ __forceinline__ float x32_sum(float x) { return x32_sum2(x, x); }
__device__ __forceinline__ float xrow_sum(float x) { return x32_sum(x16_sum(x)); }
__device__ __forceinline__ float fmx(float a, float b) { return __builtin_amdgcn_fmed3f(a, b, __builtin_inff()); }
__device__ __forceinline__ float xrow_max(float x) {
  auto s = __builtin_amdgcn_permlane16_swap(__float_as_uint(x), __float_as_uint(x), false, false);
  x = fmx(__uint_as_float(s[0]), __uint_as_float(s[1]));
  auto t = __builtin_amdgcn_permlane32_swap(__float_as_uint(x), __float_as_uint(x), false, false);
  return fmx(__uint_as_float(t[0]), __uint_as_float(t[1]));
}
__device__ __forceinline__ float oct_sum(float x) { x += dppf<XOR1>(x); x += dppf<XOR2>(x); x += dppf<XOR7>(x); return x; }
__device__ __forceinline__ float oct_max(float x) { x = fmx(x, dppf<XOR1>(x)); x = fmx(x, dppf<XOR2>(x)); x = fmx(x, dppf<XOR7>(x)); return x; }
__device__ __forceinline__ float row_sum(float x) { x = oct_sum(x); x += dppf<ROR8>(x); return x; }
__device__ __forceinline__ float row_max(float x) { x = oct_max(x); x = fmx(x, dppf<ROR8>(x)); return x; }
__device__ __forceinline__ float col_sum(float a, float b) { float v = x16_sum(x32_sum2(a, b)); return v + dppf<ROR8>(v); }
__device__ __forceinline__ float wave_sum(float x) { return xrow_sum(row_sum(x)); }
__device__ __forceinline__ float wave_max(float x) { return xrow_max(row_max(x)); }
__device__ __forceinline__ f32x4 mfma(u32x4 a, u32x4 b, f32x4 c) {
  return __builtin_amdgcn_mfma_f32_16x16x32_bf16(__builtin_bit_cast(bf16x8, a), __builtin_bit_cast(bf16x8, b), c, 0, 0, 0);
}
__device__ __forceinline__ u32x4 diag(unsigned pk, const u32x4& m) { return u32x4{pk & m[0], pk & m[1], pk & m[2], pk & m[3]}; }
__device__ __forceinline__ float silu(float z) { return z * __builtin_amdgcn_rcpf(1.f + __builtin_amdgcn_exp2f(-z * LOG2E)); }
__device__ __forceinline__ rsrc_t mk_rsrc(const void* p, unsigned bytes) { return __builtin_amdgcn_make_buffer_rsrc((void*)p, 0, bytes, 0x00020000); }
#define LD128(rs, vo, so, aux) __builtin_amdgcn_raw_buffer_load_b128(rs, vo, so, aux)
#define LD64(rs, vo, so, aux) __builtin_amdgcn_raw_buffer_load_b64(rs, vo, so, aux)
#define ST128(a, b, tag, rs, vo) __builtin_amdgcn_raw_buffer_store_b128((u32x4{__float_as_uint(a), tag, __float_as_uint(b), tag}), rs, vo, 0, SC1)
#define ST64(a, tag, rs, vo) __builtin_amdgcn_raw_buffer_store_b64((u32x2{__float_as_uint(a), tag}), rs, vo, 0, SC1)
#define LDS_LD(p) __hip_atomic_load(p, __ATOMIC_RELAXED, __HIP_MEMORY_SCOPE_WORKGROUP)
#define PACE(STEP) asm volatile("1: s_memrealtime s[90:91]\n s_waitcnt lgkmcnt(0)\n s_sub_u32 s90, s90, %0\n s_cmp_lt_i32 s90, 0\n s_cbranch_scc0 2f\n s_sleep 2\n s_branch 1b\n 2: s_add_u32 %0, %0, %1" : "+s"(dl) : "s"(stp) : "scc", "s90", "s91")
#define SB() __builtin_amdgcn_sched_barrier(0)
#define MEMFENCE() asm volatile("" ::: "memory")
#define STAMPC(i, cond) do { unsigned long long t_ = __builtin_amdgcn_s_memrealtime(); \
    __builtin_amdgcn_raw_buffer_store_b64(u32x2{(unsigned)t_, (unsigned)(t_ >> 32)}, mk_rsrc((void*)(pws + WS_STAMP), 32768), (cond) ? blockIdx.x * 128 + (i) * 8 : OOB, 0, SC1); } while (0)
#define STAMP(i) do { if ((i) == PROBE_K) STAMPC(i, lane == 0); } while (0)
#define STAMPW(i, wv) do { if (w == (wv)) STAMP(i); } while (0)

template <int N, int SLEEP> __device__ __forceinline__ void poll128(u32x4 (&g)[N], rsrc_t rs, int off, int stride, unsigned tag) {
#pragma unroll
  for (int i = 0; i < N; ++i) g[i] = LD128(rs, off + i * stride, 0, SC1);
  _Pragma("nounroll") for (unsigned spins = 0; spins < SPIN_CAP; ++spins) {
    bool bad[N], any = false;
#pragma unroll
    for (int i = 0; i < N; ++i) { bad[i] = (g[i][1] != tag) | (g[i][3] != tag); any |= bad[i]; }
    if (!__any(any)) break;
    if (SLEEP) __builtin_amdgcn_s_sleep(SLEEP);
    MEMFENCE();
    u32x4 n[N];
#pragma unroll
    for (int i = 0; i < N; ++i) n[i] = LD128(rs, bad[i] ? off + i * stride : OOB, 0, SC1);
    SB();
#pragma unroll
    for (int i = 0; i < N; ++i) g[i] = bad[i] ? n[i] : g[i];
  }
}

struct Lds {
  float part[16][128];
  float ypart[16][64];
  float wmax[16];
  float wsum[16];
  unsigned qb[64];
  float own[128];
  unsigned tw[16];
  unsigned qready, cnt, tadj, pad_, tag;
};

__device__ __forceinline__ void ln_stats(const u32x4 xa, float& mb, float& rden) {
  float xv[8];
#pragma unroll
  for (int j = 0; j < 4; ++j) { xv[2 * j] = blo(xa[j]); xv[2 * j + 1] = bhi(xa[j]); }
  float sm = ((xv[0] + xv[1]) + (xv[2] + xv[3])) + ((xv[4] + xv[5]) + (xv[6] + xv[7]));
  const float mean = wave_sum(sm) * (1.f / D);
  float sq = 0.f;
#pragma unroll
  for (int j = 0; j < 8; ++j) { const float d = xv[j] - mean; sq = fmaf(d, d, sq); }
  const float var = rbf(wave_sum(sq) * (1.f / D));
  mb = rbf(mean);
  rden = __builtin_amdgcn_rcpf(rbf(__builtin_amdgcn_sqrtf(rbf(var + 1e-5f))));
}
__device__ __forceinline__ unsigned xn_pk(float xi, float mb, float rden) { const float v = rbf(xi - mb) * rden; return pkbf(v, v); }

__global__ void __launch_bounds__(1024) fused(const u16* __restrict__ kc, const u16* __restrict__ vc, const u16* __restrict__ x,
                                               const u16* __restrict__ wq, const u16* __restrict__ w1, unsigned char* ws, const u16* __restrict__ wo,
                                               const u16* __restrict__ w2, u16* __restrict__ out) {
  __shared__ __attribute__((aligned(16))) Lds L;
  unsigned long long tstart;
  asm volatile("s_memrealtime %0\n s_waitcnt lgkmcnt(0)" : "=s"(tstart));
  const int b = blockIdx.x;
  const int h = (b & 7) * 4 + (b >> 6), s = (b >> 3) & 7;
  const int tid = threadIdx.x, lane = tid & 63, w = __builtin_amdgcn_readfirstlane(tid >> 6);
  typedef unsigned long long u64x8 __attribute__((ext_vector_type(8)));
  u64x8 ka; unsigned long long pout;
  asm volatile("s_load_dwordx16 %0, %2, 0x0\n s_load_dwordx2 %1, %2, 0x40\n"
               " s_mov_b64 exec, 0\n s_getpc_b64 s[90:91]\n s_lshl_b32 s92, %3, 2\n s_add_u32 s90, s90, s92\n s_addc_u32 s91, s91, 0\n s_add_u32 s90, s90, 24\n s_addc_u32 s91, s91, 0\n s_setpc_b64 s[90:91]\n"
               " s_branch fz_w0\n s_branch fz_w1\n s_branch fz_w2\n s_branch fz_w3\n s_branch fz_w4\n s_branch fz_w5\n s_branch fz_w6\n s_branch fz_w7\n"
               " s_branch fz_w8\n s_branch fz_w9\n s_branch fz_w10\n s_branch fz_w11\n s_branch fz_w12\n s_branch fz_w13\n s_branch fz_w14\n s_branch fz_w15\n"
               "fz_wret: s_mov_b64 exec, -1\n"
               " s_waitcnt lgkmcnt(0)"
               : "=&s"(ka), "=&s"(pout) : "s"(__builtin_amdgcn_kernarg_segment_ptr()), "s"(w) : "s90", "s91", "s92", "scc");
#define PAD(i) asm volatile("fz_w%c0: s_cbranch_execz fz_wret" :: "i"(i))
  const unsigned long long pk = ka[0], pv = ka[1], px = ka[2], pq = ka[3], p1 = ka[4], pws = ka[5], po = ka[6], p2 = ka[7];
  int kvs = h * (SK * 256) + (LAYB == 0 ? s * 16384 + w * 1024 : LAYB == 1 ? w * 8192 + s * 1024 : LAYB == 2 ? (w >> 2) * 32768 + s * 4096 + (w & 3) * 1024 : (w >> 1) * 16384 + s * 2048 + (w & 1) * 1024), kvv = lane * 16;
  const int rot = ROTX;
  const rsrc_t rk = mk_rsrc((void*)pk, 64u << 20);
  const rsrc_t rs = mk_rsrc((void*)pws, WS_END);
  const f32x4 z4 = {0.f, 0.f, 0.f, 0.f};
  unsigned dl, stp;
  unsigned te, tanc = 0;
#define ANCHOR() do { const unsigned sp_ = TREL - (15 - w) * 4 - (w < 2 ? 0 : (w - AW) * QOFF); te = tanc + sp_; stp = sp_ * (65536u * (100 + KEPS) / 100 / (31 - NK0)) >> 16; dl = tanc; } while (0)
#define REANCHOR(R) do { const unsigned tr_ = __builtin_amdgcn_readfirstlane(LDS_LD(&L.tadj)); te = tr_ ? tr_ + TREL - (15 - w) * 4 : te; const int span_ = (int)(te - dl); \
    stp = (unsigned)max(span_, 0) * (65536u / ((R) - 1)) >> 16; dl = te - ((R) - 1) * stp; } while (0)
#define LANE_VARS() int ln_ = lane; asm volatile("" : "+v"(ln_)); const int r = ln_ >> 4, c = ln_ & 15, r8 = ln_ >> 3, c8 = ln_ & 7; (void)r; (void)c; (void)r8; (void)c8;
#define MASK8() u32x4 m8; _Pragma("unroll") for (int i = 0; i < 4; ++i) m8[i] = ((c8 >> 1) == i) ? ((c8 & 1) ? 0xffff0000u : 0x0000ffffu) : 0u; const bool valid8 = (r >> 1) == (c >> 3); (void)valid8;
#define MASK16() u32x4 mk; _Pragma("unroll") for (int i = 0; i < 4; ++i) mk[i] = ((c >> 1) == i) ? ((c & 1) ? 0xffff0000u : 0x0000ffffu) : 0u;
#define TOFF(t) (kvs + (XORR ? (((t) ^ rot) & 15) : (((t) + rot) & 15)) * (16384 * GW))
#define LDK(t) kt[t] = LD128(rk, kvv, TOFF(t), KV_AUX)
  u32x4 kt[16], vt[16];
  const rsrc_t rv = mk_rsrc((void*)pv, 64u << 20);
  if (PRIO) __builtin_amdgcn_s_setprio(3);
  LDK(0); SB();
#pragma unroll
  for (int t = 1; t < NK0; ++t) { LDK(t); SB(); }
  PAD(7);
  if (w == 2 && lane < 20) L.tw[lane] = 0u;
  asm volatile("s_waitcnt lgkmcnt(0)\n s_barrier" ::: "memory");
  unsigned pfv;
#define PFTOUCH() do { unsigned long long pc; unsigned len; \
    asm volatile("s_getpc_b64 %0\n fz_pc: s_mov_b32 %1, fz_last-fz_pc\n s_max_i32 %1, %1, fz_last0-fz_pc\n s_max_i32 %1, %1, fz_last1-fz_pc" : "=s"(pc), "=s"(len) :: "scc"); \
    pfv = __builtin_amdgcn_raw_buffer_load_b32(mk_rsrc((void*)pc, len), lane * 64, (15 - w) * 4096, 0); \
    SB(); } while (0)
  if (!LATEPF) PFTOUCH();
  const int nch = 5 + (h < 12), ch0 = 5 * h + min(h, 12);
  unsigned stag = 0;
  if (__builtin_expect(w < 2, 0)) {
    PAD(0);
    const bool r0 = w == 0;
    int sa0 = s * (64 * 8192) + h * 256, sa1 = s * (64 * FF * 2) + ch0 * 16;
    asm volatile("" : "+s"(sa0), "+s"(sa1));
    const int sa = r0 ? sa0 : sa1, da = r0 ? 8192 : FF * 2;
    {
      unsigned long long ta0 = pv + kvs, ta1 = po + h * (DH * 1024), tc1 = p2 + ch0 * 8192;
      asm volatile("" : "+s"(ta0), "+s"(ta1), "+s"(tc1));
      asm volatile("s_load_dword s92, %0, 0x0\n s_load_dword s93, %1, 0x0\n s_load_dword s94, %2, 0x0\n s_load_dword s95, %3, 0x0"
                   :: "s"((r0 ? pq : p1) + sa), "s"(r0 ? pws : px), "s"(r0 ? ta0 : ta1), "s"(r0 ? pout : tc1) : "s92", "s93", "s94", "s95");
    }
    const rsrc_t rx = mk_rsrc((void*)px, 1024);
    const rsrc_t ra = mk_rsrc((void*)(r0 ? pq : p1), r0 ? 4u << 20 : 512 * FF * 2);
    LANE_VARS();
    const int rho = RHO;
    const int va = c < (r0 ? 16 : nch) ? r * da + c * 16 : OOB;
    asm volatile("s_waitcnt lgkmcnt(0)" ::: "memory");
    stag = __builtin_amdgcn_raw_buffer_load_b32(rs, WS_EPOCH, 0, SC1);
    const u32x4 xa = LD128(rx, kvv, 0, 0);
    const unsigned xe = __builtin_amdgcn_raw_buffer_load_b16(rx, (4 * ((c + rho) & 15) + r) * 2, s * 128, 0);
    u32x4 wv[16];
    SB();
    PAD(1);
#pragma unroll
    for (int i = 0; i < 16; ++i) { wv[i] = LD128(ra, va, sa + ((i + rho) & 15) * 4 * da, KV_AUX); SB(); if (i == 9) PAD(2); }
    if (tid == 0) __hip_atomic_store((GAS unsigned long long*)(pws + WS_STAMP) + blockIdx.x * 16, tstart, __ATOMIC_RELAXED, __HIP_MEMORY_SCOPE_AGENT);
    SB();
    PAD(3);
    MASK16();
    int o_a = (r0 ? WS_Q + (h * 8 + s) * 1024 : WS_H + (h * 8 + s) * 512) + (4 * c + 2 * (r & 1) + (r >> 1)) * 16;
    o_a = (r0 || c < 6) ? o_a : OOB;
    asm volatile("" : "+v"(stag), "+v"(o_a), "+v"(mk));
    stag += 1u;
    float mb, rden; ln_stats(xa, mb, rden);
    unsigned xp = xn_pk(blo(xe), mb, rden);
    asm volatile("" : "+v"(xp));
    SB();
    PAD(4);
    f32x4 qa = z4;
#define QA(i) qa = mfma(diag(dppu<0x150 + (i)>(xp), mk), wv[i], qa);
    QA(0) QA(1) QA(2) QA(3) QA(4) QA(5)
    asm volatile("" : "+v"(qa)); SB(); PAD(5);
    QA(6) QA(7) QA(8) QA(9) QA(10) QA(11)
    asm volatile("" : "+v"(qa)); SB(); PAD(6);
    QA(12) QA(13) QA(14) QA(15)
#undef QA
    ST128(x32_sum2(qa[0], qa[2]), x32_sum2(qa[1], qa[3]), stag, rs, o_a);
    STAMPW(1, 0);
    {
      const unsigned now = (unsigned)__builtin_amdgcn_s_memrealtime(), tw = LDS_LD(&L.tw[c]);
      const float n = row_sum(tw ? 1.f : 0.f), d = row_sum(tw ? (float)(int)(now - tw) : 0.f);
      tanc = now - __builtin_amdgcn_readfirstlane((unsigned)(d * __builtin_amdgcn_rcpf(fmaxf(n, 1.f))));
      __builtin_amdgcn_raw_buffer_store_b64(u32x2{tanc, stag}, rs, ((b >> 3) == LEAD && w == 1 && lane < 32) ? WS_T + lane * 128 + (b & 7) * 8 : OOB, 0, SC1);
    }
  }
  PAD(8);
  asm volatile("" :: "v"(kt[0]));
  if (w >= 2) {
    tanc = (unsigned)__builtin_amdgcn_s_memrealtime() | 1u;
    L.tw[w] = tanc;
  }
  ANCHOR();
  STAMPW(15, 9); STAMPW(14, 2);
  asm volatile("" : "+s"(kvs), "+v"(kvv));
#pragma unroll
  for (int t = NK0; t < TB; ++t) { if (((t - NK0) & 1) == 0) PAD(9 + (t - NK0) / 2); PACE(DK); LDK(t); SB(); }
  if (__builtin_expect(w < 2, 0)) {
    LANE_VARS(); MASK8();
    const unsigned tag = stag;
    if (w == 0) {
      u32x4 g[8];
      _Pragma("nounroll") for (unsigned spins = 0; spins < SPIN_CAP; ++spins) {
        bool ok = true;
#pragma unroll
        for (int i = 0; i < 8; ++i) g[i] = LD128(rs, WS_Q + h * 8192 + kvv + i * 1024, 0, SC1);
        SB();
#pragma unroll
        for (int i = 0; i < 8; ++i) ok &= (g[i][1] == tag) & (g[i][3] == tag);
        if (__all(ok)) break;
        asm volatile("s_sleep 8" ::: "memory");
      }
      float q0 = 0.f, q1 = 0.f;
#pragma unroll
      for (int i = 0; i < 8; ++i) { q0 += __uint_as_float(g[i][0]); q1 += __uint_as_float(g[i][2]); }
      L.qb[lane] = pkbf(q0, q1);
      if (lane == 0) L.tag = tag;
      __hip_atomic_store(&L.qready, 1u, __ATOMIC_RELEASE, __HIP_MEMORY_SCOPE_WORKGROUP);
      STAMP(2);
      asm volatile("fz_last0:");
    } else {
      const rsrc_t r2 = mk_rsrc((void*)p2, (ch0 + nch) * 8192);
      u32x4 wv[6];
#pragma unroll
      for (int i = 0; i < 6; ++i) { wv[i] = LD128(r2, r8 * 1024 + c8 * 16, (ch0 + i) * 8192 + s * 128, KV_AUX); SB(); }
      u32x2 g[6], gt;
      _Pragma("nounroll") for (unsigned spins = 0; spins < SPIN_CAP; ++spins) {
        gt = LD64(rs, WS_T + h * 128 + c8 * 8, 0, SC1);
#pragma unroll
        for (int i = 0; i < 6; ++i) g[i] = LD64(rs, WS_H + h * 4096 + c8 * 512 + r8 * 8 + i * 64, 0, SC1);
        SB();
        bool ok = gt[1] == tag;
#pragma unroll
        for (int i = 0; i < 6; ++i) ok &= g[i][1] == tag;
        if (__all(ok)) break;
        asm volatile("s_sleep 8" ::: "memory");
      }
      L.tadj = (tanc + (int)fminf(fmaxf(oct_sum((float)(int)(gt[0] - tanc)) * 0.125f, -300.f), 300.f)) | 1u;
      f32x4 za = z4;
#pragma unroll
      for (int i = 0; i < 6; ++i) {
        float hv = silu(rbf(oct_sum(__uint_as_float(g[i][0]))));
        hv = i < nch ? hv : 0.f;
        za = mfma(diag(pkbf(hv, hv), m8), wv[i], za);
      }
#pragma unroll
      for (int j = 0; j < 4; ++j) { float v = valid8 ? za[j] : 0.f; za[j] = v + dppf<ROR8>(v); }
      ST128(x32_sum2(za[0], za[2]), x32_sum2(za[1], za[3]), tag, rs, c < 8 ? WS_Z + (s * 32 + h) * 512 + (4 * c + 2 * (r & 1) + (r >> 1)) * 16 : OOB);
      STAMP(10);
      asm volatile("fz_last1:");
    }
  }

  asm volatile("" : "+s"(kvs), "+v"(kvv));
#pragma unroll
  for (int t = TB; t < 16; ++t) { if (((t - NK0) & 1) == 0) PAD(9 + (t - NK0) / 2); PACE(DK); LDK(t); SB(); }
  asm volatile("" : "+s"(kvs), "+v"(kvv));
#define LDV(t) do { PACE(DV); vt[t] = LD128(rv, kvv, TOFF(t), KV_AUX); SB(); } while (0)
  if (PRIO) __builtin_amdgcn_s_setprio(2);
  if (GT) REANCHOR(16);
  LDV(0); LDV(1); LDV(2); LDV(3);
  _Pragma("nounroll") for (unsigned spins = 0; LDS_LD(&L.qready) == 0u && spins < (SPIN_CAP << 6); ++spins) __builtin_amdgcn_s_sleep(2);
  MEMFENCE();
  STAMPW(3, 2);
  const u32x4 qp = *(const u32x4*)&L.qb[4 * (lane & 15)];
  const unsigned tag = L.tag;
  float xs[16];
#pragma unroll
  for (int t = 0; t < 16; ++t) {
    float d = dot2(kt[t][0], qp[0], 0.f);
    d = dot2(kt[t][1], qp[1], d); d = dot2(kt[t][2], qp[2], d); d = dot2(kt[t][3], qp[3], d);
    xs[t] = d;
  }
  asm volatile("" :: "v"(pfv));
  LANE_VARS();
#define TR_STEP(HALF, CTRL, BIT) _Pragma("unroll") for (int i = 0; i < (HALF); ++i) { \
    const float t1 = xs[i] + dppf<CTRL>(xs[i]), t2 = xs[i + (HALF)] + dppf<CTRL>(xs[i + (HALF)]); xs[i] = (c & (BIT)) ? t2 : t1; }
  TR_STEP(8, ROR8, 8) TR_STEP(4, XOR7, 4) TR_STEP(2, XOR2, 2) TR_STEP(1, XOR1, 1)
#undef TR_STEP
  asm volatile("" : "+v"(xs[0]));
  if (GT) REANCHOR(12);
  LDV(4);
  const float sc = rbf(rbf(xs[0]) * 0.08838834764831845f) * LOG2E;
  const float wm = wave_max(sc);
  const float p = __builtin_amdgcn_exp2f(sc - wm);
  {
    const float wsu = wave_sum(p);
    unsigned old = 0;
    if (lane == 0) {
      L.wmax[w] = wm; L.wsum[w] = wsu;
      old = __hip_atomic_fetch_add(&L.cnt, 1u, __ATOMIC_ACQ_REL, __HIP_MEMORY_SCOPE_WORKGROUP);
    }
    if (__builtin_amdgcn_readfirstlane(old) == 15u) {
      const float wmc = L.wmax[c];
      const float M = row_max(wmc);
      const float lse = M + __builtin_amdgcn_logf(row_sum(L.wsum[c] * __builtin_amdgcn_exp2f(wmc - M)));
      ST128(lse, lse, tag, rs, lane == 0 ? WS_F + h * 128 + s * 16 : OOB);
      STAMP(4);
    }
  }
  unsigned pp = pkbf(p, p);
  asm volatile("" : "+v"(pp));
  if (PRIO) __builtin_amdgcn_s_setprio(1);
  LDV(5);
  MASK16();
  MASK8();
  const int wc = w & 7;
  const bool fin = w == 5 && (h & 7) == s;
  int a_part = w * 512 + (8 * c + 4 * r) * 4;
  int a_mrg = r * 2048 + (32 * w + 2 * c) * 4;
  int a_od = (32 * w + 2 * c + (r >> 1)) * 4;
  int a_yw = (2 * wc + (r >> 1)) * 256 + (8 * c8 + 4 * (r & 1)) * 4;
  int a_yr = r8 * 512 + (16 * w + 2 * c8) * 4;
  int a_row = (16 * wc + 2 * r8) * 4;
  int o_o = c8 == s ? OOB : WS_O + h * 8192 + c8 * 1024 + wc * 128 + r8 * 16;
  int o_pub = (r & 1) ? OOB : WS_O + (h * 8 + s) * 1024 + a_od * 2;
  int o_y = (lane & 24) == 0 ? WS_Y + (s * 32 + h) * 512 + w * 128 + c8 * 16 + (r >> 1) * 8 : OOB;
  int o_fin = s * 16384 + (h >> 3) * 128 + r8 * 2048 + c8 * 16;
  int o_out = (lane & 24) == 0 ? 128 * s + 32 * (h >> 3) + 4 * c8 + 2 * (r >> 1) : OOB;
  int o_wo = r8 * 2048 + c8 * 16;
  int o_f = w < 4 ? WS_F + h * 128 + c8 * 16 : fin ? WS_Z + o_fin : w != 7 ? OOB
          : lane < 8 ? WS_O + (h * 8 + s) * 1024 + lane * 128 : lane < 12 ? WS_Y + (s * 32 + h) * 512 + (lane - 8) * 128 : OOB;
  asm volatile("" : "+v"(a_part), "+v"(a_mrg), "+v"(a_od), "+v"(a_yw), "+v"(a_yr), "+v"(a_row), "+v"(o_o), "+v"(o_pub), "+v"(o_y), "+v"(o_fin), "+v"(o_out), "+v"(o_wo), "+v"(o_f));
  asm volatile("" : "+v"(m8[0]), "+v"(m8[1]), "+v"(m8[2]), "+v"(m8[3]), "+v"(mk[0]), "+v"(mk[1]), "+v"(mk[2]), "+v"(mk[3]));
  const bool me = c8 == s;
  char* const lds = (char*)&L;
  f32x4 oa = z4;
#define PV(T) do { oa = mfma(diag(dppu<0x150 + (T)>(pp), mk), vt[T], oa); asm volatile("" : "+v"(oa)); } while (0)
  LDV(6); PV(0);
  LDV(7); PV(1);
  LDV(8); PV(2);
  if (w == 6) {
    unsigned long long sv;
    asm volatile("s_mov_b64 %0, exec\n s_mov_b64 exec, 0\n s_branch fz_hop0\n fz_ret:\n s_mov_b64 exec, %0" : "=s"(sv) :: "memory");
  }
#define HOP(i, nx) asm volatile("fz_hop" #i ": s_cbranch_execz " nx)
  LDV(9); PV(3);
  u32x4 gz[4];
  gz[0] = LD128(rs, o_f, 0, SC1);
  {
    const rsrc_t rz = mk_rsrc((void*)pws, fin ? WS_END : 0);
#pragma unroll
    for (int i = 1; i < 4; ++i) gz[i] = LD128(rz, o_f + i * 512, 0, SC1);
  }
  const unsigned xo = __builtin_amdgcn_raw_buffer_load_b16(mk_rsrc((void*)px, fin ? 1024 : 0), o_out, 0, 0);
  SB();
  LDV(10); PV(4);
  if (PRIO) __builtin_amdgcn_s_setprio(0);
  LDV(11); PV(5);
  LDV(12); PV(6);
  const bool bad1 = w < 4 && gz[0][1] != tag;
  const u32x2 gf2 = LD64(rs, bad1 ? o_f : OOB, 0, SC1);
  SB();
  LDV(13); PV(7);
  SB();
  LDV(14);
  LDV(15);
  PV(8); PV(9); PV(10); PV(11); PV(12); PV(13);
  if (w >= 8) { dl = te + (15 - w) * 4 + WOD; PACE(0); }
  const rsrc_t rwo = mk_rsrc((void*)po, (w >= 8 && !NOWO) ? 4u << 20 : 0);
  const u32x4 wov0 = LD128(rwo, o_wo, (h * DH + 16 * wc) * 1024 + s * 128, KV_AUX);
  const u32x4 wov1 = LD128(rwo, o_wo + 1024, (h * DH + 16 * wc) * 1024 + s * 128, KV_AUX);
  SB();
  float g0 = 0.f, g1 = 0.f;
  f32x4 fw = z4;
  if (w < 4) {
    u32x2 gf = bad1 ? gf2 : u32x2{gz[0][0], gz[0][1]};
    if (__any(gf[1] != tag)) {
      _Pragma("nounroll") for (unsigned spins = 0; spins < SPIN_CAP; ++spins) {
        MEMFENCE();
        gf = LD64(rs, o_f, 0, SC1);
        if (!__any(gf[1] != tag)) break;
        __builtin_amdgcn_s_sleep(4);
      }
    }
    MEMFENCE();
    fw = *(const f32x4*)&L.wmax[4 * r];
    const float ls = __uint_as_float(gf[0]);
    const float MM = oct_max(ls);
    const float lh = MM + __builtin_amdgcn_logf(oct_sum(__builtin_amdgcn_exp2f(ls - MM)));
#pragma unroll
    for (int i = 0; i < 4; ++i) fw[i] = __builtin_amdgcn_exp2f(fw[i] - lh);
    STAMPW(13, 0);
  } else if (fin) {
    bool bad = false;
#pragma unroll
    for (int i = 0; i < 4; ++i) bad |= (gz[i][1] != tag) | (gz[i][3] != tag);
    _Pragma("nounroll") for (unsigned spins = 0; __any(bad) && spins < SPIN_CAP; ++spins) {
      __builtin_amdgcn_s_sleep(4);
      MEMFENCE();
      bad = false;
#pragma unroll
      for (int i = 0; i < 4; ++i) gz[i] = LD128(rs, o_f + i * 512, 0, SC1);
#pragma unroll
      for (int i = 0; i < 4; ++i) bad |= (gz[i][1] != tag) | (gz[i][3] != tag);
    }
    float v0 = (__uint_as_float(gz[0][0]) + __uint_as_float(gz[1][0])) + (__uint_as_float(gz[2][0]) + __uint_as_float(gz[3][0]));
    float v1 = (__uint_as_float(gz[0][2]) + __uint_as_float(gz[1][2])) + (__uint_as_float(gz[2][2]) + __uint_as_float(gz[3][2]));
    g0 = rbf(silu(rbf(col_sum(v0, v1))));
    g1 = blo(xo);
    o_fin += WS_Y;
    asm volatile("" : "+v"(g0), "+v"(g1), "+v"(o_fin));
  }
  asm volatile("" :: "v"(gz[0]));
  PV(14); PV(15);
#undef PV
  if (lane < 32) *(f32x4*)(lds + offsetof(Lds, part) + a_part) = oa;
  STAMPW(5, 15);
  asm volatile("s_waitcnt lgkmcnt(0)\n s_barrier" ::: "memory");
  HOP(0, "fz_hop1");
  STAMPW(6, 6);
  u32x4 go;
  if (w < 4) {
    const float* src = (const float*)(lds + offsetof(Lds, part) + a_mrg);
    const f32x2 v0 = *(const f32x2*)src, v1 = *(const f32x2*)(src + 128), v2 = *(const f32x2*)(src + 256), v3 = *(const f32x2*)(src + 384);
    const f32x2 av = (fw[0] * v0 + fw[1] * v1) + (fw[2] * v2 + fw[3] * v3);
    const float a = x16_sum(x32_sum2(av[0], av[1]));
    ST64(a, tag, rs, o_pub);
    *(float*)(lds + offsetof(Lds, own) + a_od) = a;
    STAMPW(12, 0);
  }
  asm volatile("s_waitcnt lgkmcnt(0)\n s_barrier" ::: "memory");
  HOP(1, "fz_hop2");
  if (w >= 8) {
    f32x2 ownv = *(const f32x2*)(lds + offsetof(Lds, own) + a_row);
    asm volatile("" : "+v"(ownv));
    if (DL1) __builtin_amdgcn_s_sleep(DL1);
    go = LD128(rs, o_o, 0, SC1);
    go = me ? u32x4{__float_as_uint(ownv[0]), tag, __float_as_uint(ownv[1]), tag} : go;
    _Pragma("nounroll") for (unsigned spins = 0; spins < SPIN_CAP; ++spins) {
      const bool bad = (go[1] != tag) | (go[3] != tag);
      if (!__any(bad)) break;
      MEMFENCE();
      if (bad) go = LD128(rs, o_o, 0, SC1);
    }
    HOP(2, "fz_hop3");
    STAMPW(7, 8);
    const float o0 = oct_sum(__uint_as_float(go[0])), o1 = oct_sum(__uint_as_float(go[2]));
    f32x4 y = mfma(diag(pkbf(o0, o0), m8), wov0, z4);
    y = mfma(diag(pkbf(o1, o1), m8), wov1, y);
    if (valid8) *(f32x4*)(lds + offsetof(Lds, ypart) + a_yw) = y;
  }
  asm volatile("s_waitcnt lgkmcnt(0)\n s_barrier" ::: "memory");
  HOP(3, "fz_hop4");
  STAMPW(8, 6);
  if (w < 4) {
    HOP(4, "fz_hop5");
    const float* src = (const float*)(lds + offsetof(Lds, ypart) + a_yr);
    f32x2 a = *(const f32x2*)src + *(const f32x2*)(src + 64);
    ST64(col_sum(a[0], a[1]), tag, rs, o_y);
  } else if (fin) {
    HOP(5, "fz_hop6");
    if (DL2) __builtin_amdgcn_s_sleep(DL2);
    float va, vb, vc2, vd;
    typedef unsigned u32x16 __attribute__((ext_vector_type(16)));
    u32x16 keepA, keepB;
    unsigned cnt_;
    asm volatile("buffer_load_dwordx4 v[64:67], %[off], %[rs], 0 offen offset:0 sc1\n buffer_load_dwordx4 v[68:71], %[off], %[rs], 0 offen offset:512 sc1\n buffer_load_dwordx4 v[72:75], %[off], %[rs], 0 offen offset:1024 sc1\n buffer_load_dwordx4 v[76:79], %[off], %[rs], 0 offen offset:1536 sc1\n s_sleep %[p2]\n s_mov_b32 %[cnt], 262144\n"
      "1: buffer_load_dwordx4 v[80:83], %[off], %[rs], 0 offen offset:0 sc1\n buffer_load_dwordx4 v[84:87], %[off], %[rs], 0 offen offset:512 sc1\n buffer_load_dwordx4 v[88:91], %[off], %[rs], 0 offen offset:1024 sc1\n buffer_load_dwordx4 v[92:95], %[off], %[rs], 0 offen offset:1536 sc1\n s_waitcnt vmcnt(4)\n v_cmpx_eq_u32 v65, %[tag]\n v_cmpx_eq_u32 v67, %[tag]\n v_cmpx_eq_u32 v69, %[tag]\n v_cmpx_eq_u32 v71, %[tag]\n v_cmpx_eq_u32 v73, %[tag]\n v_cmpx_eq_u32 v75, %[tag]\n v_cmpx_eq_u32 v77, %[tag]\n v_cmpx_eq_u32 v79, %[tag]\n s_cmp_eq_u64 exec, -1\n s_mov_b64 exec, -1\n s_cbranch_scc1 2f\n"
      "buffer_load_dwordx4 v[64:67], %[off], %[rs], 0 offen offset:0 sc1\n buffer_load_dwordx4 v[68:71], %[off], %[rs], 0 offen offset:512 sc1\n buffer_load_dwordx4 v[72:75], %[off], %[rs], 0 offen offset:1024 sc1\n buffer_load_dwordx4 v[76:79], %[off], %[rs], 0 offen offset:1536 sc1\n s_waitcnt vmcnt(4)\n v_cmpx_eq_u32 v81, %[tag]\n v_cmpx_eq_u32 v83, %[tag]\n v_cmpx_eq_u32 v85, %[tag]\n v_cmpx_eq_u32 v87, %[tag]\n v_cmpx_eq_u32 v89, %[tag]\n v_cmpx_eq_u32 v91, %[tag]\n v_cmpx_eq_u32 v93, %[tag]\n v_cmpx_eq_u32 v95, %[tag]\n s_cmp_eq_u64 exec, -1\n s_mov_b64 exec, -1\n s_cbranch_scc1 3f\n"
      "s_sub_u32 %[cnt], %[cnt], 1\n s_cmp_lg_u32 %[cnt], 0\n s_cbranch_scc1 1b\n"
      "3: v_add_f32 %[a], v80, v84\n v_add_f32 %[b], v82, v86\n v_add_f32 %[c], v88, v92\n v_add_f32 %[d], v90, v94\n s_branch 4f\n"
      "2: v_add_f32 %[a], v64, v68\n v_add_f32 %[b], v66, v70\n v_add_f32 %[c], v72, v76\n v_add_f32 %[d], v74, v78\n 4:"
      : [a] "=&v"(va), [b] "=&v"(vb), [c] "=&v"(vc2), [d] "=&v"(vd), [cnt] "=&s"(cnt_), "=&{v[64:79]}"(keepA), "=&{v[80:95]}"(keepB)
      : [off] "v"(o_fin), [rs] "s"(rs), [tag] "v"(tag), [p2] "i"(P2) : "scc", "vcc", "memory");
    HOP(6, "fz_hop7");
    STAMP(9);
    const float ov = g1 + rbf(rbf(col_sum(va + vc2, vb + vd)) * g0);
    __builtin_amdgcn_raw_buffer_store_b16((u16)pkbf(ov, ov), mk_rsrc((void*)pout, 1024), o_out, 0, OUT_AUX);
    HOP(7, "fz_ret");
    asm volatile("fz_last:");
    if (b == 0 && lane == 0) __builtin_amdgcn_raw_buffer_store_b32(tag, rs, WS_EPOCH, 0, SC1);
    if (b == 0) {
      const unsigned long long te_ = __builtin_amdgcn_s_memrealtime();
      int st = 0, mx = -0x7fffffff, mn = 0x7fffffff, sum = 0;
      for (int i = 0; i < 4; ++i) {
        const int d0 = (int)(te_ - __hip_atomic_load((GAS unsigned long long*)(pws + WS_STAMP) + (lane + 64 * i) * 16, __ATOMIC_RELAXED, __HIP_MEMORY_SCOPE_AGENT));
        const int dk = PROBE_K == 99 ? 0 : (int)(te_ - __hip_atomic_load((GAS unsigned long long*)(pws + WS_STAMP) + (lane + 64 * i) * 16 + (PROBE_K & 15), __ATOMIC_RELAXED, __HIP_MEMORY_SCOPE_AGENT));
        st = max(st, d0); mx = max(mx, dk); mn = min(mn, dk); sum += dk;
      }
      for (int o = 32; o > 0; o >>= 1) { st = max(st, __shfl_xor(st, o)); mn = min(mn, __shfl_xor(mn, o)); mx = max(mx, __shfl_xor(mx, o)); sum += __shfl_xor(sum, o); }
      int v = PROBE_M == 0 ? st - mn : PROBE_M == 1 ? st - sum / 256 : PROBE_M == 2 ? st - mx : 0;
      int dl_ = PROBE_G * __builtin_amdgcn_readfirstlane(v);
      dl_ = min(max(dl_, 0), 100000) + 200;
      while ((int)(__builtin_amdgcn_s_memrealtime() - te_) < dl_) __builtin_amdgcn_s_sleep(1);
    }
    asm volatile("" :: "v"(keepA), "v"(keepB));
  }
}

extern "C" void kernel_launch(void* const* d_in, const int* in_sizes, int n_in, void* d_out, int out_size, void* d_ws, size_t ws_size, hipStream_t stream) {
  fused<<<256, 1024, 0, stream>>>((const u16*)d_in[1], (const u16*)d_in[2], (const u16*)d_in[0], (const u16*)d_in[3], (const u16*)d_in[6],
                                  (unsigned char*)d_ws, (const u16*)d_in[4], (const u16*)d_in[7], (u16*)d_out);
}
```

```cpp
#include <hip/hip_runtime.h>
#include <stdint.h>
#include <stddef.h>

typedef unsigned short u16;
typedef unsigned u32x2 __attribute__((ext_vector_type(2)));
typedef unsigned u32x4 __attribute__((ext_vector_type(4)));
typedef unsigned u32x16 __attribute__((ext_vector_type(16)));
typedef unsigned long long u64;
typedef unsigned long long u64x8 __attribute__((ext_vector_type(8)));
typedef float f32x2 __attribute__((ext_vector_type(2)));
typedef float f32x4 __attribute__((ext_vector_type(4)));
typedef __bf16 bf16x2 __attribute__((ext_vector_type(2)));
typedef __bf16 bf16x8 __attribute__((ext_vector_type(8)));
typedef __amdgpu_buffer_rsrc_t rsrc_t;

#define TREL 1826
#define KEPS 45
#define WOD 30
#define DL1 16
#define DL2 12
#define PSL "2"
#define VT 1
#define AGG 0.5f
#define STG 60
#define MAPX 0
#define STAMPS 0

constexpr int D = 512, SK = 8192, DH = 128, FF = 1376;
constexpr float LOG2E = 1.4426950408889634f;
constexpr int NT = 2, SC1 = 16;
constexpr int OOB = 0x40000000;
constexpr int TB = 12;
constexpr int LEAD = 17;
constexpr unsigned SPIN_CAP = 1u << 18;
constexpr unsigned WS_EPOCH = 0;
constexpr unsigned WS_T = 4096;
constexpr unsigned WS_Q = 8192;
constexpr unsigned WS_H = WS_Q + 256 * 1024;
constexpr unsigned WS_F = WS_H + 256 * 512;
constexpr unsigned WS_O = WS_F + 32 * 128;
constexpr unsigned WS_Y = WS_O + 256 * 1024;
constexpr unsigned WS_Z = WS_Y + 256 * 512;
constexpr unsigned WS_END = WS_Z + 256 * 512;

__device__ __forceinline__ float rbf(float f) { return (float)(__bf16)f; }
__device__ __forceinline__ float blo(unsigned u) { return __uint_as_float(u << 16); }
__device__ __forceinline__ float bhi(unsigned u) { return __uint_as_float(u & 0xffff0000u); }
__device__ __forceinline__ unsigned pkbf(float a, float b) { bf16x2 v = {(__bf16)a, (__bf16)b}; return __builtin_bit_cast(unsigned, v); }
__device__ __forceinline__ float dot2(unsigned a, unsigned b, float acc) {
  return __builtin_amdgcn_fdot2_f32_bf16(__builtin_bit_cast(bf16x2, a), __builtin_bit_cast(bf16x2, b), acc, false);
}
template <int CTRL> __device__ __forceinline__ float dppf(float x) {
  return __builtin_bit_cast(float, __builtin_amdgcn_mov_dpp(__builtin_bit_cast(int, x), CTRL, 0xf, 0xf, true));
}
template <int CTRL> __device__ __forceinline__ unsigned dppu(unsigned x) {
  return (unsigned)__builtin_amdgcn_mov_dpp((int)x, CTRL, 0xf, 0xf, true);
}
constexpr int XOR1 = 0xB1, XOR2 = 0x4E, XOR7 = 0x141, ROR8 = 0x128, BCAST = 0x150;
__device__ __forceinline__ float x16_sum(float x) {
  auto s = __builtin_amdgcn_permlane16_swap(__float_as_uint(x), __float_as_uint(x), false, false);
  return __uint_as_float(s[0]) + __uint_as_float(s[1]);
}
__device__ __forceinline__ float x32_sum2(float a, float b) {
  auto t = __builtin_amdgcn_permlane32_swap(__float_as_uint(a), __float_as_uint(b), false, false);
  return __uint_as_float(t[0]) + __uint_as_float(t[1]);
}
__device__ __forceinline__ float xrow_max(float x) {
  auto s = __builtin_amdgcn_permlane16_swap(__float_as_uint(x), __float_as_uint(x), false, false);
  x = fmaxf(__uint_as_float(s[0]), __uint_as_float(s[1]));
  auto t = __builtin_amdgcn_permlane32_swap(__float_as_uint(x), __float_as_uint(x), false, false);
  return fmaxf(__uint_as_float(t[0]), __uint_as_float(t[1]));
}
__device__ __forceinline__ float oct_sum(float x) { x += dppf<XOR1>(x); x += dppf<XOR2>(x); x += dppf<XOR7>(x); return x; }
__device__ __forceinline__ float oct_max(float x) { x = fmaxf(x, dppf<XOR1>(x)); x = fmaxf(x, dppf<XOR2>(x)); x = fmaxf(x, dppf<XOR7>(x)); return x; }
__device__ __forceinline__ float row_sum(float x) { x = oct_sum(x); x += dppf<ROR8>(x); return x; }
__device__ __forceinline__ float row_max(float x) { x = oct_max(x); x = fmaxf(x, dppf<ROR8>(x)); return x; }
__device__ __forceinline__ float wave_sum(float x) { x = x16_sum(row_sum(x)); return x32_sum2(x, x); }
__device__ __forceinline__ float wave_max(float x) { return xrow_max(row_max(x)); }
__device__ __forceinline__ float col_sum(float a, float b) { const float v = x16_sum(x32_sum2(a, b)); return v + dppf<ROR8>(v); }
__device__ __forceinline__ f32x4 mfma(u32x4 a, u32x4 b, f32x4 c) {
  return __builtin_amdgcn_mfma_f32_16x16x32_bf16(__builtin_bit_cast(bf16x8, a), __builtin_bit_cast(bf16x8, b), c, 0, 0, 0);
}
__device__ __forceinline__ u32x4 diag(unsigned pk, const u32x4& m) { return u32x4{pk & m[0], pk & m[1], pk & m[2], pk & m[3]}; }
__device__ __forceinline__ float silu(float z) { return z * __builtin_amdgcn_rcpf(1.f + __builtin_amdgcn_exp2f(-z * LOG2E)); }
__device__ __forceinline__ rsrc_t mk_rsrc(u64 p, unsigned bytes) { return __builtin_amdgcn_make_buffer_rsrc((void*)p, 0, bytes, 0x00020000); }
#define F(u) __uint_as_float(u)
#define LD128(rs, vo, so, aux) __builtin_amdgcn_raw_buffer_load_b128(rs, vo, so, aux)
#define LD64(rs, vo, so, aux) __builtin_amdgcn_raw_buffer_load_b64(rs, vo, so, aux)
#define ST128(a, b, tag, rs, vo) __builtin_amdgcn_raw_buffer_store_b128((u32x4{__float_as_uint(a), tag, __float_as_uint(b), tag}), rs, vo, 0, SC1)
#define ST64(a, tag, rs, vo) __builtin_amdgcn_raw_buffer_store_b64((u32x2{__float_as_uint(a), tag}), rs, vo, 0, SC1)
#define LDS_LD(p) __hip_atomic_load(p, __ATOMIC_RELAXED, __HIP_MEMORY_SCOPE_WORKGROUP)
#define SB() __builtin_amdgcn_sched_barrier(0)
#define MEMFENCE() asm volatile("" ::: "memory")
#define BARRIER() asm volatile("s_waitcnt lgkmcnt(0)\n s_barrier" ::: "memory")
#define SPIN _Pragma("nounroll") for
#define PACE() asm volatile("1: s_memrealtime s[90:91]\n s_waitcnt lgkmcnt(0)\n s_lshl_b32 s90, s90, 4\n s_sub_u32 s90, s90, %0\n s_cmp_lt_i32 s90, 0\n" \
                            " s_cbranch_scc0 2f\n s_sleep " PSL "\n s_branch 1b\n 2: s_add_u32 %0, %0, %1" : "+s"(dl) : "s"(stp) : "scc", "s90", "s91")
#define PAD(i) asm volatile("fz_w%c0: s_cbranch_execz fz_wret" :: "i"(i))
#define HOP(i, nx) asm volatile("fz_hop" #i ": s_cbranch_execz " nx)
#define STAMP(i) do {} while (0)
#define STAMPW(i, wv) do {} while (0)

struct Lds {
  float part[16][128];
  float ypart[16][64];
  float wmax[16];
  float wsum[16];
  unsigned qb[64];
  unsigned tw[16];
  unsigned qready, cnt, tadj, spare, tag;
};

__global__ void __launch_bounds__(1024, 4) fused(const u16* kc, const u16* vc, const u16* x, const u16* wq, const u16* w1,
                                                  unsigned char* ws, const u16* wo, const u16* w2, u16* out) {
  __shared__ __attribute__((aligned(16))) Lds L;
  const int b = blockIdx.x;
  const int h = MAPX ? b >> 3 : (b & 7) * 4 + (b >> 6), s = MAPX ? b & 7 : (b >> 3) & 7;
  const int lane = threadIdx.x & 63, w = __builtin_amdgcn_readfirstlane(threadIdx.x >> 6);
  u64x8 ka; u64 pout;
  asm volatile("s_load_dwordx16 %0, %2, 0x0\n s_load_dwordx2 %1, %2, 0x40\n"
               " s_mov_b64 exec, 0\n s_getpc_b64 s[90:91]\n s_lshl_b32 s92, %3, 2\n s_add_u32 s90, s90, s92\n s_addc_u32 s91, s91, 0\n"
               " s_add_u32 s90, s90, 24\n s_addc_u32 s91, s91, 0\n s_setpc_b64 s[90:91]\n"
               " s_branch fz_w0\n s_branch fz_w1\n s_branch fz_w2\n s_branch fz_w3\n s_branch fz_w4\n s_branch fz_w5\n s_branch fz_w6\n s_branch fz_w7\n"
               " s_branch fz_w8\n s_branch fz_w9\n s_branch fz_w10\n s_branch fz_w11\n s_branch fz_w12\n s_branch fz_w13\n s_branch fz_w14\n s_branch fz_w15\n"
               "fz_wret: s_mov_b64 exec, -1\n s_waitcnt lgkmcnt(0)"
               : "=&s"(ka), "=&s"(pout) : "s"(__builtin_amdgcn_kernarg_segment_ptr()), "s"(w) : "s90", "s91", "s92", "scc");
  const u64 pk = ka[0], pv = ka[1], px = ka[2], pq = ka[3], p1 = ka[4], pws = ka[5], po = ka[6], p2 = ka[7];
  const rsrc_t rk = mk_rsrc(pk, 64u << 20), rv = mk_rsrc(pv, 64u << 20), rs = mk_rsrc(pws, WS_END);
  const f32x4 z4 = {0.f, 0.f, 0.f, 0.f};
  int kvs = h * (SK * 256) + s * 16384 + w * 1024, kvv = lane * 16;
#define TOFF(t) (kvs + (((t) + h) & 15) * 131072)
#define LDK(t) do { kt[t] = LD128(rk, kvv, TOFF(t), NT); SB(); } while (0)
#define LDV(t) do { PACE(); vt[t] = LD128(rv, kvv, TOFF(t), NT); SB(); } while (0)
#define LANE_VARS() int ln_ = lane; asm volatile("" : "+v"(ln_)); const int r = ln_ >> 4, c = ln_ & 15, r8 = ln_ >> 3, c8 = ln_ & 7; (void)r; (void)c; (void)r8; (void)c8
#define MASK16() u32x4 mk; _Pragma("unroll") for (int i = 0; i < 4; ++i) mk[i] = ((c >> 1) == i) ? ((c & 1) ? 0xffff0000u : 0x0000ffffu) : 0u
#define MASK8() u32x4 m8; _Pragma("unroll") for (int i = 0; i < 4; ++i) m8[i] = ((c8 >> 1) == i) ? ((c8 & 1) ? 0xffff0000u : 0x0000ffffu) : 0u; const bool valid8 = (r >> 1) == (c >> 3)
  u32x4 kt[16], vt[16];
  __builtin_amdgcn_s_setprio(3);
  LDK(0); LDK(1);
  PAD(7);
  if (w == 2 && lane < 20) L.tw[lane] = 0u;
  BARRIER();
  unsigned pfv;
  {
    u64 pc; unsigned len;
    asm volatile("s_getpc_b64 %0\n fz_pc: s_mov_b32 %1, fz_last-fz_pc\n s_max_i32 %1, %1, fz_last0-fz_pc\n s_max_i32 %1, %1, fz_last1-fz_pc" : "=s"(pc), "=s"(len) :: "scc");
    pfv = __builtin_amdgcn_raw_buffer_load_b32(mk_rsrc(pc, len), lane * 64, (15 - w) * 4096, 0);
    SB();
  }
  const int nch = 5 + (h < 12), ch0 = 5 * h + min(h, 12);
  unsigned stag = 0, tanc = 0;
  if (__builtin_expect(w < 2, 0)) {
    PAD(0);
    const bool r0 = w == 0;
    int sa0 = s * (64 * 8192) + h * 256, sa1 = s * (64 * FF * 2) + ch0 * 16;
    asm volatile("" : "+s"(sa0), "+s"(sa1));
    const int sa = r0 ? sa0 : sa1, da = r0 ? 8192 : FF * 2;
    {
      u64 ta0 = pv + kvs, ta1 = po + h * (DH * 1024), tc1 = p2 + ch0 * 8192;
      asm volatile("" : "+s"(ta0), "+s"(ta1), "+s"(tc1));
      asm volatile("s_load_dword s92, %0, 0x0\n s_load_dword s93, %1, 0x0\n s_load_dword s94, %2, 0x0\n s_load_dword s95, %3, 0x0"
                   :: "s"((r0 ? pq : p1) + sa), "s"(r0 ? pws : px), "s"(r0 ? ta0 : ta1), "s"(r0 ? pout : tc1) : "s92", "s93", "s94", "s95");
    }
    const rsrc_t rx = mk_rsrc(px, 1024), ra = mk_rsrc(r0 ? pq : p1, r0 ? 4u << 20 : 512 * FF * 2);
    LANE_VARS();
    const int va = c < (r0 ? 16 : nch) ? r * da + c * 16 : OOB;
    asm volatile("s_waitcnt lgkmcnt(0)" ::: "memory");
    stag = __builtin_amdgcn_raw_buffer_load_b32(rs, WS_EPOCH, 0, SC1);
    const u32x4 xa = LD128(rx, kvv, 0, 0);
    const unsigned xe = __builtin_amdgcn_raw_buffer_load_b16(rx, (4 * c + r) * 2, s * 128, 0);
    u32x4 wv[16];
    SB();
    PAD(1);
#pragma unroll
    for (int i = 0; i < 16; ++i) { wv[i] = LD128(ra, va, sa + i * 4 * da, NT); SB(); }
    PAD(2);
    MASK16();
    int o_a = (r0 ? WS_Q + (h * 8 + s) * 1024 : WS_H + (h * 8 + s) * 512) + (4 * c + 2 * (r & 1) + (r >> 1)) * 16;
    o_a = (r0 || c < 6) ? o_a : OOB;
    asm volatile("" : "+v"(stag), "+v"(o_a), "+v"(mk));
    stag += 1u;
    unsigned xp;
    {
      float xv[8];
#pragma unroll
      for (int j = 0; j < 4; ++j) { xv[2 * j] = blo(xa[j]); xv[2 * j + 1] = bhi(xa[j]); }
      float mean = wave_sum(((xv[0] + xv[1]) + (xv[2] + xv[3])) + ((xv[4] + xv[5]) + (xv[6] + xv[7]))) * (1.f / D);
      asm volatile("" : "+v"(mean));
      PAD(3);
      float sq = 0.f;
#pragma unroll
      for (int j = 0; j < 8; ++j) { const float d = xv[j] - mean; sq = fmaf(d, d, sq); }
      const float var = rbf(wave_sum(sq) * (1.f / D));
      const float rden = __builtin_amdgcn_rcpf(rbf(__builtin_amdgcn_sqrtf(rbf(var + 1e-5f))));
      const float xn = rbf(blo(xe) - rbf(mean)) * rden;
      xp = pkbf(xn, xn);
    }
    asm volatile("" : "+v"(xp));
    SB();
    PAD(4);
    f32x4 qa = z4;
#define QA(i) qa = mfma(diag(dppu<BCAST + (i)>(xp), mk), wv[i], qa);
    QA(0) QA(1) QA(2) QA(3) QA(4) QA(5)
    asm volatile("" : "+v"(qa)); SB(); PAD(5);
    QA(6) QA(7) QA(8) QA(9) QA(10) QA(11)
    asm volatile("" : "+v"(qa)); SB(); PAD(6);
    QA(12) QA(13) QA(14) QA(15)
#undef QA
    ST128(x32_sum2(qa[0], qa[2]), x32_sum2(qa[1], qa[3]), stag, rs, o_a);
    STAMPW(1, 0);
    {
      const unsigned now = (unsigned)__builtin_amdgcn_s_memrealtime(), tw = LDS_LD(&L.tw[c]);
      const float n = row_sum(tw ? 1.f : 0.f), d = row_sum(tw ? (float)(int)(now - tw) : 0.f);
      tanc = now - __builtin_amdgcn_readfirstlane((unsigned)(d * __builtin_amdgcn_rcpf(fmaxf(n, 1.f))));
      ST64(F(tanc), stag, rs, ((b >> 3) == LEAD && w == 1 && lane < 32) ? WS_T + lane * 128 + (b & 7) * 8 : OOB);
    }
  }
  PAD(8);
  asm volatile("" :: "v"(kt[0]));
  if (w >= 2) {
    tanc = (unsigned)__builtin_amdgcn_s_memrealtime() | 1u;
    L.tw[w] = tanc;
  }
  const unsigned tsp = TREL * 16 - (15 - w) * STG;
  unsigned te = tanc * 16 + tsp, dl = tanc * 16, stp = TREL * 16 * (1000 + KEPS) / 29000;
#define REANCHOR(R) do { const unsigned tr_ = __builtin_amdgcn_readfirstlane(LDS_LD(&L.tadj)); te = tr_ ? tr_ * 16 + tsp : te; \
    stp = (unsigned)max((int)(te - dl), 0) * (65536u / ((R) - 1)) >> 16; } while (0)
  STAMPW(15, 9); STAMPW(14, 2);
  asm volatile("" : "+s"(kvs), "+v"(kvv));
#pragma unroll
  for (int t = 2; t < TB; ++t) { if ((t & 1) == 0) PAD(8 + t / 2); PACE(); LDK(t); }
  if (__builtin_expect(w < 2, 0)) {
    LANE_VARS();
    if (w == 0) {
      u32x4 g[8];
      SPIN (unsigned spins = 0; spins < SPIN_CAP; ++spins) {
        bool ok = true;
#pragma unroll
        for (int i = 0; i < 8; ++i) g[i] = LD128(rs, WS_Q + h * 8192 + kvv + i * 1024, 0, SC1);
        SB();
#pragma unroll
        for (int i = 0; i < 8; ++i) ok &= (g[i][1] == stag) & (g[i][3] == stag);
        if (__all(ok)) break;
        asm volatile("s_sleep 8" ::: "memory");
      }
      float q0 = 0.f, q1 = 0.f;
#pragma unroll
      for (int i = 0; i < 8; ++i) { q0 += F(g[i][0]); q1 += F(g[i][2]); }
      L.qb[lane] = pkbf(q0, q1);
      if (lane == 0) L.tag = stag;
      __hip_atomic_store(&L.qready, 1u, __ATOMIC_RELEASE, __HIP_MEMORY_SCOPE_WORKGROUP);
      STAMP(2);
      asm volatile("fz_last0:");
    } else {
      MASK8();
      const rsrc_t r2 = mk_rsrc(p2, (ch0 + nch) * 8192);
      u32x4 wv[6];
#pragma unroll
      for (int i = 0; i < 6; ++i) { wv[i] = LD128(r2, r8 * 1024 + c8 * 16, (ch0 + i) * 8192 + s * 128, NT); SB(); }
      u32x2 g[6], gt;
      SPIN (unsigned spins = 0; spins < SPIN_CAP; ++spins) {
        gt = LD64(rs, WS_T + h * 128 + c8 * 8, 0, SC1);
#pragma unroll
        for (int i = 0; i < 6; ++i) g[i] = LD64(rs, WS_H + h * 4096 + c8 * 512 + r8 * 8 + i * 64, 0, SC1);
        SB();
        bool ok = gt[1] == stag;
#pragma unroll
        for (int i = 0; i < 6; ++i) ok &= g[i][1] == stag;
        if (__all(ok)) break;
        asm volatile("s_sleep 8" ::: "memory");
      }
      {
        const float dt = (float)(int)(gt[0] - tanc);
        L.tadj = (tanc + (int)fminf(fmaxf(oct_sum(dt) * (0.125f * (1.f - AGG)) + oct_max(dt) * AGG, -300.f), 300.f)) | 1u;
      }
      f32x4 za = z4;
#pragma unroll
      for (int i = 0; i < 6; ++i) {
        float hv = silu(rbf(oct_sum(F(g[i][0]))));
        hv = i < nch ? hv : 0.f;
        za = mfma(diag(pkbf(hv, hv), m8), wv[i], za);
      }
#pragma unroll
      for (int j = 0; j < 4; ++j) { const float v = valid8 ? za[j] : 0.f; za[j] = v + dppf<ROR8>(v); }
      ST128(x32_sum2(za[0], za[2]), x32_sum2(za[1], za[3]), stag, rs, c < 8 ? WS_Z + (s * 32 + h) * 512 + (4 * c + 2 * (r & 1) + (r >> 1)) * 16 : OOB);
      STAMP(10);
      asm volatile("fz_last1:");
    }
  }
  asm volatile("" : "+s"(kvs), "+v"(kvv));
#pragma unroll
  for (int t = TB; t < 16; ++t) { if ((t & 1) == 0) PAD(8 + t / 2); PACE(); LDK(t); }

  __builtin_amdgcn_s_setprio(2);
  REANCHOR(16);
  LDV(0); LDV(1); LDV(2); LDV(3);
  SPIN (unsigned spins = 0; LDS_LD(&L.qready) == 0u && spins < (SPIN_CAP << 6); ++spins) __builtin_amdgcn_s_sleep(2);
  MEMFENCE();
  STAMPW(3, 2);
  const u32x4 qp = *(const u32x4*)&L.qb[4 * (lane & 15)];
  const unsigned tag = L.tag;
  float xs[16];
#pragma unroll
  for (int t = 0; t < 16; ++t) {
    float d = dot2(kt[t][0], qp[0], 0.f);
    d = dot2(kt[t][1], qp[1], d); d = dot2(kt[t][2], qp[2], d); d = dot2(kt[t][3], qp[3], d);
    xs[t] = d;
  }
  asm volatile("" :: "v"(pfv));
  LANE_VARS();
#define TR_STEP(HALF, CTRL, BIT) _Pragma("unroll") for (int i = 0; i < (HALF); ++i) { \
    const float t1 = xs[i] + dppf<CTRL>(xs[i]), t2 = xs[i + (HALF)] + dppf<CTRL>(xs[i + (HALF)]); xs[i] = (c & (BIT)) ? t2 : t1; }
  TR_STEP(8, ROR8, 8) TR_STEP(4, XOR7, 4) TR_STEP(2, XOR2, 2) TR_STEP(1, XOR1, 1)
#undef TR_STEP
  asm volatile("" : "+v"(xs[0]));
  REANCHOR(12);
  LDV(4);
  const float sc = rbf(rbf(xs[0]) * 0.08838834764831845f) * LOG2E;
  const float wm = wave_max(sc);
  const float p = __builtin_amdgcn_exp2f(sc - wm);
  {
    const float wsu = wave_sum(p);
    unsigned old = 0;
    if (lane == 0) {
      L.wmax[w] = wm; L.wsum[w] = wsu;
      old = __hip_atomic_fetch_add(&L.cnt, 1u, __ATOMIC_ACQ_REL, __HIP_MEMORY_SCOPE_WORKGROUP);
    }
    if (__builtin_amdgcn_readfirstlane(old) == 15u) {
      const float wmc = L.wmax[c];
      const float M = row_max(wmc);
      const float lse = M + __builtin_amdgcn_logf(row_sum(L.wsum[c] * __builtin_amdgcn_exp2f(wmc - M)));
      ST128(lse, lse, tag, rs, lane == 0 ? WS_F + h * 128 + s * 16 : OOB);
      STAMP(4);
    }
  }
  unsigned pp = pkbf(p, p);
  asm volatile("" : "+v"(pp));
  __builtin_amdgcn_s_setprio(1);
  LDV(5);
  MASK16();
  MASK8();
  const int wc = w & 7;
  const bool fin = w == 5 && (h & 7) == s;
  int a_part = offsetof(Lds, part) + w * 512 + (8 * c + 4 * r) * 4;
  int a_mrg = offsetof(Lds, part) + r * 2048 + (32 * w + 2 * c) * 4;
  int a_yw = offsetof(Lds, ypart) + (2 * wc + (r >> 1)) * 256 + (8 * c8 + 4 * (r & 1)) * 4;
  int a_yr = offsetof(Lds, ypart) + r8 * 512 + (16 * w + 2 * c8) * 4;
  int o_o = WS_O + h * 8192 + c8 * 1024 + wc * 128 + r8 * 16;
  int o_pub = (r & 1) ? OOB : WS_O + (h * 8 + s) * 1024 + (32 * w + 2 * c + (r >> 1)) * 8;
  int o_y = (lane & 24) == 0 ? WS_Y + (s * 32 + h) * 512 + w * 128 + c8 * 16 + (r >> 1) * 8 : OOB;
  int o_fin = s * 16384 + (h >> 3) * 128 + r8 * 2048 + c8 * 16;
  int o_out = (lane & 24) == 0 ? 128 * s + 32 * (h >> 3) + 4 * c8 + 2 * (r >> 1) : OOB;
  int o_wo = r8 * 2048 + c8 * 16;
  int o_f = w < 4 ? WS_F + h * 128 + c8 * 16 : fin ? WS_Z + o_fin : w != 7 ? OOB
          : lane < 8 ? WS_O + (h * 8 + s) * 1024 + lane * 128 : lane < 12 ? WS_Y + (s * 32 + h) * 512 + (lane - 8) * 128 : OOB;
  asm volatile("" : "+v"(a_part), "+v"(a_mrg), "+v"(a_yw), "+v"(a_yr), "+v"(o_o), "+v"(o_pub), "+v"(o_y), "+v"(o_fin), "+v"(o_out), "+v"(o_wo), "+v"(o_f));
  asm volatile("" : "+v"(m8), "+v"(mk));
  char* const lds = (char*)&L;
  f32x4 oa = z4;
#define PV(T) do { oa = mfma(diag(dppu<BCAST + (T)>(pp), mk), vt[T], oa); asm volatile("" : "+v"(oa)); } while (0)
  LDV(6); PV(0);
  LDV(7); PV(1);
  LDV(8); PV(2);
  if (w == 6) asm volatile("s_mov_b64 exec, 0\n s_branch fz_hop0\n fz_ret:\n s_mov_b64 exec, -1" ::: "memory");
  LDV(9); PV(3);
  u32x4 gz[4];
  gz[0] = LD128(rs, o_f, 0, SC1);
  {
    const rsrc_t rz = mk_rsrc(pws, fin ? WS_END : 0);
#pragma unroll
    for (int i = 1; i < 4; ++i) gz[i] = LD128(rz, o_f + i * 512, 0, SC1);
  }
  const unsigned xo = __builtin_amdgcn_raw_buffer_load_b16(mk_rsrc(w == 4 ? po + h * (DH * 1024) : w == 6 ? pout : px, (fin || (VT && (w == 4 || w == 6))) ? 1024 : 0), o_out, 0, 0);
  SB();
  LDV(10); PV(4);
  __builtin_amdgcn_s_setprio(0);
  LDV(11); PV(5);
  LDV(12); PV(6);
  const bool bad1 = w < 4 && gz[0][1] != tag;
  const u32x2 gf2 = LD64(rs, bad1 ? o_f : OOB, 0, SC1);
  SB();
  LDV(13); PV(7);
  LDV(14);
  LDV(15);
  PV(8); PV(9); PV(10); PV(11); PV(12); PV(13);
  if (w >= 8) { dl = te + (15 - w) * STG + WOD * 16; PACE(); }
  const rsrc_t rwo = mk_rsrc(po, w >= 8 ? 4u << 20 : 0);
  const u32x4 wov0 = LD128(rwo, o_wo, (h * DH + 16 * wc) * 1024 + s * 128, NT);
  const u32x4 wov1 = LD128(rwo, o_wo + 1024, (h * DH + 16 * wc) * 1024 + s * 128, NT);
  SB();
  float gate = 0.f, xres = 0.f;
  f32x4 fw = z4;
  if (w < 4) {
    u32x2 gf = bad1 ? gf2 : u32x2{gz[0][0], gz[0][1]};
    if (__any(gf[1] != tag)) {
      SPIN (unsigned spins = 0; spins < SPIN_CAP; ++spins) {
        MEMFENCE();
        gf = LD64(rs, o_f, 0, SC1);
        if (!__any(gf[1] != tag)) break;
        __builtin_amdgcn_s_sleep(4);
      }
    }
    MEMFENCE();
    fw = *(const f32x4*)&L.wmax[4 * r];
    const float ls = F(gf[0]);
    const float MM = oct_max(ls);
    const float lh = MM + __builtin_amdgcn_logf(oct_sum(__builtin_amdgcn_exp2f(ls - MM)));
#pragma unroll
    for (int i = 0; i < 4; ++i) fw[i] = __builtin_amdgcn_exp2f(fw[i] - lh);
    STAMPW(13, 0);
  } else if (fin) {
    bool bad = false;
#pragma unroll
    for (int i = 0; i < 4; ++i) bad |= (gz[i][1] != tag) | (gz[i][3] != tag);
    SPIN (unsigned spins = 0; __any(bad) && spins < SPIN_CAP; ++spins) {
      __builtin_amdgcn_s_sleep(4);
      MEMFENCE();
      bad = false;
#pragma unroll
      for (int i = 0; i < 4; ++i) gz[i] = LD128(rs, o_f + i * 512, 0, SC1);
#pragma unroll
      for (int i = 0; i < 4; ++i) bad |= (gz[i][1] != tag) | (gz[i][3] != tag);
    }
    gate = rbf(silu(rbf(col_sum((F(gz[0][0]) + F(gz[1][0])) + (F(gz[2][0]) + F(gz[3][0])), (F(gz[0][2]) + F(gz[1][2])) + (F(gz[2][2]) + F(gz[3][2]))))));
    xres = blo(xo);
    o_fin += WS_Y;
    asm volatile("" : "+v"(gate), "+v"(xres), "+v"(o_fin));
  }
  asm volatile("" :: "v"(gz[0]), "v"(xo));
  PV(14); PV(15);
#undef PV
  if (lane < 32) *(f32x4*)(lds + a_part) = oa;
  STAMPW(5, 15);
  BARRIER();
  STAMPW(6, 6);
  if (w < 4) {
    const float* src = (const float*)(lds + a_mrg);
    const f32x2 v0 = *(const f32x2*)src, v1 = *(const f32x2*)(src + 128), v2 = *(const f32x2*)(src + 256), v3 = *(const f32x2*)(src + 384);
    HOP(0, "fz_hop2");
    const f32x2 av = (fw[0] * v0 + fw[1] * v1) + (fw[2] * v2 + fw[3] * v3);
    ST64(x16_sum(x32_sum2(av[0], av[1])), tag, rs, o_pub);
    STAMPW(12, 0);
  } else if (w >= 8) {
    u32x4 go;
    unsigned cnt_;
    asm volatile("s_sleep %[dl]\n s_mov_b32 %[cnt], 0x40000\n s_branch 2f\n"
                 "fz_hop2: s_branch fz_hop8\n"
                 "1: s_sub_u32 %[cnt], %[cnt], 1\n s_cbranch_scc1 3f\n"
                 "2: buffer_load_dwordx4 v[64:67], %[off], %[rs], 0 offen sc1\n s_waitcnt vmcnt(0)\n"
                 " v_cmp_ne_u32 vcc, v65, %[tag]\n v_cmp_ne_u32 s[90:91], v67, %[tag]\n s_or_b64 exec, vcc, s[90:91]\n s_cbranch_scc1 1b\n"
                 "3: s_mov_b64 exec, -1\n"
                 " v_add_f32_dpp v64, v64, v64 quad_perm:[1,0,3,2] row_mask:0xf bank_mask:0xf\n v_add_f32_dpp v66, v66, v66 quad_perm:[1,0,3,2] row_mask:0xf bank_mask:0xf\n s_nop 0\n"
                 " v_add_f32_dpp v64, v64, v64 quad_perm:[2,3,0,1] row_mask:0xf bank_mask:0xf\n v_add_f32_dpp v66, v66, v66 quad_perm:[2,3,0,1] row_mask:0xf bank_mask:0xf\n s_nop 0\n"
                 " v_add_f32_dpp v64, v64, v64 row_half_mirror row_mask:0xf bank_mask:0xf\n v_add_f32_dpp v66, v66, v66 row_half_mirror row_mask:0xf bank_mask:0xf\n"
                 " v_cvt_pk_bf16_f32 v64, v64, v64\n v_cvt_pk_bf16_f32 v66, v66, v66"
                 : "=&{v[64:67]}"(go), [cnt] "=&s"(cnt_) : [off] "v"(o_o), [rs] "s"(rs), [tag] "v"(tag), [dl] "i"(DL1)
                 : "scc", "vcc", "s90", "s91", "memory");
    STAMPW(7, 8);
    f32x4 y = mfma(diag(go[0], m8), wov0, z4);
    y = mfma(diag(go[2], m8), wov1, y);
    HOP(8, "fz_hop5");
    if (valid8) *(f32x4*)(lds + a_yw) = y;
  }
  BARRIER();
  STAMPW(8, 6);
  if (w < 4) {
    const float* src = (const float*)(lds + a_yr);
    const f32x2 a = *(const f32x2*)src + *(const f32x2*)(src + 64);
    ST64(col_sum(a[0], a[1]), tag, rs, o_y);
  } else if (fin) {
    HOP(5, "fz_hop6");
    __builtin_amdgcn_s_sleep(DL2);
    float va, vb, vc2, vd;
    u32x16 keepA, keepB;
    unsigned cnt_;
#define SWEEP(B0, B1, B2, B3) "buffer_load_dwordx4 " B0 ", %[off], %[rs], 0 offen sc1\n buffer_load_dwordx4 " B1 ", %[off], %[rs], 0 offen offset:512 sc1\n" \
    " buffer_load_dwordx4 " B2 ", %[off], %[rs], 0 offen offset:1024 sc1\n buffer_load_dwordx4 " B3 ", %[off], %[rs], 0 offen offset:1536 sc1\n"
#define SWEEP_A SWEEP("v[64:67]", "v[68:71]", "v[72:75]", "v[76:79]")
#define SWEEP_B SWEEP("v[80:83]", "v[84:87]", "v[88:91]", "v[92:95]")
#define CK(v) " v_cmpx_eq_u32 " v ", %[tag]\n"
#define CK_END " s_cmp_eq_u64 exec, -1\n s_mov_b64 exec, -1\n"
    asm volatile(SWEEP_A " s_sleep 5\n s_mov_b32 %[cnt], 0x40000\n"
      "1: " SWEEP_B " s_waitcnt vmcnt(4)\n" CK("v65") CK("v67") CK("v69") CK("v71") CK("v73") CK("v75") CK("v77") CK("v79") CK_END " s_cbranch_scc1 2f\n"
      SWEEP_A "fz_hop6: s_cbranch_execz fz_hop9\n s_waitcnt vmcnt(4)\n" CK("v81") CK("v83") CK("v85") CK("v87") CK("v89") CK("v91") CK("v93") CK("v95") CK_END " s_cbranch_scc1 3f\n"
      " s_sub_u32 %[cnt], %[cnt], 1\n s_cmp_lg_u32 %[cnt], 0\n s_cbranch_scc1 1b\n"
      "3: v_add_f32 %[a], v80, v84\n v_add_f32 %[b], v82, v86\n v_add_f32 %[c], v88, v92\n v_add_f32 %[d], v90, v94\n s_branch 4f\n"
      "fz_hop9: s_branch fz_hop7\n"
      "2: v_add_f32 %[a], v64, v68\n v_add_f32 %[b], v66, v70\n v_add_f32 %[c], v72, v76\n v_add_f32 %[d], v74, v78\n 4:"
      : [a] "=&v"(va), [b] "=&v"(vb), [c] "=&v"(vc2), [d] "=&v"(vd), [cnt] "=&s"(cnt_), "=&{v[64:79]}"(keepA), "=&{v[80:95]}"(keepB)
      : [off] "v"(o_fin), [rs] "s"(rs), [tag] "v"(tag) : "scc", "vcc", "memory");
    STAMP(9);
    const float ov = xres + rbf(rbf(col_sum(va + vc2, vb + vd)) * gate);
    __builtin_amdgcn_raw_buffer_store_b16((u16)pkbf(ov, ov), mk_rsrc(pout, 1024), o_out, 0, 0);
    HOP(7, "fz_ret");
    asm volatile("fz_last:");
    __builtin_amdgcn_raw_buffer_store_b32(tag, rs, (h == 0 && lane == 0) ? WS_EPOCH : OOB, 0, SC1);
    STAMP(11);
    asm volatile("" :: "v"(keepA), "v"(keepB));
  }
}

extern "C" void kernel_launch(void* const* d_in, const int* in_sizes, int n_in, void* d_out, int out_size, void* d_ws, size_t ws_size, hipStream_t stream) {
  fused<<<256, 1024, 0, stream>>>((const u16*)d_in[1], (const u16*)d_in[2], (const u16*)d_in[0], (const u16*)d_in[3], (const u16*)d_in[6],
                                  (unsigned char*)d_ws, (const u16*)d_in[4], (const u16*)d_in[7], (u16*)d_out);
}
```

```cpp
#include <hip/hip_runtime.h>
#include <stdint.h>
#include <stddef.h>

typedef unsigned short u16;
typedef unsigned u32x2 __attribute__((ext_vector_type(2)));
typedef unsigned u32x4 __attribute__((ext_vector_type(4)));
typedef unsigned u32x16 __attribute__((ext_vector_type(16)));
typedef unsigned long long u64;
typedef unsigned long long u64x8 __attribute__((ext_vector_type(8)));
typedef float f32x2 __attribute__((ext_vector_type(2)));
typedef float f32x4 __attribute__((ext_vector_type(4)));
typedef __bf16 bf16x2 __attribute__((ext_vector_type(2)));
typedef __bf16 bf16x8 __attribute__((ext_vector_type(8)));
typedef __amdgpu_buffer_rsrc_t rsrc_t;

constexpr unsigned TREL = 1808;
constexpr unsigned KEPS = 52;
constexpr unsigned STG = 60;
constexpr unsigned WOD = 32;
constexpr int DL1 = 17, DL2 = 12;

constexpr int D = 512, SK = 8192, DH = 128, FF = 1376;
constexpr float LOG2E = 1.4426950408889634f;
constexpr int NT = 2, SC1 = 16;
constexpr int OOB = 0x40000000;
constexpr int TB = 12;
constexpr int LEAD = 17;
constexpr unsigned SPIN_CAP = 1u << 18;
constexpr unsigned WS_EPOCH = 0;
constexpr unsigned WS_T = 4096;
constexpr unsigned WS_Q = 8192;
constexpr unsigned WS_H = WS_Q + 256 * 1024;
constexpr unsigned WS_F = WS_H + 256 * 512;
constexpr unsigned WS_O = WS_F + 32 * 128;
constexpr unsigned WS_Y = WS_O + 256 * 1024;
constexpr unsigned WS_Z = WS_Y + 256 * 512;
constexpr unsigned WS_END = WS_Z + 256 * 512;

__device__ __forceinline__ float rbf(float f) { return (float)(__bf16)f; }
__device__ __forceinline__ float blo(unsigned u) { return __uint_as_float(u << 16); }
__device__ __forceinline__ float bhi(unsigned u) { return __uint_as_float(u & 0xffff0000u); }
__device__ __forceinline__ unsigned pkbf(float a, float b) { bf16x2 v = {(__bf16)a, (__bf16)b}; return __builtin_bit_cast(unsigned, v); }
__device__ __forceinline__ float dot2(unsigned a, unsigned b, float acc) {
  return __builtin_amdgcn_fdot2_f32_bf16(__builtin_bit_cast(bf16x2, a), __builtin_bit_cast(bf16x2, b), acc, false);
}
template <int CTRL> __device__ __forceinline__ float dppf(float x) {
  return __builtin_bit_cast(float, __builtin_amdgcn_mov_dpp(__builtin_bit_cast(int, x), CTRL, 0xf, 0xf, true));
}
template <int CTRL> __device__ __forceinline__ unsigned dppu(unsigned x) {
  return (unsigned)__builtin_amdgcn_mov_dpp((int)x, CTRL, 0xf, 0xf, true);
}
constexpr int XOR1 = 0xB1, XOR2 = 0x4E, XOR7 = 0x141, ROR8 = 0x128, BCAST = 0x150;
__device__ __forceinline__ float x16_sum(float x) {
  auto s = __builtin_amdgcn_permlane16_swap(__float_as_uint(x), __float_as_uint(x), false, false);
  return __uint_as_float(s[0]) + __uint_as_float(s[1]);
}
__device__ __forceinline__ float x32_sum2(float a, float b) {
  auto t = __builtin_amdgcn_permlane32_swap(__float_as_uint(a), __float_as_uint(b), false, false);
  return __uint_as_float(t[0]) + __uint_as_float(t[1]);
}
__device__ __forceinline__ float xrow_max(float x) {
  auto s = __builtin_amdgcn_permlane16_swap(__float_as_uint(x), __float_as_uint(x), false, false);
  x = fmaxf(__uint_as_float(s[0]), __uint_as_float(s[1]));
  auto t = __builtin_amdgcn_permlane32_swap(__float_as_uint(x), __float_as_uint(x), false, false);
  return fmaxf(__uint_as_float(t[0]), __uint_as_float(t[1]));
}
__device__ __forceinline__ float oct_sum(float x) { x += dppf<XOR1>(x); x += dppf<XOR2>(x); x += dppf<XOR7>(x); return x; }
__device__ __forceinline__ float oct_max(float x) { x = fmaxf(x, dppf<XOR1>(x)); x = fmaxf(x, dppf<XOR2>(x)); x = fmaxf(x, dppf<XOR7>(x)); return x; }
__device__ __forceinline__ float row_sum(float x) { x = oct_sum(x); x += dppf<ROR8>(x); return x; }
__device__ __forceinline__ float row_max(float x) { x = oct_max(x); x = fmaxf(x, dppf<ROR8>(x)); return x; }
__device__ __forceinline__ float wave_sum(float x) { x = x16_sum(row_sum(x)); return x32_sum2(x, x); }
__device__ __forceinline__ float wave_max(float x) { return xrow_max(row_max(x)); }
__device__ __forceinline__ float col_sum(float a, float b) { const float v = x16_sum(x32_sum2(a, b)); return v + dppf<ROR8>(v); }
__device__ __forceinline__ f32x4 mfma(u32x4 a, u32x4 b, f32x4 c) {
  return __builtin_amdgcn_mfma_f32_16x16x32_bf16(__builtin_bit_cast(bf16x8, a), __builtin_bit_cast(bf16x8, b), c, 0, 0, 0);
}
__device__ __forceinline__ u32x4 diag(unsigned pk, const u32x4& m) { return u32x4{pk & m[0], pk & m[1], pk & m[2], pk & m[3]}; }
__device__ __forceinline__ float silu(float z) { return z * __builtin_amdgcn_rcpf(1.f + __builtin_amdgcn_exp2f(-z * LOG2E)); }
__device__ __forceinline__ rsrc_t mk_rsrc(u64 p, unsigned bytes) { return __builtin_amdgcn_make_buffer_rsrc((void*)p, 0, bytes, 0x00020000); }
#define F(u) __uint_as_float(u)
#define LD128(rs, vo, so, aux) __builtin_amdgcn_raw_buffer_load_b128(rs, vo, so, aux)
#define LD64(rs, vo, so, aux) __builtin_amdgcn_raw_buffer_load_b64(rs, vo, so, aux)
#define ST128(a, b, tag, rs, vo) __builtin_amdgcn_raw_buffer_store_b128((u32x4{__float_as_uint(a), tag, __float_as_uint(b), tag}), rs, vo, 0, SC1)
#define ST64(a, tag, rs, vo) __builtin_amdgcn_raw_buffer_store_b64((u32x2{__float_as_uint(a), tag}), rs, vo, 0, SC1)
#define LDS_LD(p) __hip_atomic_load(p, __ATOMIC_RELAXED, __HIP_MEMORY_SCOPE_WORKGROUP)
#define SB() __builtin_amdgcn_sched_barrier(0)
#define MEMFENCE() asm volatile("" ::: "memory")
#define BARRIER() asm volatile("s_waitcnt lgkmcnt(0)\n s_barrier" ::: "memory")
#define SPIN _Pragma("nounroll") for
#define PACE() asm volatile("1: s_memrealtime s[90:91]\n s_waitcnt lgkmcnt(0)\n s_lshl_b32 s90, s90, 4\n s_sub_u32 s90, s90, %0\n s_cmp_lt_i32 s90, 0\n" \
                            " s_cbranch_scc0 2f\n s_sleep 1\n s_branch 1b\n 2: s_add_u32 %0, %0, %1" : "+s"(dl) : "s"(stp) : "scc", "s90", "s91")
#define PAD(i) asm volatile("fz_w%c0: s_cbranch_execz fz_wret" :: "i"(i))
#define HOP(i, nx) asm volatile("fz_hop" #i ": s_cbranch_execz " nx)

struct Lds {
  float part[16][128];
  float ypart[16][64];
  float wmax[16];
  float wsum[16];
  unsigned qb[64];
  unsigned tw[16];
  unsigned qready, cnt, tadj, spare, tag;
};

__global__ void __launch_bounds__(1024, 4) fused(const u16* kc, const u16* vc, const u16* x, const u16* wq, const u16* w1,
                                                  unsigned char* ws, const u16* wo, const u16* w2, u16* out) {
  __shared__ __attribute__((aligned(16))) Lds L;
  const int b = blockIdx.x;
  const int h = (b & 7) * 4 + (b >> 6), s = (b >> 3) & 7;
  const int lane = threadIdx.x & 63, w = __builtin_amdgcn_readfirstlane(threadIdx.x >> 6);
  u64x8 ka; u64 pout;
  asm volatile("s_load_dwordx16 %0, %2, 0x0\n s_load_dwordx2 %1, %2, 0x40\n"
               " s_mov_b64 exec, 0\n s_getpc_b64 s[90:91]\n s_lshl_b32 s92, %3, 2\n s_add_u32 s90, s90, s92\n s_addc_u32 s91, s91, 0\n"
               " s_add_u32 s90, s90, 24\n s_addc_u32 s91, s91, 0\n s_setpc_b64 s[90:91]\n"
               " s_branch fz_w0\n s_branch fz_w1\n s_branch fz_w2\n s_branch fz_w3\n s_branch fz_w4\n s_branch fz_w5\n s_branch fz_w6\n s_branch fz_w7\n"
               " s_branch fz_w8\n s_branch fz_w9\n s_branch fz_w10\n s_branch fz_w11\n s_branch fz_w12\n s_branch fz_w13\n s_branch fz_w14\n s_branch fz_w15\n"
               "fz_wret: s_mov_b64 exec, -1\n s_waitcnt lgkmcnt(0)"
               : "=&s"(ka), "=&s"(pout) : "s"(__builtin_amdgcn_kernarg_segment_ptr()), "s"(w) : "s90", "s91", "s92", "scc");
  const u64 pk = ka[0], pv = ka[1], px = ka[2], pq = ka[3], p1 = ka[4], pws = ka[5], po = ka[6], p2 = ka[7];
  const rsrc_t rk = mk_rsrc(pk, 64u << 20), rv = mk_rsrc(pv, 64u << 20), rs = mk_rsrc(pws, WS_END);
  const f32x4 z4 = {0.f, 0.f, 0.f, 0.f};
  int kvs = h * (SK * 256) + s * 16384 + w * 1024, kvv = lane * 16;
#define TOFF(t) (kvs + (((t) + h) & 15) * 131072)
#define LDK(t) do { kt[t] = LD128(rk, kvv, TOFF(t), NT); SB(); } while (0)
#define LDV(t) do { PACE(); vt[t] = LD128(rv, kvv, TOFF(t), NT); SB(); } while (0)
#define LANE_VARS() int ln_ = lane; asm volatile("" : "+v"(ln_)); const int r = ln_ >> 4, c = ln_ & 15, r8 = ln_ >> 3, c8 = ln_ & 7; (void)r; (void)c; (void)r8; (void)c8
#define MASK16() u32x4 mk; _Pragma("unroll") for (int i = 0; i < 4; ++i) mk[i] = ((c >> 1) == i) ? ((c & 1) ? 0xffff0000u : 0x0000ffffu) : 0u
#define MASK8() u32x4 m8; _Pragma("unroll") for (int i = 0; i < 4; ++i) m8[i] = ((c8 >> 1) == i) ? ((c8 & 1) ? 0xffff0000u : 0x0000ffffu) : 0u; const bool valid8 = (r >> 1) == (c >> 3)
  u32x4 kt[16], vt[16];
  __builtin_amdgcn_s_setprio(3);
  LDK(0); LDK(1);
  PAD(7);
  if (w == 2 && lane < 20) ((unsigned*)((char*)&L + offsetof(Lds, tw)))[lane] = 0u;
  BARRIER();
  unsigned pfv;
  {
    u64 pc; unsigned len;
    asm volatile("s_getpc_b64 %0\n fz_pc: s_mov_b32 %1, fz_last-fz_pc\n s_max_i32 %1, %1, fz_last0-fz_pc\n s_max_i32 %1, %1, fz_last1-fz_pc" : "=s"(pc), "=s"(len) :: "scc");
    pfv = __builtin_amdgcn_raw_buffer_load_b32(mk_rsrc(pc, len), lane * 64, (15 - w) * 4096, 0);
    SB();
  }
  const int nch = 5 + (h < 12), ch0 = 5 * h + min(h, 12);
  unsigned stag = 0, tanc = 0;
  if (__builtin_expect(w < 2, 0)) {
    PAD(0);
    const bool r0 = w == 0;
    int sa0 = s * (64 * 8192) + h * 256, sa1 = s * (64 * FF * 2) + ch0 * 16;
    asm volatile("" : "+s"(sa0), "+s"(sa1));
    const int sa = r0 ? sa0 : sa1, da = r0 ? 8192 : FF * 2;
    {
      u64 ta0 = pv + kvs, ta1 = po + h * (DH * 1024), tc1 = p2 + ch0 * 8192;
      asm volatile("" : "+s"(ta0), "+s"(ta1), "+s"(tc1));
      asm volatile("s_load_dword s92, %0, 0x0\n s_load_dword s93, %1, 0x0\n s_load_dword s94, %2, 0x0\n s_load_dword s95, %3, 0x0"
                   :: "s"((r0 ? pq : p1) + sa), "s"(r0 ? pws : px), "s"(r0 ? ta0 : ta1), "s"(r0 ? pout : tc1) : "s92", "s93", "s94", "s95");
    }
    const rsrc_t rx = mk_rsrc(px, 1024), ra = mk_rsrc(r0 ? pq : p1, r0 ? 4u << 20 : 512 * FF * 2);
    LANE_VARS();
    const int va = c < (r0 ? 16 : nch) ? r * da + c * 16 : OOB;
    asm volatile("s_waitcnt lgkmcnt(0)" ::: "memory");
    stag = __builtin_amdgcn_raw_buffer_load_b32(rs, WS_EPOCH, 0, SC1);
    const u32x4 xa = LD128(rx, kvv, 0, 0);
    const unsigned xe = __builtin_amdgcn_raw_buffer_load_b16(rx, (4 * c + r) * 2, s * 128, 0);
    u32x4 wv[16];
    SB();
    PAD(1);
#pragma unroll
    for (int i = 0; i < 16; ++i) { wv[i] = LD128(ra, va, sa + i * 4 * da, NT); SB(); }
    PAD(2);
    MASK16();
    int o_a = (r0 ? WS_Q + (h * 8 + s) * 1024 : WS_H + (h * 8 + s) * 512) + (4 * c + 2 * (r & 1) + (r >> 1)) * 16;
    o_a = (r0 || c < 6) ? o_a : OOB;
    asm volatile("" : "+v"(stag), "+v"(o_a), "+v"(mk));
    stag += 1u;
    unsigned xp;
    {
      float xv[8];
#pragma unroll
      for (int j = 0; j < 4; ++j) { xv[2 * j] = blo(xa[j]); xv[2 * j + 1] = bhi(xa[j]); }
      float mean = wave_sum(((xv[0] + xv[1]) + (xv[2] + xv[3])) + ((xv[4] + xv[5]) + (xv[6] + xv[7]))) * (1.f / D);
      asm volatile("" : "+v"(mean));
      PAD(3);
      float sq = 0.f;
#pragma unroll
      for (int j = 0; j < 8; ++j) { const float d = xv[j] - mean; sq = fmaf(d, d, sq); }
      const float var = rbf(wave_sum(sq) * (1.f / D));
      const float rden = __builtin_amdgcn_rcpf(rbf(__builtin_amdgcn_sqrtf(rbf(var + 1e-5f))));
      const float xn = rbf(blo(xe) - rbf(mean)) * rden;
      xp = pkbf(xn, xn);
    }
    asm volatile("" : "+v"(xp));
    SB();
    PAD(4);
    f32x4 qa = z4;
#define QA(i) qa = mfma(diag(dppu<BCAST + (i)>(xp), mk), wv[i], qa);
    QA(0) QA(1) QA(2) QA(3) QA(4) QA(5)
    asm volatile("" : "+v"(qa)); SB(); PAD(5);
    QA(6) QA(7) QA(8) QA(9) QA(10) QA(11)
    asm volatile("" : "+v"(qa)); SB(); PAD(6);
    QA(12) QA(13) QA(14) QA(15)
#undef QA
    ST128(x32_sum2(qa[0], qa[2]), x32_sum2(qa[1], qa[3]), stag, rs, o_a);
    {
      const unsigned now = (unsigned)__builtin_amdgcn_s_memrealtime(), tw = LDS_LD(&L.tw[c]);
      const float n = row_sum(tw ? 1.f : 0.f), d = row_sum(tw ? (float)(int)(now - tw) : 0.f);
      tanc = now - __builtin_amdgcn_readfirstlane((unsigned)(d * __builtin_amdgcn_rcpf(fmaxf(n, 1.f))));
      ST64(F(tanc), stag, rs, ((b >> 3) == LEAD && w == 1 && lane < 32) ? WS_T + lane * 128 + (b & 7) * 8 : OOB);
    }
  }
  PAD(8);
  asm volatile("" :: "v"(kt[0]));
  if (w >= 2) {
    tanc = (unsigned)__builtin_amdgcn_s_memrealtime() | 1u;
    L.tw[w] = tanc;
  }
  const unsigned tsp = TREL * 16 - (15 - w) * STG;
  unsigned te = tanc * 16 + tsp, dl = tanc * 16, stp = TREL * 16 * (1000 + KEPS) / 29000;
#define REANCHOR(R) do { const unsigned tr_ = __builtin_amdgcn_readfirstlane(LDS_LD(&L.tadj)); te = tr_ ? tr_ * 16 + tsp : te; \
    stp = (unsigned)max((int)(te - dl), 0) * (65536u / ((R) - 1)) >> 16; } while (0)
  asm volatile("" : "+s"(kvs), "+v"(kvv));
#pragma unroll
  for (int t = 2; t < TB; ++t) { if ((t & 1) == 0) PAD(8 + t / 2); PACE(); LDK(t); }
  if (__builtin_expect(w < 2, 0)) {
    LANE_VARS();
    if (w == 0) {
      u32x4 g[8];
      SPIN (unsigned spins = 0; spins < SPIN_CAP; ++spins) {
        bool ok = true;
#pragma unroll
        for (int i = 0; i < 8; ++i) g[i] = LD128(rs, WS_Q + h * 8192 + kvv + i * 1024, 0, SC1);
        SB();
#pragma unroll
        for (int i = 0; i < 8; ++i) ok &= (g[i][1] == stag) & (g[i][3] == stag);
        if (__all(ok)) break;
        asm volatile("s_sleep 8" ::: "memory");
      }
      float q0 = 0.f, q1 = 0.f;
#pragma unroll
      for (int i = 0; i < 8; ++i) { q0 += F(g[i][0]); q1 += F(g[i][2]); }
      L.qb[lane] = pkbf(q0, q1);
      if (lane == 0) L.tag = stag;
      __hip_atomic_store(&L.qready, 1u, __ATOMIC_RELEASE, __HIP_MEMORY_SCOPE_WORKGROUP);
      asm volatile("fz_last0:");
    } else {
      MASK8();
      const rsrc_t r2 = mk_rsrc(p2, (ch0 + nch) * 8192);
      u32x4 wv[6];
#pragma unroll
      for (int i = 0; i < 6; ++i) { wv[i] = LD128(r2, r8 * 1024 + c8 * 16, (ch0 + i) * 8192 + s * 128, NT); SB(); }
      u32x2 g[6], gt;
      SPIN (unsigned spins = 0; spins < SPIN_CAP; ++spins) {
        gt = LD64(rs, WS_T + h * 128 + c8 * 8, 0, SC1);
#pragma unroll
        for (int i = 0; i < 6; ++i) g[i] = LD64(rs, WS_H + h * 4096 + c8 * 512 + r8 * 8 + i * 64, 0, SC1);
        SB();
        bool ok = gt[1] == stag;
#pragma unroll
        for (int i = 0; i < 6; ++i) ok &= g[i][1] == stag;
        if (__all(ok)) break;
        asm volatile("s_sleep 8" ::: "memory");
      }
      {
        const float dt = (float)(int)(gt[0] - tanc);
        L.tadj = (tanc + (int)fminf(fmaxf(oct_sum(dt) * 0.0625f + oct_max(dt) * 0.5f, -300.f), 300.f)) | 1u;
      }
      f32x4 za = z4;
#pragma unroll
      for (int i = 0; i < 6; ++i) {
        float hv = silu(rbf(oct_sum(F(g[i][0]))));
        hv = i < nch ? hv : 0.f;
        za = mfma(diag(pkbf(hv, hv), m8), wv[i], za);
      }
#pragma unroll
      for (int j = 0; j < 4; ++j) { const float v = valid8 ? za[j] : 0.f; za[j] = v + dppf<ROR8>(v); }
      ST128(x32_sum2(za[0], za[2]), x32_sum2(za[1], za[3]), stag, rs, c < 8 ? WS_Z + (s * 32 + h) * 512 + (4 * c + 2 * (r & 1) + (r >> 1)) * 16 : OOB);
      asm volatile("fz_last1:");
    }
  }
  asm volatile("" : "+s"(kvs), "+v"(kvv));
#pragma unroll
  for (int t = TB; t < 16; ++t) { if ((t & 1) == 0) PAD(8 + t / 2); PACE(); LDK(t); }

  __builtin_amdgcn_s_setprio(2);
  REANCHOR(16);
  LDV(0); LDV(1); LDV(2); LDV(3);
  SPIN (unsigned spins = 0; LDS_LD(&L.qready) == 0u && spins < (SPIN_CAP << 6); ++spins) __builtin_amdgcn_s_sleep(2);
  MEMFENCE();
  const u32x4 qp = *(const u32x4*)&L.qb[4 * (lane & 15)];
  const unsigned tag = L.tag;
  float xs[16];
#pragma unroll
  for (int t = 0; t < 16; ++t) {
    float d = dot2(kt[t][0], qp[0], 0.f);
    d = dot2(kt[t][1], qp[1], d); d = dot2(kt[t][2], qp[2], d); d = dot2(kt[t][3], qp[3], d);
    xs[t] = d;
  }
  asm volatile("" :: "v"(pfv));
  LANE_VARS();
#define TR_STEP(HALF, CTRL, BIT) _Pragma("unroll") for (int i = 0; i < (HALF); ++i) { \
    const float t1 = xs[i] + dppf<CTRL>(xs[i]), t2 = xs[i + (HALF)] + dppf<CTRL>(xs[i + (HALF)]); xs[i] = (c & (BIT)) ? t2 : t1; }
  TR_STEP(8, ROR8, 8) TR_STEP(4, XOR7, 4) TR_STEP(2, XOR2, 2) TR_STEP(1, XOR1, 1)
#undef TR_STEP
  asm volatile("" : "+v"(xs[0]));
  REANCHOR(12);
  LDV(4);
  const float sc = rbf(rbf(xs[0]) * 0.08838834764831845f) * LOG2E;
  const float wm = wave_max(sc);
  const float p = __builtin_amdgcn_exp2f(sc - wm);
  {
    const float wsu = wave_sum(p);
    unsigned old = 0;
    if (lane == 0) {
      L.wmax[w] = wm; L.wsum[w] = wsu;
      old = __hip_atomic_fetch_add(&L.cnt, 1u, __ATOMIC_ACQ_REL, __HIP_MEMORY_SCOPE_WORKGROUP);
    }
    if (__builtin_amdgcn_readfirstlane(old) == 15u) {
      const float wmc = L.wmax[c];
      const float M = row_max(wmc);
      const float lse = M + __builtin_amdgcn_logf(row_sum(L.wsum[c] * __builtin_amdgcn_exp2f(wmc - M)));
      ST128(lse, lse, tag, rs, lane == 0 ? WS_F + h * 128 + s * 16 : OOB);
    }
  }
  unsigned pp = pkbf(p, p);
  asm volatile("" : "+v"(pp));
  __builtin_amdgcn_s_setprio(1);
  LDV(5);
  MASK16();
  MASK8();
  const int wc = w & 7;
  const bool fin = w == 5 && (h & 7) == s;
  int a_part = offsetof(Lds, part) + w * 512 + (8 * c + 4 * r) * 4;
  int a_mrg = offsetof(Lds, part) + r * 2048 + (32 * w + 2 * c) * 4;
  int a_yw = offsetof(Lds, ypart) + (2 * wc + (r >> 1)) * 256 + (8 * c8 + 4 * (r & 1)) * 4;
  int a_yr = offsetof(Lds, ypart) + r8 * 512 + (16 * w + 2 * c8) * 4;
  int o_o = WS_O + h * 8192 + c8 * 1024 + wc * 128 + r8 * 16;
  int o_pub = (r & 1) ? OOB : WS_O + (h * 8 + s) * 1024 + (32 * w + 2 * c + (r >> 1)) * 8;
  int o_y = (lane & 24) == 0 ? WS_Y + (s * 32 + h) * 512 + w * 128 + c8 * 16 + (r >> 1) * 8 : OOB;
  int o_fin = s * 16384 + (h >> 3) * 128 + r8 * 2048 + c8 * 16;
  int o_out = (lane & 24) == 0 ? 128 * s + 32 * (h >> 3) + 4 * c8 + 2 * (r >> 1) : OOB;
  int o_wo = r8 * 2048 + c8 * 16;
  int o_f = w < 4 ? WS_F + h * 128 + c8 * 16 : fin ? WS_Z + o_fin : w != 7 ? OOB
          : lane < 8 ? WS_O + (h * 8 + s) * 1024 + lane * 128 : lane < 12 ? WS_Y + (s * 32 + h) * 512 + (lane - 8) * 128 : OOB;
  asm volatile("" : "+v"(a_part), "+v"(a_mrg), "+v"(a_yw), "+v"(a_yr), "+v"(o_o), "+v"(o_pub), "+v"(o_y), "+v"(o_fin), "+v"(o_out), "+v"(o_wo), "+v"(o_f));
  asm volatile("" : "+v"(m8), "+v"(mk));
  char* const lds = (char*)&L;
  f32x4 oa = z4;
#define PV(T) do { oa = mfma(diag(dppu<BCAST + (T)>(pp), mk), vt[T], oa); asm volatile("" : "+v"(oa)); } while (0)
  LDV(6); PV(0);
  LDV(7); PV(1);
  LDV(8); PV(2);
  if (w == 6) asm volatile("s_mov_b64 exec, 0\n s_branch fz_hop0\n fz_ret:\n s_mov_b64 exec, -1" ::: "memory");
  LDV(9); PV(3);
  u32x4 gz[4];
  gz[0] = LD128(rs, o_f, 0, SC1);
  {
    const rsrc_t rz = mk_rsrc(pws, fin ? WS_END : 0);
#pragma unroll
    for (int i = 1; i < 4; ++i) gz[i] = LD128(rz, o_f + i * 512, 0, SC1);
  }
  const unsigned xo = __builtin_amdgcn_raw_buffer_load_b16(mk_rsrc(w == 4 ? po + h * (DH * 1024) : w == 6 ? pout : px, (fin || w == 4 || w == 6) ? 1024 : 0), o_out, 0, 0);
  SB();
  LDV(10); PV(4);
  __builtin_amdgcn_s_setprio(0);
  LDV(11); PV(5);
  LDV(12); PV(6);
  const bool bad1 = w < 4 && gz[0][1] != tag;
  const u32x2 gf2 = LD64(rs, bad1 ? o_f : OOB, 0, SC1);
  SB();
  LDV(13); PV(7);
  LDV(14);
  LDV(15);
  PV(8); PV(9); PV(10); PV(11); PV(12); PV(13);
  if (w >= 8) { dl = te + (15 - w) * STG + WOD * 16; PACE(); }
  const rsrc_t rwo = mk_rsrc(po, w >= 8 ? 4u << 20 : 0);
  const u32x4 wov0 = LD128(rwo, o_wo, (h * DH + 16 * wc) * 1024 + s * 128, NT);
  const u32x4 wov1 = LD128(rwo, o_wo + 1024, (h * DH + 16 * wc) * 1024 + s * 128, NT);
  SB();
  float gate = 0.f, xres = 0.f;
  f32x4 fw = z4;
  if (w < 4) {
    u32x2 gf = bad1 ? gf2 : u32x2{gz[0][0], gz[0][1]};
    if (__any(gf[1] != tag)) {
      SPIN (unsigned spins = 0; spins < SPIN_CAP; ++spins) {
        MEMFENCE();
        gf = LD64(rs, o_f, 0, SC1);
        if (!__any(gf[1] != tag)) break;
        __builtin_amdgcn_s_sleep(4);
      }
    }
    MEMFENCE();
    fw = *(const f32x4*)&L.wmax[4 * r];
    const float ls = F(gf[0]);
    const float MM = oct_max(ls);
    const float lh = MM + __builtin_amdgcn_logf(oct_sum(__builtin_amdgcn_exp2f(ls - MM)));
#pragma unroll
    for (int i = 0; i < 4; ++i) fw[i] = __builtin_amdgcn_exp2f(fw[i] - lh);
  } else if (fin) {
    bool bad = false;
#pragma unroll
    for (int i = 0; i < 4; ++i) bad |= (gz[i][1] != tag) | (gz[i][3] != tag);
    SPIN (unsigned spins = 0; __any(bad) && spins < SPIN_CAP; ++spins) {
      __builtin_amdgcn_s_sleep(4);
      MEMFENCE();
      bad = false;
#pragma unroll
      for (int i = 0; i < 4; ++i) gz[i] = LD128(rs, o_f + i * 512, 0, SC1);
#pragma unroll
      for (int i = 0; i < 4; ++i) bad |= (gz[i][1] != tag) | (gz[i][3] != tag);
    }
    gate = rbf(silu(rbf(col_sum((F(gz[0][0]) + F(gz[1][0])) + (F(gz[2][0]) + F(gz[3][0])), (F(gz[0][2]) + F(gz[1][2])) + (F(gz[2][2]) + F(gz[3][2]))))));
    xres = blo(xo);
    o_fin += WS_Y;
    asm volatile("" : "+v"(gate), "+v"(xres), "+v"(o_fin));
  }
  asm volatile("" :: "v"(gz[0]), "v"(xo));
  PV(14); PV(15);
#undef PV
  if (lane < 32) *(f32x4*)(lds + a_part) = oa;
  BARRIER();
  if (w < 4) {
    const float* src = (const float*)(lds + a_mrg);
    const f32x2 v0 = *(const f32x2*)src, v1 = *(const f32x2*)(src + 128), v2 = *(const f32x2*)(src + 256), v3 = *(const f32x2*)(src + 384);
    HOP(0, "fz_hop2");
    const f32x2 av = (fw[0] * v0 + fw[1] * v1) + (fw[2] * v2 + fw[3] * v3);
    ST64(x16_sum(x32_sum2(av[0], av[1])), tag, rs, o_pub);
  } else if (w >= 8) {
    u32x4 go;
    unsigned cnt_;
    asm volatile("s_sleep %[dl]\n s_mov_b32 %[cnt], 0x40000\n s_branch 2f\n"
                 "fz_hop2: s_branch fz_hop8\n"
                 "1: s_sub_u32 %[cnt], %[cnt], 1\n s_cbranch_scc1 3f\n"
                 "2: buffer_load_dwordx4 v[64:67], %[off], %[rs], 0 offen sc1\n s_waitcnt vmcnt(0)\n"
                 " v_cmp_ne_u32 vcc, v65, %[tag]\n v_cmp_ne_u32 s[90:91], v67, %[tag]\n s_or_b64 exec, vcc, s[90:91]\n s_cbranch_scc1 1b\n"
                 "3: s_mov_b64 exec, -1\n"
                 " v_add_f32_dpp v64, v64, v64 quad_perm:[1,0,3,2] row_mask:0xf bank_mask:0xf\n v_add_f32_dpp v66, v66, v66 quad_perm:[1,0,3,2] row_mask:0xf bank_mask:0xf\n s_nop 0\n"
                 " v_add_f32_dpp v64, v64, v64 quad_perm:[2,3,0,1] row_mask:0xf bank_mask:0xf\n v_add_f32_dpp v66, v66, v66 quad_perm:[2,3,0,1] row_mask:0xf bank_mask:0xf\n s_nop 0\n"
                 " v_add_f32_dpp v64, v64, v64 row_half_mirror row_mask:0xf bank_mask:0xf\n v_add_f32_dpp v66, v66, v66 row_half_mirror row_mask:0xf bank_mask:0xf\n"
                 " v_cvt_pk_bf16_f32 v64, v64, v64\n v_cvt_pk_bf16_f32 v66, v66, v66"
                 : "=&{v[64:67]}"(go), [cnt] "=&s"(cnt_) : [off] "v"(o_o), [rs] "s"(rs), [tag] "v"(tag), [dl] "i"(DL1)
                 : "scc", "vcc", "s90", "s91", "memory");
    f32x4 y = mfma(diag(go[0], m8), wov0, z4);
    y = mfma(diag(go[2], m8), wov1, y);
    HOP(8, "fz_hop5");
    if (valid8) *(f32x4*)(lds + a_yw) = y;
  }
  BARRIER();
  if (w < 4) {
    const float* src = (const float*)(lds + a_yr);
    const f32x2 a = *(const f32x2*)src + *(const f32x2*)(src + 64);
    ST64(col_sum(a[0], a[1]), tag, rs, o_y);
  } else if (fin) {
    HOP(5, "fz_hop6");
    __builtin_amdgcn_s_sleep(DL2);
    float va, vb, vc2, vd;
    u32x16 keepA, keepB;
    unsigned cnt_;
#define SWEEP(B0, B1, B2, B3) "buffer_load_dwordx4 " B0 ", %[off], %[rs], 0 offen sc1\n buffer_load_dwordx4 " B1 ", %[off], %[rs], 0 offen offset:512 sc1\n" \
    " buffer_load_dwordx4 " B2 ", %[off], %[rs], 0 offen offset:1024 sc1\n buffer_load_dwordx4 " B3 ", %[off], %[rs], 0 offen offset:1536 sc1\n"
#define SWEEP_A SWEEP("v[64:67]", "v[68:71]", "v[72:75]", "v[76:79]")
#define SWEEP_B SWEEP("v[80:83]", "v[84:87]", "v[88:91]", "v[92:95]")
#define CK(v) " v_cmpx_eq_u32 " v ", %[tag]\n"
#define CK_END " s_cmp_eq_u64 exec, -1\n s_mov_b64 exec, -1\n"
    asm volatile(SWEEP_A " s_sleep 5\n s_mov_b32 %[cnt], 0x40000\n"
      "1: " SWEEP_B " s_waitcnt vmcnt(4)\n" CK("v65") CK("v67") CK("v69") CK("v71") CK("v73") CK("v75") CK("v77") CK("v79") CK_END " s_cbranch_scc1 2f\n"
      SWEEP_A "fz_hop6: s_cbranch_execz fz_hop9\n s_waitcnt vmcnt(4)\n" CK("v81") CK("v83") CK("v85") CK("v87") CK("v89") CK("v91") CK("v93") CK("v95") CK_END " s_cbranch_scc1 3f\n"
      " s_sub_u32 %[cnt], %[cnt], 1\n s_cmp_lg_u32 %[cnt], 0\n s_cbranch_scc1 1b\n"
      "3: v_add_f32 %[a], v80, v84\n v_add_f32 %[b], v82, v86\n v_add_f32 %[c], v88, v92\n v_add_f32 %[d], v90, v94\n s_branch 4f\n"
      "fz_hop9: s_branch fz_hop7\n"
      "2: v_add_f32 %[a], v64, v68\n v_add_f32 %[b], v66, v70\n v_add_f32 %[c], v72, v76\n v_add_f32 %[d], v74, v78\n 4:"
      : [a] "=&v"(va), [b] "=&v"(vb), [c] "=&v"(vc2), [d] "=&v"(vd), [cnt] "=&s"(cnt_), "=&{v[64:79]}"(keepA), "=&{v[80:95]}"(keepB)
      : [off] "v"(o_fin), [rs] "s"(rs), [tag] "v"(tag) : "scc", "vcc", "memory");
    const float ov = xres + rbf(rbf(col_sum(va + vc2, vb + vd)) * gate);
    __builtin_amdgcn_raw_buffer_store_b16((u16)pkbf(ov, ov), mk_rsrc(pout, 1024), o_out, 0, SC1);
    HOP(7, "fz_ret");
    asm volatile("fz_last:");
    __builtin_amdgcn_raw_buffer_store_b32(tag, rs, (h == 0 && lane == 0) ? WS_EPOCH : OOB, 0, SC1);
    asm volatile("" :: "v"(keepA), "v"(keepB));
  }
}

extern "C" void kernel_launch(void* const* d_in, const int* in_sizes, int n_in, void* d_out, int out_size, void* d_ws, size_t ws_size, hipStream_t stream) {
  fused<<<256, 1024, 0, stream>>>((const u16*)d_in[1], (const u16*)d_in[2], (const u16*)d_in[0], (const u16*)d_in[3], (const u16*)d_in[6],
                                  (unsigned char*)d_ws, (const u16*)d_in[4], (const u16*)d_in[7], (u16*)d_out);
}
```

```cpp
#include <hip/hip_runtime.h>
#include <stdint.h>
#include <stddef.h>

typedef unsigned short u16;
typedef unsigned u32x2 __attribute__((ext_vector_type(2)));
typedef unsigned u32x4 __attribute__((ext_vector_type(4)));
typedef unsigned u32x16 __attribute__((ext_vector_type(16)));
typedef unsigned long long u64;
typedef unsigned long long u64x8 __attribute__((ext_vector_type(8)));
typedef float f32x2 __attribute__((ext_vector_type(2)));
typedef float f32x4 __attribute__((ext_vector_type(4)));
typedef __bf16 bf16x2 __attribute__((ext_vector_type(2)));
typedef __bf16 bf16x8 __attribute__((ext_vector_type(8)));
typedef __amdgpu_buffer_rsrc_t rsrc_t;

constexpr unsigned TREL = 1775;
constexpr unsigned KEPS = 48;
constexpr unsigned STG = 58;
constexpr unsigned WOD = 32;
constexpr int DL1 = 14, DL2 = 8, ZSL = 20;

constexpr int D = 512, SK = 8192, DH = 128, FF = 1376;
constexpr float LOG2E = 1.4426950408889634f;
constexpr int NT = 2, SC1 = 16;
constexpr int OOB = 0x40000000;
constexpr int TB = 12;
constexpr int LEAD = 21;
constexpr unsigned SPIN_CAP = 1u << 18;
constexpr unsigned WS_EPOCH = 0;
constexpr unsigned WS_T = 4096;
constexpr unsigned WS_Q = 8192;
constexpr unsigned WS_H = WS_Q + 128 * 1024;
constexpr unsigned WS_F = WS_H + 128 * 512;
constexpr unsigned WS_O = WS_F + 32 * 128;
constexpr unsigned WS_Y = WS_O + 256 * 1024;
constexpr unsigned WS_Z = WS_Y + 256 * 512;
constexpr unsigned WS_END = WS_Z + 256 * 512;

__device__ __forceinline__ float rbf(float f) { return (float)(__bf16)f; }
__device__ __forceinline__ float blo(unsigned u) { return __uint_as_float(u << 16); }
__device__ __forceinline__ float bhi(unsigned u) { return __uint_as_float(u & 0xffff0000u); }
__device__ __forceinline__ unsigned pkbf(float a, float b) { bf16x2 v = {(__bf16)a, (__bf16)b}; return __builtin_bit_cast(unsigned, v); }
__device__ __forceinline__ float dot2(unsigned a, unsigned b, float acc) {
  return __builtin_amdgcn_fdot2_f32_bf16(__builtin_bit_cast(bf16x2, a), __builtin_bit_cast(bf16x2, b), acc, false);
}
template <int CTRL> __device__ __forceinline__ float dppf(float x) {
  return __builtin_bit_cast(float, __builtin_amdgcn_mov_dpp(__builtin_bit_cast(int, x), CTRL, 0xf, 0xf, true));
}
template <int CTRL> __device__ __forceinline__ unsigned dppu(unsigned x) {
  return (unsigned)__builtin_amdgcn_mov_dpp((int)x, CTRL, 0xf, 0xf, true);
}
constexpr int XOR1 = 0xB1, XOR2 = 0x4E, XOR7 = 0x141, ROR8 = 0x128, BCAST = 0x150;
__device__ __forceinline__ float x16_sum(float x) {
  auto s = __builtin_amdgcn_permlane16_swap(__float_as_uint(x), __float_as_uint(x), false, false);
  return __uint_as_float(s[0]) + __uint_as_float(s[1]);
}
__device__ __forceinline__ float x32_sum2(float a, float b) {
  auto t = __builtin_amdgcn_permlane32_swap(__float_as_uint(a), __float_as_uint(b), false, false);
  return __uint_as_float(t[0]) + __uint_as_float(t[1]);
}
__device__ __forceinline__ float xrow_max(float x) {
  auto s = __builtin_amdgcn_permlane16_swap(__float_as_uint(x), __float_as_uint(x), false, false);
  x = fmaxf(__uint_as_float(s[0]), __uint_as_float(s[1]));
  auto t = __builtin_amdgcn_permlane32_swap(__float_as_uint(x), __float_as_uint(x), false, false);
  return fmaxf(__uint_as_float(t[0]), __uint_as_float(t[1]));
}
__device__ __forceinline__ float oct_sum(float x) { x += dppf<XOR1>(x); x += dppf<XOR2>(x); x += dppf<XOR7>(x); return x; }
__device__ __forceinline__ float oct_max(float x) { x = fmaxf(x, dppf<XOR1>(x)); x = fmaxf(x, dppf<XOR2>(x)); x = fmaxf(x, dppf<XOR7>(x)); return x; }
__device__ __forceinline__ float row_sum(float x) { x = oct_sum(x); x += dppf<ROR8>(x); return x; }
__device__ __forceinline__ float row_max(float x) { x = oct_max(x); x = fmaxf(x, dppf<ROR8>(x)); return x; }
__device__ __forceinline__ float wave_sum(float x) { x = x16_sum(row_sum(x)); return x32_sum2(x, x); }
__device__ __forceinline__ float wave_max(float x) { return xrow_max(row_max(x)); }
__device__ __forceinline__ float col_sum(float a, float b) { const float v = x16_sum(x32_sum2(a, b)); return v + dppf<ROR8>(v); }
__device__ __forceinline__ f32x4 mfma(u32x4 a, u32x4 b, f32x4 c) {
  return __builtin_amdgcn_mfma_f32_16x16x32_bf16(__builtin_bit_cast(bf16x8, a), __builtin_bit_cast(bf16x8, b), c, 0, 0, 0);
}
__device__ __forceinline__ u32x4 diag(unsigned pk, const u32x4& m) { return u32x4{pk & m[0], pk & m[1], pk & m[2], pk & m[3]}; }
__device__ __forceinline__ float silu(float z) { return z * __builtin_amdgcn_rcpf(1.f + __builtin_amdgcn_exp2f(-z * LOG2E)); }
__device__ __forceinline__ rsrc_t mk_rsrc(u64 p, unsigned bytes) { return __builtin_amdgcn_make_buffer_rsrc((void*)p, 0, bytes, 0x00020000); }
#define F(u) __uint_as_float(u)
#define LD128(rs, vo, so, aux) __builtin_amdgcn_raw_buffer_load_b128(rs, vo, so, aux)
#define LD64(rs, vo, so, aux) __builtin_amdgcn_raw_buffer_load_b64(rs, vo, so, aux)
#define ST128(a, b, tag, rs, vo) __builtin_amdgcn_raw_buffer_store_b128((u32x4{__float_as_uint(a), tag, __float_as_uint(b), tag}), rs, vo, 0, SC1)
#define ST64(a, tag, rs, vo) __builtin_amdgcn_raw_buffer_store_b64((u32x2{__float_as_uint(a), tag}), rs, vo, 0, SC1)
#define LDS_LD(p) __hip_atomic_load(p, __ATOMIC_RELAXED, __HIP_MEMORY_SCOPE_WORKGROUP)
#define SB() __builtin_amdgcn_sched_barrier(0)
#define MEMFENCE() asm volatile("" ::: "memory")
#define BARRIER() asm volatile("s_waitcnt lgkmcnt(0)\n s_barrier" ::: "memory")
#define SPIN _Pragma("nounroll") for
#define PACE() asm volatile("1: s_memrealtime s[90:91]\n s_waitcnt lgkmcnt(0)\n s_lshl_b32 s90, s90, 4\n s_sub_u32 s90, s90, %0\n s_cmp_lt_i32 s90, 0\n" \
                            " s_cbranch_scc0 2f\n s_sleep 1\n s_branch 1b\n 2: s_add_u32 %0, %0, %1" : "+s"(dl) : "s"(stp) : "scc", "s90", "s91")
#define PAD(i) asm volatile("fz_w%c0: s_cbranch_execz fz_wret" :: "i"(i))
#define HOP(i, nx) asm volatile("fz_hop" #i ": s_cbranch_execz " nx)

struct Lds {
  float part[16][128];
  float ypart[16][64];
  float junk[128];
  float z[64];
  float wmax[16];
  float wsum[16];
  unsigned qb[64];
  unsigned tw[16];
  unsigned qready, cnt, tadj, half, go, tag;
  float lse, wgt[8];
};

__global__ void __launch_bounds__(1024, 4) fused(const u16* kc, const u16* vc, const u16* x, const u16* wq, const u16* w1,
                                                  unsigned char* ws, const u16* wo, const u16* w2, u16* out) {
  __shared__ __attribute__((aligned(16))) Lds L;
  const int b = blockIdx.x;
  const int h = (b & 7) * 4 + (b >> 6), s = (b >> 3) & 7;
  const int lane = threadIdx.x & 63, w = __builtin_amdgcn_readfirstlane(threadIdx.x >> 6);
  u64x8 ka; u64 pout;
  asm volatile("s_load_dwordx16 %0, %2, 0x0\n s_load_dwordx2 %1, %2, 0x40\n"
               " s_mov_b64 exec, 0\n s_getpc_b64 s[90:91]\n s_lshl_b32 s92, %3, 2\n s_add_u32 s90, s90, s92\n s_addc_u32 s91, s91, 0\n"
               " s_add_u32 s90, s90, 24\n s_addc_u32 s91, s91, 0\n s_setpc_b64 s[90:91]\n"
               " s_branch fz_w0\n s_branch fz_w1\n s_branch fz_w2\n s_branch fz_w3\n s_branch fz_w4\n s_branch fz_w5\n s_branch fz_w6\n s_branch fz_w7\n"
               " s_branch fz_w8\n s_branch fz_w9\n s_branch fz_w10\n s_branch fz_w11\n s_branch fz_w12\n s_branch fz_w13\n s_branch fz_w14\n s_branch fz_w15\n"
               "fz_wret: s_mov_b64 exec, -1\n s_waitcnt lgkmcnt(0)"
               : "=&s"(ka), "=&s"(pout) : "s"(__builtin_amdgcn_kernarg_segment_ptr()), "s"(w) : "s90", "s91", "s92", "scc");
  const u64 pk = ka[0], pv = ka[1], px = ka[2], pq = ka[3], p1 = ka[4], pws = ka[5], po = ka[6], p2 = ka[7];
  const rsrc_t rk = mk_rsrc(pk, 64u << 20), rv = mk_rsrc(pv, 64u << 20), rs = mk_rsrc(pws, WS_END);
  const f32x4 z4 = {0.f, 0.f, 0.f, 0.f};
  int kvs = h * (SK * 256) + s * 16384 + w * 1024, kvv = lane * 16;
#define TOFF(t) (kvs + (((t) + h) & 15) * 131072)
#define LDK(t) do { kt[t] = LD128(rk, kvv, TOFF(t), NT); SB(); } while (0)
#define LDV(t) do { PACE(); vt[t] = LD128(rv, kvv, TOFF(t), NT); SB(); } while (0)
#define LANE_VARS() int ln_ = lane; asm volatile("" : "+v"(ln_)); const int r = ln_ >> 4, c = ln_ & 15, r8 = ln_ >> 3, c8 = ln_ & 7; (void)r; (void)c; (void)r8; (void)c8
#define MASK16() u32x4 mk; _Pragma("unroll") for (int i = 0; i < 4; ++i) mk[i] = ((c >> 1) == i) ? ((c & 1) ? 0xffff0000u : 0x0000ffffu) : 0u
#define MASK8() u32x4 m8; _Pragma("unroll") for (int i = 0; i < 4; ++i) m8[i] = ((c8 >> 1) == i) ? ((c8 & 1) ? 0xffff0000u : 0x0000ffffu) : 0u; const bool valid8 = (r >> 1) == (c >> 3)
  u32x4 kt[16], vt[16];
  __builtin_amdgcn_s_setprio(3);
  LDK(0); LDK(1);
  PAD(7);
  if (w == 2 && lane < 21) ((unsigned*)((char*)&L + offsetof(Lds, tw)))[lane] = 0u;
  BARRIER();
  unsigned pfv;
  {
    u64 pc; unsigned len;
    asm volatile("s_getpc_b64 %0\n fz_pc: s_mov_b32 %1, fz_last-fz_pc\n s_max_i32 %1, %1, fz_last0-fz_pc\n s_max_i32 %1, %1, fz_last1-fz_pc" : "=s"(pc), "=s"(len) :: "scc");
    pfv = __builtin_amdgcn_raw_buffer_load_b32(mk_rsrc(pc, len), lane * 64, (15 - w) * 4096, 0);
    SB();
  }
  const int nch = 5 + (h < 12), ch0 = 5 * h + min(h, 12);
  unsigned stag = 0, tanc = 0;
  if (__builtin_expect(w < 2, 0)) {
    PAD(0);
    const bool r0 = s < 4;
    const int kb = (s & 3) * 2 + w;
    int sa0 = kb * (64 * 8192) + h * 256, sa1 = kb * (64 * FF * 2) + ch0 * 16;
    int ob0 = WS_Q + (h * 4 + s) * 1024, ob1 = WS_H + (h * 4 + s - 4) * 512;
    asm volatile("" : "+s"(sa0), "+s"(sa1), "+s"(ob0), "+s"(ob1));
    const int sa = r0 ? sa0 : sa1, da = r0 ? 8192 : FF * 2;
    {
      u64 ta0 = pv + kvs, ta1 = po + h * (DH * 1024), tc1 = p2 + ch0 * 8192;
      asm volatile("" : "+s"(ta0), "+s"(ta1), "+s"(tc1));
      asm volatile("s_load_dword s92, %0, 0x0\n s_load_dword s93, %1, 0x0\n s_load_dword s94, %2, 0x0\n s_load_dword s95, %3, 0x0"
                   :: "s"((r0 ? pq : p1) + sa), "s"(w == 0 ? pws : px), "s"(w == 0 ? ta0 : ta1), "s"(w == 0 ? pout : tc1) : "s92", "s93", "s94", "s95");
    }
    const rsrc_t rx = mk_rsrc(px, 1024), ra = mk_rsrc(r0 ? pq : p1, r0 ? 4u << 20 : 512 * FF * 2);
    LANE_VARS();
    const int va = c < (r0 ? 16 : nch) ? r * da + c * 16 : OOB;
    asm volatile("s_waitcnt lgkmcnt(0)" ::: "memory");
    stag = __builtin_amdgcn_raw_buffer_load_b32(rs, WS_EPOCH, 0, SC1);
    const u32x4 xa = LD128(rx, kvv, 0, 0);
    const unsigned xe = __builtin_amdgcn_raw_buffer_load_b16(rx, (4 * c + r) * 2, kb * 128, 0);
    u32x4 wv[16];
    SB();
    PAD(1);
#pragma unroll
    for (int i = 0; i < 16; ++i) { wv[i] = LD128(ra, va, sa + i * 4 * da, NT); SB(); }
    PAD(2);
    SB();
    MASK16();
    int o_a = (r0 ? ob0 : ob1) + (4 * c + 2 * (r & 1) + (r >> 1)) * 16;
    o_a = (r0 || c < 6) ? o_a : OOB;
    asm volatile("" : "+v"(stag), "+v"(o_a), "+v"(mk));
    stag += 1u;
    unsigned xp;
    {
      float xv[8];
#pragma unroll
      for (int j = 0; j < 4; ++j) { xv[2 * j] = blo(xa[j]); xv[2 * j + 1] = bhi(xa[j]); }
      float mean = wave_sum(((xv[0] + xv[1]) + (xv[2] + xv[3])) + ((xv[4] + xv[5]) + (xv[6] + xv[7]))) * (1.f / D);
      asm volatile("" : "+v"(mean));
      PAD(3);
      float sq = 0.f;
#pragma unroll
      for (int j = 0; j < 8; ++j) { const float d = xv[j] - mean; sq = fmaf(d, d, sq); }
      const float var = rbf(wave_sum(sq) * (1.f / D));
      const float rden = __builtin_amdgcn_rcpf(rbf(__builtin_amdgcn_sqrtf(rbf(var + 1e-5f))));
      const float xn = rbf(blo(xe) - rbf(mean)) * rden;
      xp = pkbf(xn, xn);
    }
    asm volatile("" : "+v"(xp));
    SB();
    PAD(4);
    f32x4 qa = z4;
#define QA(i) qa = mfma(diag(dppu<BCAST + (i)>(xp), mk), wv[i], qa);
    QA(0) QA(1) QA(2) QA(3) QA(4) QA(5)
    asm volatile("" : "+v"(qa)); SB(); PAD(5);
    QA(6) QA(7) QA(8) QA(9) QA(10) QA(11)
    asm volatile("" : "+v"(qa)); SB(); PAD(6);
    QA(12) QA(13) QA(14) QA(15)
#undef QA
    const f32x2 pa = {x32_sum2(qa[0], qa[2]), x32_sum2(qa[1], qa[3])};
    if (w == 1) {
      *(f32x2*)&L.junk[2 * lane] = pa;
      __hip_atomic_store(&L.half, 1u, __ATOMIC_RELEASE, __HIP_MEMORY_SCOPE_WORKGROUP);
    } else {
      SPIN (unsigned spins = 0; LDS_LD(&L.half) == 0u && spins < (SPIN_CAP << 6); ++spins) __builtin_amdgcn_s_sleep(1);
      MEMFENCE();
      const f32x2 pb = *(const f32x2*)&L.junk[2 * lane];
      ST128(pa[0] + pb[0], pa[1] + pb[1], stag, rs, o_a);
    }
    {
      const unsigned now = (unsigned)__builtin_amdgcn_s_memrealtime(), tw = LDS_LD(&L.tw[c]);
      const float n = row_sum(tw ? 1.f : 0.f), d = row_sum(tw ? (float)(int)(now - tw) : 0.f);
      tanc = now - __builtin_amdgcn_readfirstlane((unsigned)(d * __builtin_amdgcn_rcpf(fmaxf(n, 1.f))));
      ST64(F(tanc), stag, rs, ((b >> 3) == LEAD && w == 1 && lane < 8) ? WS_T + lane * 128 + (b & 7) * 8 : OOB);
    }
  }
  PAD(8);
  asm volatile("" :: "v"(kt[0]));
  if (w >= 2) {
    tanc = (unsigned)__builtin_amdgcn_s_memrealtime() | 1u;
    L.tw[w] = tanc;
  }
  const unsigned tsp = TREL * 16 - (15 - w) * STG;
  unsigned te = tanc * 16 + tsp, dl = tanc * 16, stp = TREL * 16 * (1000 + KEPS) / 29000;
#define REANCHOR(R) do { const unsigned tr_ = __builtin_amdgcn_readfirstlane(LDS_LD(&L.tadj)); te = tr_ ? tr_ * 16 + tsp : te; \
    stp = (unsigned)max((int)(te - dl), 0) * (65536u / ((R) - 1)) >> 16; } while (0)
  asm volatile("" : "+s"(kvs), "+v"(kvv));
#pragma unroll
  for (int t = 2; t < TB; ++t) { if ((t & 1) == 0) PAD(8 + t / 2); PACE(); LDK(t); }
  if (__builtin_expect(w < 2, 0)) {
    LANE_VARS();
    if (w == 0) {
      u32x4 g[4];
      SPIN (unsigned spins = 0; spins < SPIN_CAP; ++spins) {
        bool ok = true;
#pragma unroll
        for (int i = 0; i < 4; ++i) g[i] = LD128(rs, WS_Q + h * 4096 + kvv + i * 1024, 0, SC1);
        SB();
#pragma unroll
        for (int i = 0; i < 4; ++i) ok &= (g[i][1] == stag) & (g[i][3] == stag);
        if (__all(ok)) break;
        asm volatile("s_sleep 8" ::: "memory");
      }
      float q0 = 0.f, q1 = 0.f;
#pragma unroll
      for (int i = 0; i < 4; ++i) { q0 += F(g[i][0]); q1 += F(g[i][2]); }
      L.qb[lane] = pkbf(q0, q1);
      if (lane == 0) L.tag = stag;
      __hip_atomic_store(&L.qready, 1u, __ATOMIC_RELEASE, __HIP_MEMORY_SCOPE_WORKGROUP);
      asm volatile("fz_last0:");
    } else {
      MASK8();
      const rsrc_t r2 = mk_rsrc(p2, (ch0 + nch) * 8192);
      u32x4 wv[6];
#pragma unroll
      for (int i = 0; i < 6; ++i) { wv[i] = LD128(r2, r8 * 1024 + c8 * 16, (ch0 + i) * 8192 + s * 128, NT); SB(); }
      u32x2 g[6], gt;
      SPIN (unsigned spins = 0; spins < SPIN_CAP; ++spins) {
        gt = LD64(rs, WS_T + (h & 7) * 128 + c8 * 8, 0, SC1);
#pragma unroll
        for (int i = 0; i < 6; ++i) g[i] = LD64(rs, WS_H + h * 2048 + (c8 & 3) * 512 + r8 * 8 + i * 64, 0, SC1);
        SB();
        bool ok = gt[1] == stag;
#pragma unroll
        for (int i = 0; i < 6; ++i) ok &= g[i][1] == stag;
        if (__all(ok)) break;
        asm volatile("s_sleep 8" ::: "memory");
      }
      {
        const float dt = (float)(int)(gt[0] - tanc);
        L.tadj = (tanc + (int)fminf(fmaxf(oct_sum(dt) * 0.0625f + oct_max(dt) * 0.5f, -300.f), 300.f)) | 1u;
      }
      f32x4 za = z4;
#pragma unroll
      for (int i = 0; i < 6; ++i) {
        float hv = F(g[i][0]);
        hv += dppf<XOR1>(hv); hv += dppf<XOR2>(hv);
        hv = silu(rbf(hv));
        hv = i < nch ? hv : 0.f;
        za = mfma(diag(pkbf(hv, hv), m8), wv[i], za);
      }
#pragma unroll
      for (int j = 0; j < 4; ++j) { const float v = valid8 ? za[j] : 0.f; za[j] = v + dppf<ROR8>(v); }
      const f32x2 zz = {x32_sum2(za[0], za[2]), x32_sum2(za[1], za[3])};
      if (c < 8) *(f32x2*)&L.z[8 * c + 4 * (r & 1) + 2 * (r >> 1)] = zz;
      asm volatile("fz_last1:");
    }
  }
  asm volatile("" : "+s"(kvs), "+v"(kvv));
#pragma unroll
  for (int t = TB; t < 16; ++t) { if ((t & 1) == 0) PAD(8 + t / 2); PACE(); LDK(t); }

  __builtin_amdgcn_s_setprio(2);
  REANCHOR(16);
  LDV(0); LDV(1); LDV(2); LDV(3);
  SPIN (unsigned spins = 0; LDS_LD(&L.qready) == 0u && spins < (SPIN_CAP << 6); ++spins) __builtin_amdgcn_s_sleep(2);
  MEMFENCE();
  const u32x4 qp = *(const u32x4*)&L.qb[4 * (lane & 15)];
  const unsigned tag = L.tag;
  float xs[16];
#pragma unroll
  for (int t = 0; t < 16; ++t) {
    float d = dot2(kt[t][0], qp[0], 0.f);
    d = dot2(kt[t][1], qp[1], d); d = dot2(kt[t][2], qp[2], d); d = dot2(kt[t][3], qp[3], d);
    xs[t] = d;
  }
  asm volatile("" :: "v"(pfv));
  LANE_VARS();
#define TR_STEP(HALF, CTRL, BIT) _Pragma("unroll") for (int i = 0; i < (HALF); ++i) { \
    const float t1 = xs[i] + dppf<CTRL>(xs[i]), t2 = xs[i + (HALF)] + dppf<CTRL>(xs[i + (HALF)]); xs[i] = (c & (BIT)) ? t2 : t1; }
  TR_STEP(8, ROR8, 8) TR_STEP(4, XOR7, 4) TR_STEP(2, XOR2, 2) TR_STEP(1, XOR1, 1)
#undef TR_STEP
  asm volatile("" : "+v"(xs[0]));
  REANCHOR(12);
  LDV(4);
  const float sc = rbf(rbf(xs[0]) * 0.08838834764831845f) * LOG2E;
  const float wm = wave_max(sc);
  const float p = __builtin_amdgcn_exp2f(sc - wm);
  {
    const float wsu = wave_sum(p);
    unsigned old = 0;
    if (lane == 0) {
      L.wmax[w] = wm; L.wsum[w] = wsu;
      old = __hip_atomic_fetch_add(&L.cnt, 1u, __ATOMIC_ACQ_REL, __HIP_MEMORY_SCOPE_WORKGROUP);
    }
    if (__builtin_amdgcn_readfirstlane(old) == 15u) {
      const float wmc = L.wmax[c];
      const float M = row_max(wmc);
      const float lse = M + __builtin_amdgcn_logf(row_sum(L.wsum[c] * __builtin_amdgcn_exp2f(wmc - M)));
      ST128(lse, lse, tag, rs, lane == 0 ? WS_F + h * 128 + s * 16 : OOB);
      L.lse = lse;
      __hip_atomic_store(&L.cnt, 17u, __ATOMIC_RELEASE, __HIP_MEMORY_SCOPE_WORKGROUP);
    }
  }
  unsigned pp = pkbf(p, p);
  asm volatile("" : "+v"(pp));
  __builtin_amdgcn_s_setprio(1);
  LDV(5);
  MASK16();
  MASK8();
  const int wc = w & 7;
  const bool fin = w == 5 && (h & 7) == s;
  int a_part = r < 2 ? offsetof(Lds, part) + w * 512 + (8 * c + 4 * r) * 4 : offsetof(Lds, junk) + (lane - 32) * 16;
  int a_mrg = offsetof(Lds, part) + r * 2048 + (32 * w + 2 * c) * 4;
  int a_yw = valid8 ? offsetof(Lds, ypart) + (2 * wc + (r >> 1)) * 256 + (8 * c8 + 4 * (r & 1)) * 4 : offsetof(Lds, junk) + (r * 8 + c8) * 16;
  int a_yr = offsetof(Lds, ypart) + r8 * 512 + (16 * w + 2 * c8) * 4;
  int o_o = WS_O + h * 8192 + c8 * 1024 + wc * 128 + r8 * 16;
  int o_pub = (r & 1) ? OOB : WS_O + (h * 8 + s) * 1024 + (32 * w + 2 * c + (r >> 1)) * 8;
  int o_y = (lane & 24) == 0 ? WS_Y + (s * 32 + h) * 512 + w * 128 + c8 * 16 + (r >> 1) * 8 : OOB;
  int o_fin = s * 16384 + (h >> 3) * 128 + r8 * 2048 + c8 * 16;
  int o_out = (lane & 24) == 0 ? 128 * s + 32 * (h >> 3) + 4 * c8 + 2 * (r >> 1) : OOB;
  int o_wo = r8 * 2048 + c8 * 16;
  int o_f = w == 4 ? WS_F + h * 128 + c8 * 16 : OOB;
  const int o_pt = w != 7 ? OOB : lane < 8 ? WS_O + (h * 8 + s) * 1024 + lane * 128 : lane < 16 ? (lane < 12 ? WS_Y : WS_Z) + (s * 32 + h) * 512 + (lane & 3) * 128 : OOB;
  asm volatile("" : "+v"(a_part), "+v"(a_mrg), "+v"(a_yw), "+v"(a_yr), "+v"(o_o), "+v"(o_pub), "+v"(o_y), "+v"(o_fin), "+v"(o_out), "+v"(o_wo), "+v"(o_f));
  asm volatile("" : "+v"(m8), "+v"(mk));
  char* const lds = (char*)&L;
  f32x4 oa = z4;
#define PV(T) do { oa = mfma(diag(dppu<BCAST + (T)>(pp), mk), vt[T], oa); asm volatile("" : "+v"(oa)); } while (0)
  LDV(6); PV(0);
  LDV(7); PV(1);
  LDV(8); PV(2);
  if (w == 6) asm volatile("s_mov_b64 exec, 0\n s_branch fz_hop0\n fz_ret:\n s_mov_b64 exec, -1" ::: "memory");
  LDV(9); PV(3);
  const unsigned ptv = __builtin_amdgcn_raw_buffer_load_b32(rs, o_pt, 0, SC1);
  const unsigned xo = __builtin_amdgcn_raw_buffer_load_b16(mk_rsrc(w == 4 ? po + h * (DH * 1024) : w == 6 ? pout : px, (fin || w == 4 || w == 6) ? 1024 : 0), o_out, 0, 0);
  SB();
  LDV(10); PV(4);
  __builtin_amdgcn_s_setprio(0);
  LDV(11); PV(5);
  const u32x4 gf1 = LD128(rs, o_f, 0, SC1);
  SB();
  LDV(12); PV(6);
  LDV(13); PV(7);
  LDV(14);
  LDV(15);
  PV(8); PV(9); PV(10); PV(11); PV(12); PV(13);
  if (w >= 8) { dl = te + (15 - w) * STG + WOD * 16; PACE(); }
  const rsrc_t rwo = mk_rsrc(po, w >= 8 ? 4u << 20 : 0);
  const u32x4 wov0 = LD128(rwo, o_wo, (h * DH + 16 * wc) * 1024 + s * 128, NT);
  const u32x4 wov1 = LD128(rwo, o_wo + 1024, (h * DH + 16 * wc) * 1024 + s * 128, NT);
  SB();
  f32x4 fw = z4;
  if (w < 4) {
    SPIN (unsigned spins = 0; LDS_LD(&L.cnt) != 17u && spins < (SPIN_CAP << 6); ++spins) __builtin_amdgcn_s_sleep(1);
    MEMFENCE();
    fw = *(const f32x4*)&L.wmax[4 * r];
    const float ls = L.lse;
#pragma unroll
    for (int i = 0; i < 4; ++i) fw[i] = __builtin_amdgcn_exp2f(fw[i] - ls);
  }
  asm volatile("" :: "v"(gf1), "v"(xo), "v"(ptv));
  PV(14);
#define LAST() do { PV(15); *(f32x4*)(lds + a_part) = oa; BARRIER(); } while (0)
  if (w >= 8) {
    LAST();
    __builtin_amdgcn_s_sleep(DL1);
    SPIN (unsigned spins = 0; LDS_LD(&L.go) == 0u && spins < (SPIN_CAP << 6); ++spins) __builtin_amdgcn_s_sleep(1);
    MEMFENCE();
    const float wg = L.wgt[c8];
    u32x4 go;
    unsigned cnt_;
    asm volatile("s_mov_b32 %[cnt], 0x40000\n s_branch 2f\n"
                 "fz_hop2: s_branch fz_hop1\n"
                 "1: s_sub_u32 %[cnt], %[cnt], 1\n s_cbranch_scc1 3f\n"
                 "2: buffer_load_dwordx4 v[64:67], %[off], %[rs], 0 offen sc1\n fz_hop1: s_cbranch_execz fz_hopc\n s_waitcnt vmcnt(0)\n"
                 " v_cmp_ne_u32 vcc, v65, %[tag]\n v_cmp_ne_u32 s[90:91], v67, %[tag]\n s_or_b64 exec, vcc, s[90:91]\n s_cbranch_scc1 1b\n"
                 "3: s_mov_b64 exec, -1\n v_mul_f32 v64, v64, %[wg]\n v_mul_f32 v66, v66, %[wg]\n s_nop 0\n"
                 " v_add_f32_dpp v64, v64, v64 quad_perm:[1,0,3,2] row_mask:0xf bank_mask:0xf\n v_add_f32_dpp v66, v66, v66 quad_perm:[1,0,3,2] row_mask:0xf bank_mask:0xf\n s_nop 0\n"
                 " v_add_f32_dpp v64, v64, v64 quad_perm:[2,3,0,1] row_mask:0xf bank_mask:0xf\n v_add_f32_dpp v66, v66, v66 quad_perm:[2,3,0,1] row_mask:0xf bank_mask:0xf\n fz_hopc: s_cbranch_execz fz_hop8\n"
                 " v_add_f32_dpp v64, v64, v64 row_half_mirror row_mask:0xf bank_mask:0xf\n v_add_f32_dpp v66, v66, v66 row_half_mirror row_mask:0xf bank_mask:0xf\n"
                 " v_cvt_pk_bf16_f32 v64, v64, v64\n v_cvt_pk_bf16_f32 v66, v66, v66"
                 : "=&{v[64:67]}"(go), [cnt] "=&s"(cnt_) : [off] "v"(o_o), [rs] "s"(rs), [tag] "v"(tag), [wg] "v"(wg)
                 : "scc", "vcc", "s90", "s91", "memory");
    f32x4 y = mfma(diag(go[0], m8), wov0, z4);
    y = mfma(diag(go[2], m8), wov1, y);
    SB();
    HOP(8, "fz_hopd");
    SB();
    *(f32x4*)(lds + a_yw) = y;
    BARRIER();
  } else if (w < 4) {
    LAST();
    const float* src = (const float*)(lds + a_mrg);
    const f32x2 v0 = *(const f32x2*)src, v1 = *(const f32x2*)(src + 128), v2 = *(const f32x2*)(src + 256), v3 = *(const f32x2*)(src + 384);
    HOP(0, "fz_hopa");
    const f32x2 av = (fw[0] * v0 + fw[1] * v1) + (fw[2] * v2 + fw[3] * v3);
    ST64(x16_sum(x32_sum2(av[0], av[1])), tag, rs, o_pub);
    HOP(a, "fz_hopb");
    BARRIER();
    const float* src2 = (const float*)(lds + a_yr);
    const f32x2 a = *(const f32x2*)src2 + *(const f32x2*)(src2 + 64);
    ST64(col_sum(a[0], a[1]), tag, rs, o_y);
    HOP(b, "fz_hop2");
  } else {
    LAST();
    if (w == 4) {
      HOP(e, "fz_hopf");
      u32x4 gf = gf1;
#define FBAD() __any(c8 != s && ((gf[1] != tag) | (gf[3] != tag)))
      if (__builtin_expect(FBAD(), 0)) {
        SPIN (unsigned spins = 0; spins < SPIN_CAP; ++spins) {
          MEMFENCE();
          gf = LD128(rs, o_f, 0, SC1);
          if (!FBAD()) break;
          __builtin_amdgcn_s_sleep(2);
        }
      }
      const float ls = c8 == s ? L.lse : F(gf[0]);
      const float e = __builtin_amdgcn_exp2f(ls - oct_max(ls));
      L.wgt[c8] = e * __builtin_amdgcn_rcpf(oct_sum(e));
      __hip_atomic_store(&L.go, 1u, __ATOMIC_RELEASE, __HIP_MEMORY_SCOPE_WORKGROUP);
      HOP(f, "fz_hop4");
    } else if (w == 6) {
      HOP(d, "fz_hope");
      const f32x2 zz = *(const f32x2*)&L.z[2 * (lane & 31)];
      ST128(zz[0], zz[1], tag, rs, lane < 32 ? WS_Z + (s * 32 + h) * 512 + lane * 16 : OOB);
    }
    u32x4 gz[4];
    if (fin) __builtin_amdgcn_s_sleep(ZSL);
    {
      const rsrc_t rz = mk_rsrc(pws, fin ? WS_END : 0);
#pragma unroll
      for (int i = 0; i < 4; ++i) gz[i] = LD128(rz, WS_Z + o_fin + i * 512, 0, SC1);
      SB();
    }
    BARRIER();
    if (!fin) return;
    HOP(4, "fz_hop5");
#define ZBAD() __any((gz[0][1] != tag) | (gz[0][3] != tag) | (gz[1][1] != tag) | (gz[1][3] != tag) | (gz[2][1] != tag) | (gz[2][3] != tag) | (gz[3][1] != tag) | (gz[3][3] != tag))
    if (__builtin_expect(ZBAD(), 0)) {
      SPIN (unsigned spins = 0; spins < SPIN_CAP; ++spins) {
        MEMFENCE();
#pragma unroll
        for (int i = 0; i < 4; ++i) gz[i] = LD128(rs, WS_Z + o_fin + i * 512, 0, SC1);
        if (!ZBAD()) break;
      }
    }
    float gate = rbf(silu(rbf(col_sum((F(gz[0][0]) + F(gz[1][0])) + (F(gz[2][0]) + F(gz[3][0])), (F(gz[0][2]) + F(gz[1][2])) + (F(gz[2][2]) + F(gz[3][2]))))));
    float xres = blo(xo);
    o_fin += WS_Y;
    asm volatile("" : "+v"(gate), "+v"(xres), "+v"(o_fin));
    HOP(5, "fz_hopg");
    __builtin_amdgcn_s_sleep(DL2);
    float va, vb, vc2, vd;
    u32x16 keepA, keepB;
    unsigned cnt_;
#define SWEEP(B0, B1, B2, B3) "buffer_load_dwordx4 " B0 ", %[off], %[rs], 0 offen sc1\n buffer_load_dwordx4 " B1 ", %[off], %[rs], 0 offen offset:512 sc1\n" \
    " buffer_load_dwordx4 " B2 ", %[off], %[rs], 0 offen offset:1024 sc1\n buffer_load_dwordx4 " B3 ", %[off], %[rs], 0 offen offset:1536 sc1\n"
#define SWEEP_A SWEEP("v[64:67]", "v[68:71]", "v[72:75]", "v[76:79]")
#define SWEEP_B SWEEP("v[80:83]", "v[84:87]", "v[88:91]", "v[92:95]")
#define CK(v) " v_cmpx_eq_u32 " v ", %[tag]\n"
#define CK_END " s_cmp_eq_u64 exec, -1\n s_mov_b64 exec, -1\n"
    asm volatile(SWEEP_A " s_sleep 4\n s_mov_b32 %[cnt], 0x40000\n"
      "1: " SWEEP_B "fz_hopg: s_cbranch_execz fz_hop6\n s_waitcnt vmcnt(4)\n" CK("v65") CK("v67") CK("v69") CK("v71") CK("v73") CK("v75") CK("v77") CK("v79") CK_END " s_cbranch_scc1 2f\n"
      SWEEP_A "fz_hop6: s_cbranch_execz fz_hop9\n s_waitcnt vmcnt(4)\n" CK("v81") CK("v83") CK("v85") CK("v87") CK("v89") CK("v91") CK("v93") CK("v95") CK_END " s_cbranch_scc1 3f\n"
      " s_sub_u32 %[cnt], %[cnt], 1\n s_cmp_lg_u32 %[cnt], 0\n s_cbranch_scc1 1b\n"
      "3: v_add_f32 %[a], v80, v84\n v_add_f32 %[b], v82, v86\n v_add_f32 %[c], v88, v92\n v_add_f32 %[d], v90, v94\n s_branch 4f\n"
      "fz_hop9: s_branch fz_hop3\n"
      "2: v_add_f32 %[a], v64, v68\n v_add_f32 %[b], v66, v70\n v_add_f32 %[c], v72, v76\n v_add_f32 %[d], v74, v78\n 4:"
      : [a] "=&v"(va), [b] "=&v"(vb), [c] "=&v"(vc2), [d] "=&v"(vd), [cnt] "=&s"(cnt_), "=&{v[64:79]}"(keepA), "=&{v[80:95]}"(keepB)
      : [off] "v"(o_fin), [rs] "s"(rs), [tag] "v"(tag) : "scc", "vcc", "memory");
    float sv = x32_sum2(va + vc2, vb + vd);
    asm volatile("fz_hop3: s_cbranch_execz fz_hop7" : "+v"(sv));
    sv = x16_sum(sv);
    sv += dppf<ROR8>(sv);
    const float ov = xres + rbf(rbf(sv) * gate);
    __builtin_amdgcn_raw_buffer_store_b16((u16)pkbf(ov, ov), mk_rsrc(pout, 1024), o_out, 0, SC1);
    HOP(7, "fz_ret");
    asm volatile("fz_last:");
    __builtin_amdgcn_raw_buffer_store_b32(tag, rs, (h == 0 && lane == 0) ? WS_EPOCH : OOB, 0, SC1);
    asm volatile("" :: "v"(keepA), "v"(keepB));
  }
}

extern "C" void kernel_launch(void* const* d_in, const int* in_sizes, int n_in, void* d_out, int out_size, void* d_ws, size_t ws_size, hipStream_t stream) {
  fused<<<256, 1024, 0, stream>>>((const u16*)d_in[1], (const u16*)d_in[2], (const u16*)d_in[0], (const u16*)d_in[3], (const u16*)d_in[6],
                                  (unsigned char*)d_ws, (const u16*)d_in[4], (const u16*)d_in[7], (u16*)d_out);
}
```

```cpp
#include <hip/hip_runtime.h>
#include <stdint.h>
#include <stddef.h>

typedef unsigned short u16;
typedef unsigned u32x2 __attribute__((ext_vector_type(2)));
typedef unsigned u32x4 __attribute__((ext_vector_type(4)));
typedef unsigned u32x16 __attribute__((ext_vector_type(16)));
typedef unsigned long long u64;
typedef unsigned long long u64x8 __attribute__((ext_vector_type(8)));
typedef float f32x2 __attribute__((ext_vector_type(2)));
typedef float f32x4 __attribute__((ext_vector_type(4)));
typedef __bf16 bf16x2 __attribute__((ext_vector_type(2)));
typedef __bf16 bf16x8 __attribute__((ext_vector_type(8)));
typedef __amdgpu_buffer_rsrc_t rsrc_t;

constexpr unsigned TREL = 1768;
constexpr unsigned KEPS = 50;
constexpr unsigned STG = 58;
constexpr unsigned WOD = 32;
constexpr int DL1 = 14, DL2 = 8, ZSL = 20;

constexpr int D = 512, SK = 8192, DH = 128, FF = 1376;
constexpr float LOG2E = 1.4426950408889634f;
constexpr int NT = 2, SC1 = 16;
constexpr int OOB = 0x40000000;
constexpr int TB = 12;
constexpr int LEAD = 21;
constexpr unsigned SPIN_CAP = 1u << 18;
constexpr unsigned WS_EPOCH = 0;
constexpr unsigned WS_T = 4096;
constexpr unsigned WS_Q = 8192;
constexpr unsigned WS_H = WS_Q + 128 * 1024;
constexpr unsigned WS_F = WS_H + 128 * 512;
constexpr unsigned WS_O = WS_F + 32 * 128;
constexpr unsigned WS_Y = WS_O + 256 * 1024;
constexpr unsigned WS_Z = WS_Y + 256 * 512;
constexpr unsigned WS_END = WS_Z + 256 * 512;

__device__ __forceinline__ float rbf(float f) { return (float)(__bf16)f; }
__device__ __forceinline__ float blo(unsigned u) { return __uint_as_float(u << 16); }
__device__ __forceinline__ float bhi(unsigned u) { return __uint_as_float(u & 0xffff0000u); }
__device__ __forceinline__ unsigned pkbf(float a, float b) { bf16x2 v = {(__bf16)a, (__bf16)b}; return __builtin_bit_cast(unsigned, v); }
__device__ __forceinline__ float dot2(unsigned a, unsigned b, float acc) {
  return __builtin_amdgcn_fdot2_f32_bf16(__builtin_bit_cast(bf16x2, a), __builtin_bit_cast(bf16x2, b), acc, false);
}
template <int CTRL> __device__ __forceinline__ float dppf(float x) {
  return __builtin_bit_cast(float, __builtin_amdgcn_mov_dpp(__builtin_bit_cast(int, x), CTRL, 0xf, 0xf, true));
}
template <int CTRL> __device__ __forceinline__ unsigned dppu(unsigned x) {
  return (unsigned)__builtin_amdgcn_mov_dpp((int)x, CTRL, 0xf, 0xf, true);
}
constexpr int XOR1 = 0xB1, XOR2 = 0x4E, XOR7 = 0x141, ROR8 = 0x128, BCAST = 0x150;
__device__ __forceinline__ float x16_sum(float x) {
  auto s = __builtin_amdgcn_permlane16_swap(__float_as_uint(x), __float_as_uint(x), false, false);
  return __uint_as_float(s[0]) + __uint_as_float(s[1]);
}
__device__ __forceinline__ float x32_sum2(float a, float b) {
  auto t = __builtin_amdgcn_permlane32_swap(__float_as_uint(a), __float_as_uint(b), false, false);
  return __uint_as_float(t[0]) + __uint_as_float(t[1]);
}
__device__ __forceinline__ float xrow_max(float x) {
  auto s = __builtin_amdgcn_permlane16_swap(__float_as_uint(x), __float_as_uint(x), false, false);
  x = fmaxf(__uint_as_float(s[0]), __uint_as_float(s[1]));
  auto t = __builtin_amdgcn_permlane32_swap(__float_as_uint(x), __float_as_uint(x), false, false);
  return fmaxf(__uint_as_float(t[0]), __uint_as_float(t[1]));
}
__device__ __forceinline__ float oct_sum(float x) { x += dppf<XOR1>(x); x += dppf<XOR2>(x); x += dppf<XOR7>(x); return x; }
__device__ __forceinline__ float oct_max(float x) { x = fmaxf(x, dppf<XOR1>(x)); x = fmaxf(x, dppf<XOR2>(x)); x = fmaxf(x, dppf<XOR7>(x)); return x; }
__device__ __forceinline__ float row_sum(float x) { x = oct_sum(x); x += dppf<ROR8>(x); return x; }
__device__ __forceinline__ float row_max(float x) { x = oct_max(x); x = fmaxf(x, dppf<ROR8>(x)); return x; }
__device__ __forceinline__ float wave_sum(float x) { x = x16_sum(row_sum(x)); return x32_sum2(x, x); }
__device__ __forceinline__ float wave_max(float x) { return xrow_max(row_max(x)); }
__device__ __forceinline__ float col_sum(float a, float b) { const float v = x16_sum(x32_sum2(a, b)); return v + dppf<ROR8>(v); }
__device__ __forceinline__ f32x4 mfma(u32x4 a, u32x4 b, f32x4 c) {
  return __builtin_amdgcn_mfma_f32_16x16x32_bf16(__builtin_bit_cast(bf16x8, a), __builtin_bit_cast(bf16x8, b), c, 0, 0, 0);
}
__device__ __forceinline__ u32x4 diag(unsigned pk, const u32x4& m) { return u32x4{pk & m[0], pk & m[1], pk & m[2], pk & m[3]}; }
__device__ __forceinline__ float silu(float z) { return z * __builtin_amdgcn_rcpf(1.f + __builtin_amdgcn_exp2f(-z * LOG2E)); }
__device__ __forceinline__ rsrc_t mk_rsrc(u64 p, unsigned bytes) { return __builtin_amdgcn_make_buffer_rsrc((void*)p, 0, bytes, 0x00020000); }
#define F(u) __uint_as_float(u)
#define LD128(rs, vo, so, aux) __builtin_amdgcn_raw_buffer_load_b128(rs, vo, so, aux)
#define LD64(rs, vo, so, aux) __builtin_amdgcn_raw_buffer_load_b64(rs, vo, so, aux)
#define ST128(a, b, tag, rs, vo) __builtin_amdgcn_raw_buffer_store_b128((u32x4{__float_as_uint(a), tag, __float_as_uint(b), tag}), rs, vo, 0, SC1)
#define ST64(a, tag, rs, vo) __builtin_amdgcn_raw_buffer_store_b64((u32x2{__float_as_uint(a), tag}), rs, vo, 0, SC1)
#define LDS_LD(p) __hip_atomic_load(p, __ATOMIC_RELAXED, __HIP_MEMORY_SCOPE_WORKGROUP)
#define SB() __builtin_amdgcn_sched_barrier(0)
#define MEMFENCE() asm volatile("" ::: "memory")
#define BARRIER() asm volatile("s_waitcnt lgkmcnt(0)\n s_barrier" ::: "memory")
#define SPIN _Pragma("nounroll") for
#define PACE() asm volatile("1: s_memrealtime s[90:91]\n s_waitcnt lgkmcnt(0)\n s_lshl_b32 s90, s90, 4\n s_sub_u32 s90, s90, %0\n s_cmp_lt_i32 s90, 0\n" \
                            " s_cbranch_scc0 2f\n s_sleep 1\n s_branch 1b\n 2: s_add_u32 %0, %0, %1" : "+s"(dl) : "s"(stp) : "scc", "s90", "s91")
#define PAD(i) asm volatile("fz_w%c0: s_cbranch_execz fz_wret" :: "i"(i))
#define HOP(i, nx) asm volatile("fz_hop" #i ": s_cbranch_execz " nx)

struct Lds {
  float part[16][128];
  float ypart[16][64];
  float junk[128];
  float z[64];
  float wmax[16];
  float wsum[16];
  unsigned qb[64];
  unsigned tw[16];
  unsigned qready, cnt, tadj, half, go, tag;
  float lse, wgt[8];
};

__global__ void __launch_bounds__(1024, 4) fused(const u16* kc, const u16* vc, const u16* x, const u16* wq, const u16* w1,
                                                  unsigned char* ws, const u16* wo, const u16* w2, u16* out) {
  __shared__ __attribute__((aligned(16))) Lds L;
  const int b = blockIdx.x;
  const int h = (b & 7) * 4 + (b >> 6), s = (b >> 3) & 7;
  const int lane = threadIdx.x & 63, w = __builtin_amdgcn_readfirstlane(threadIdx.x >> 6);
  u64x8 ka; u64 pout;
  asm volatile("s_load_dwordx16 %0, %2, 0x0\n s_load_dwordx2 %1, %2, 0x40\n"
               " s_mov_b64 exec, 0\n s_getpc_b64 s[90:91]\n s_lshl_b32 s92, %3, 2\n s_add_u32 s90, s90, s92\n s_addc_u32 s91, s91, 0\n"
               " s_add_u32 s90, s90, 24\n s_addc_u32 s91, s91, 0\n s_setpc_b64 s[90:91]\n"
               " s_branch fz_w0\n s_branch fz_w1\n s_branch fz_w2\n s_branch fz_w3\n s_branch fz_w4\n s_branch fz_w5\n s_branch fz_w6\n s_branch fz_w7\n"
               " s_branch fz_w8\n s_branch fz_w9\n s_branch fz_w10\n s_branch fz_w11\n s_branch fz_w12\n s_branch fz_w13\n s_branch fz_w14\n s_branch fz_w15\n"
               "fz_wret: s_mov_b64 exec, -1\n s_waitcnt lgkmcnt(0)"
               : "=&s"(ka), "=&s"(pout) : "s"(__builtin_amdgcn_kernarg_segment_ptr()), "s"(w) : "s90", "s91", "s92", "scc");
  const u64 pk = ka[0], pv = ka[1], px = ka[2], pq = ka[3], p1 = ka[4], pws = ka[5], po = ka[6], p2 = ka[7];
  const rsrc_t rk = mk_rsrc(pk, 64u << 20), rv = mk_rsrc(pv, 64u << 20), rs = mk_rsrc(pws, WS_END);
  const f32x4 z4 = {0.f, 0.f, 0.f, 0.f};
  int kvs = h * (SK * 256) + s * 16384 + w * 1024, kvv = lane * 16;
#define TOFF(t) (kvs + (((t) + h) & 15) * 131072)
#define LDK(t) do { kt[t] = LD128(rk, kvv, TOFF(t), NT); SB(); } while (0)
#define LDV(t) do { PACE(); vt[t] = LD128(rv, kvv, TOFF(t), NT); SB(); } while (0)
#define LANE_VARS() int ln_ = lane; asm volatile("" : "+v"(ln_)); const int r = ln_ >> 4, c = ln_ & 15, r8 = ln_ >> 3, c8 = ln_ & 7; (void)r; (void)c; (void)r8; (void)c8
#define MASK16() u32x4 mk; _Pragma("unroll") for (int i = 0; i < 4; ++i) mk[i] = ((c >> 1) == i) ? ((c & 1) ? 0xffff0000u : 0x0000ffffu) : 0u
#define MASK8() u32x4 m8; _Pragma("unroll") for (int i = 0; i < 4; ++i) m8[i] = ((c8 >> 1) == i) ? ((c8 & 1) ? 0xffff0000u : 0x0000ffffu) : 0u; const bool valid8 = (r >> 1) == (c >> 3)
  u32x4 kt[16], vt[16];
  __builtin_amdgcn_s_setprio(3);
  LDK(0); LDK(1);
  PAD(7);
  if (w == 2 && lane < 21) ((unsigned*)((char*)&L + offsetof(Lds, tw)))[lane] = 0u;
  BARRIER();
  unsigned pfv;
  {
    u64 pc; unsigned len;
    asm volatile("s_getpc_b64 %0\n fz_pc: s_mov_b32 %1, fz_last-fz_pc\n s_max_i32 %1, %1, fz_last0-fz_pc\n s_max_i32 %1, %1, fz_last1-fz_pc" : "=s"(pc), "=s"(len) :: "scc");
    pfv = __builtin_amdgcn_raw_buffer_load_b32(mk_rsrc(pc, len), lane * 64, (15 - w) * 4096, 0);
    SB();
  }
  const int nch = 5 + (h < 12), ch0 = 5 * h + min(h, 12);
  unsigned stag = 0, tanc = 0;
  if (__builtin_expect(w < 2, 0)) {
    PAD(0);
    const bool r0 = s < 4;
    const int kb = (s & 3) * 2 + w;
    int sa0 = kb * (64 * 8192) + h * 256, sa1 = kb * (64 * FF * 2) + ch0 * 16;
    int ob0 = WS_Q + (h * 4 + s) * 1024, ob1 = WS_H + (h * 4 + s - 4) * 512;
    asm volatile("" : "+s"(sa0), "+s"(sa1), "+s"(ob0), "+s"(ob1));
    const int sa = r0 ? sa0 : sa1, da = r0 ? 8192 : FF * 2;
    {
      u64 ta0 = pv + kvs, ta1 = po + h * (DH * 1024), tc1 = p2 + ch0 * 8192;
      asm volatile("" : "+s"(ta0), "+s"(ta1), "+s"(tc1));
      asm volatile("s_load_dword s92, %0, 0x0\n s_load_dword s93, %1, 0x0\n s_load_dword s94, %2, 0x0\n s_load_dword s95, %3, 0x0"
                   :: "s"((r0 ? pq : p1) + sa), "s"(w == 0 ? pws : px), "s"(w == 0 ? ta0 : ta1), "s"(w == 0 ? pout : tc1) : "s92", "s93", "s94", "s95");
    }
    const rsrc_t rx = mk_rsrc(px, 1024), ra = mk_rsrc(r0 ? pq : p1, r0 ? 4u << 20 : 512 * FF * 2);
    LANE_VARS();
    const int va = c < (r0 ? 16 : nch) ? r * da + c * 16 : OOB;
    asm volatile("s_waitcnt lgkmcnt(0)" ::: "memory");
    stag = __builtin_amdgcn_raw_buffer_load_b32(rs, WS_EPOCH, 0, SC1);
    const u32x4 xa = LD128(rx, kvv, 0, 0);
    const unsigned xe = __builtin_amdgcn_raw_buffer_load_b16(rx, (4 * c + r) * 2, kb * 128, 0);
    u32x4 wv[16];
    SB();
    PAD(1);
#pragma unroll
    for (int i = 0; i < 16; ++i) { wv[i] = LD128(ra, va, sa + i * 4 * da, NT); SB(); }
    PAD(2);
    SB();
    MASK16();
    int o_a = (r0 ? ob0 : ob1) + (4 * c + 2 * (r & 1) + (r >> 1)) * 16;
    o_a = (r0 || c < 6) ? o_a : OOB;
    asm volatile("" : "+v"(stag), "+v"(o_a), "+v"(mk));
    stag += 1u;
    unsigned xp;
    {
      float xv[8];
#pragma unroll
      for (int j = 0; j < 4; ++j) { xv[2 * j] = blo(xa[j]); xv[2 * j + 1] = bhi(xa[j]); }
      float mean = wave_sum(((xv[0] + xv[1]) + (xv[2] + xv[3])) + ((xv[4] + xv[5]) + (xv[6] + xv[7]))) * (1.f / D);
      asm volatile("" : "+v"(mean));
      PAD(3);
      float sq = 0.f;
#pragma unroll
      for (int j = 0; j < 8; ++j) { const float d = xv[j] - mean; sq = fmaf(d, d, sq); }
      const float var = rbf(wave_sum(sq) * (1.f / D));
      const float rden = __builtin_amdgcn_rcpf(rbf(__builtin_amdgcn_sqrtf(rbf(var + 1e-5f))));
      const float xn = rbf(blo(xe) - rbf(mean)) * rden;
      xp = pkbf(xn, xn);
    }
    asm volatile("" : "+v"(xp));
    SB();
    PAD(4);
    f32x4 qa = z4;
#define QA(i) qa = mfma(diag(dppu<BCAST + (i)>(xp), mk), wv[i], qa);
    QA(0) QA(1) QA(2) QA(3) QA(4) QA(5)
    asm volatile("" : "+v"(qa)); SB(); PAD(5);
    QA(6) QA(7) QA(8) QA(9) QA(10) QA(11)
    asm volatile("" : "+v"(qa)); SB(); PAD(6);
    QA(12) QA(13) QA(14) QA(15)
#undef QA
    const f32x2 pa = {x32_sum2(qa[0], qa[2]), x32_sum2(qa[1], qa[3])};
    if (w == 1) {
      *(f32x2*)&L.junk[2 * lane] = pa;
      __hip_atomic_store(&L.half, 1u, __ATOMIC_RELEASE, __HIP_MEMORY_SCOPE_WORKGROUP);
    } else {
      SPIN (unsigned spins = 0; LDS_LD(&L.half) == 0u && spins < (SPIN_CAP << 6); ++spins) __builtin_amdgcn_s_sleep(1);
      MEMFENCE();
      const f32x2 pb = *(const f32x2*)&L.junk[2 * lane];
      ST128(pa[0] + pb[0], pa[1] + pb[1], stag, rs, o_a);
    }
    {
      const unsigned now = (unsigned)__builtin_amdgcn_s_memrealtime(), tw = LDS_LD(&L.tw[c]);
      const float n = row_sum(tw ? 1.f : 0.f), d = row_sum(tw ? (float)(int)(now - tw) : 0.f);
      tanc = now - __builtin_amdgcn_readfirstlane((unsigned)(d * __builtin_amdgcn_rcpf(fmaxf(n, 1.f))));
      ST64(F(tanc), stag, rs, ((b >> 3) == LEAD && w == 1 && lane < 8) ? WS_T + lane * 128 + (b & 7) * 8 : OOB);
    }
  }
  PAD(8);
  asm volatile("" :: "v"(kt[0]));
  if (w >= 2) {
    tanc = (unsigned)__builtin_amdgcn_s_memrealtime() | 1u;
    L.tw[w] = tanc;
  }
  const unsigned tsp = TREL * 16 - (15 - w) * STG;
  unsigned te = tanc * 16 + tsp, dl = tanc * 16, stp = TREL * 16 * (1000 + KEPS) / 29000;
  if (w < 2) {
    const unsigned now16 = (unsigned)__builtin_amdgcn_s_memrealtime() * 16;
    stp = (unsigned)max((int)(dl + 14 * stp - now16), 0) * (65536u / 14) >> 16; dl = now16;
  }
#define REANCHOR(R) do { const unsigned tr_ = __builtin_amdgcn_readfirstlane(LDS_LD(&L.tadj)); te = tr_ ? tr_ * 16 + tsp : te; \
    stp = (unsigned)max((int)(te - dl), 0) * (65536u / ((R) - 1)) >> 16; } while (0)
  asm volatile("" : "+s"(kvs), "+v"(kvv));
#pragma unroll
  for (int t = 2; t < TB; ++t) { if ((t & 1) == 0) PAD(8 + t / 2); PACE(); LDK(t); }
  if (__builtin_expect(w < 2, 0)) {
    LANE_VARS();
    if (w == 0) {
      u32x4 g[4];
      SPIN (unsigned spins = 0; spins < SPIN_CAP; ++spins) {
        bool ok = true;
#pragma unroll
        for (int i = 0; i < 4; ++i) g[i] = LD128(rs, WS_Q + h * 4096 + kvv + i * 1024, 0, SC1);
        SB();
#pragma unroll
        for (int i = 0; i < 4; ++i) ok &= (g[i][1] == stag) & (g[i][3] == stag);
        if (__all(ok)) break;
        asm volatile("s_sleep 8" ::: "memory");
      }
      float q0 = 0.f, q1 = 0.f;
#pragma unroll
      for (int i = 0; i < 4; ++i) { q0 += F(g[i][0]); q1 += F(g[i][2]); }
      L.qb[lane] = pkbf(q0, q1);
      if (lane == 0) L.tag = stag;
      __hip_atomic_store(&L.qready, 1u, __ATOMIC_RELEASE, __HIP_MEMORY_SCOPE_WORKGROUP);
      asm volatile("fz_last0:");
    } else {
      MASK8();
      const rsrc_t r2 = mk_rsrc(p2, (ch0 + nch) * 8192);
      u32x4 wv[6];
#pragma unroll
      for (int i = 0; i < 6; ++i) { wv[i] = LD128(r2, r8 * 1024 + c8 * 16, (ch0 + i) * 8192 + s * 128, NT); SB(); }
      u32x2 g[6], gt;
      SPIN (unsigned spins = 0; spins < SPIN_CAP; ++spins) {
        gt = LD64(rs, WS_T + (h & 7) * 128 + c8 * 8, 0, SC1);
#pragma unroll
        for (int i = 0; i < 6; ++i) g[i] = LD64(rs, WS_H + h * 2048 + (c8 & 3) * 512 + r8 * 8 + i * 64, 0, SC1);
        SB();
        bool ok = gt[1] == stag;
#pragma unroll
        for (int i = 0; i < 6; ++i) ok &= g[i][1] == stag;
        if (__all(ok)) break;
        asm volatile("s_sleep 8" ::: "memory");
      }
      {
        const float dt = (float)(int)(gt[0] - tanc);
        L.tadj = (tanc + (int)fminf(fmaxf(oct_sum(dt) * 0.0625f + oct_max(dt) * 0.5f, -300.f), 300.f)) | 1u;
      }
      f32x4 za = z4;
#pragma unroll
      for (int i = 0; i < 6; ++i) {
        float hv = F(g[i][0]);
        hv += dppf<XOR1>(hv); hv += dppf<XOR2>(hv);
        hv = silu(rbf(hv));
        hv = i < nch ? hv : 0.f;
        za = mfma(diag(pkbf(hv, hv), m8), wv[i], za);
      }
#pragma unroll
      for (int j = 0; j < 4; ++j) { const float v = valid8 ? za[j] : 0.f; za[j] = v + dppf<ROR8>(v); }
      const f32x2 zz = {x32_sum2(za[0], za[2]), x32_sum2(za[1], za[3])};
      if (c < 8) *(f32x2*)&L.z[8 * c + 4 * (r & 1) + 2 * (r >> 1)] = zz;
      asm volatile("fz_last1:");
    }
  }
  asm volatile("" : "+s"(kvs), "+v"(kvv));
#pragma unroll
  for (int t = TB; t < 16; ++t) { if ((t & 1) == 0) PAD(8 + t / 2); PACE(); LDK(t); }

  __builtin_amdgcn_s_setprio(2);
  REANCHOR(16);
  LDV(0); LDV(1); LDV(2); LDV(3);
  SPIN (unsigned spins = 0; LDS_LD(&L.qready) == 0u && spins < (SPIN_CAP << 6); ++spins) __builtin_amdgcn_s_sleep(2);
  MEMFENCE();
  const u32x4 qp = *(const u32x4*)&L.qb[4 * (lane & 15)];
  const unsigned tag = L.tag;
  float xs[16];
#pragma unroll
  for (int t = 0; t < 16; ++t) {
    float d = dot2(kt[t][0], qp[0], 0.f);
    d = dot2(kt[t][1], qp[1], d); d = dot2(kt[t][2], qp[2], d); d = dot2(kt[t][3], qp[3], d);
    xs[t] = d;
  }
  asm volatile("" :: "v"(pfv));
  LANE_VARS();
#define TR_STEP(HALF, CTRL, BIT) _Pragma("unroll") for (int i = 0; i < (HALF); ++i) { \
    const float t1 = xs[i] + dppf<CTRL>(xs[i]), t2 = xs[i + (HALF)] + dppf<CTRL>(xs[i + (HALF)]); xs[i] = (c & (BIT)) ? t2 : t1; }
  TR_STEP(8, ROR8, 8) TR_STEP(4, XOR7, 4) TR_STEP(2, XOR2, 2) TR_STEP(1, XOR1, 1)
#undef TR_STEP
  asm volatile("" : "+v"(xs[0]));
  REANCHOR(12);
  LDV(4);
  const float sc = rbf(rbf(xs[0]) * 0.08838834764831845f) * LOG2E;
  const float wm = wave_max(sc);
  const float p = __builtin_amdgcn_exp2f(sc - wm);
  {
    const float wsu = wave_sum(p);
    unsigned old = 0;
    if (lane == 0) {
      L.wmax[w] = wm; L.wsum[w] = wsu;
      old = __hip_atomic_fetch_add(&L.cnt, 1u, __ATOMIC_ACQ_REL, __HIP_MEMORY_SCOPE_WORKGROUP);
    }
    if (__builtin_amdgcn_readfirstlane(old) == 15u) {
      const float wmc = L.wmax[c];
      const float M = row_max(wmc);
      const float lse = M + __builtin_amdgcn_logf(row_sum(L.wsum[c] * __builtin_amdgcn_exp2f(wmc - M)));
      ST128(lse, lse, tag, rs, lane == 0 ? WS_F + h * 128 + s * 16 : OOB);
      L.lse = lse;
      __hip_atomic_store(&L.cnt, 17u, __ATOMIC_RELEASE, __HIP_MEMORY_SCOPE_WORKGROUP);
    }
  }
  unsigned pp = pkbf(p, p);
  asm volatile("" : "+v"(pp));
  __builtin_amdgcn_s_setprio(1);
  LDV(5);
  MASK16();
  MASK8();
  const int wc = w & 7;
  const bool fin = w == 5 && (h & 7) == s;
  int a_part = r < 2 ? offsetof(Lds, part) + w * 512 + (8 * c + 4 * r) * 4 : offsetof(Lds, junk) + (lane - 32) * 16;
  int a_mrg = offsetof(Lds, part) + r * 2048 + (32 * w + 2 * c) * 4;
  int a_yw = valid8 ? offsetof(Lds, ypart) + (2 * wc + (r >> 1)) * 256 + (8 * c8 + 4 * (r & 1)) * 4 : offsetof(Lds, junk) + (r * 8 + c8) * 16;
  int a_yr = offsetof(Lds, ypart) + r8 * 512 + (16 * w + 2 * c8) * 4;
  int o_o = WS_O + h * 8192 + c8 * 1024 + wc * 128 + r8 * 16;
  int o_pub = (r & 1) ? OOB : WS_O + (h * 8 + s) * 1024 + (32 * w + 2 * c + (r >> 1)) * 8;
  int o_y = (lane & 24) == 0 ? WS_Y + (s * 32 + h) * 512 + w * 128 + c8 * 16 + (r >> 1) * 8 : OOB;
  int o_fin = s * 16384 + (h >> 3) * 128 + r8 * 2048 + c8 * 16;
  int o_out = (lane & 24) == 0 ? 128 * s + 32 * (h >> 3) + 4 * c8 + 2 * (r >> 1) : OOB;
  int o_wo = r8 * 2048 + c8 * 16;
  int o_f = w == 4 ? WS_F + h * 128 + c8 * 16 : OOB;
  const int o_pt = w != 7 ? OOB : lane < 8 ? WS_O + (h * 8 + s) * 1024 + lane * 128 : lane < 16 ? (lane < 12 ? WS_Y : WS_Z) + (s * 32 + h) * 512 + (lane & 3) * 128 : OOB;
  asm volatile("" : "+v"(a_part), "+v"(a_mrg), "+v"(a_yw), "+v"(a_yr), "+v"(o_o), "+v"(o_pub), "+v"(o_y), "+v"(o_fin), "+v"(o_out), "+v"(o_wo), "+v"(o_f));
  asm volatile("" : "+v"(m8), "+v"(mk));
  char* const lds = (char*)&L;
  f32x4 oa = z4;
#define PV(T) do { oa = mfma(diag(dppu<BCAST + (T)>(pp), mk), vt[T], oa); asm volatile("" : "+v"(oa)); } while (0)
  LDV(6); PV(0);
  LDV(7); PV(1);
  LDV(8); PV(2);
  if (w == 6) asm volatile("s_mov_b64 exec, 0\n s_branch fz_hop0\n fz_ret:\n s_mov_b64 exec, -1" ::: "memory");
  LDV(9); PV(3);
  const unsigned ptv = __builtin_amdgcn_raw_buffer_load_b32(rs, o_pt, 0, SC1);
  const unsigned xo = __builtin_amdgcn_raw_buffer_load_b16(mk_rsrc(w == 4 ? po + h * (DH * 1024) : w == 6 ? pout : px, (fin || w == 4 || w == 6) ? 1024 : 0), o_out, 0, 0);
  SB();
  LDV(10); PV(4);
  __builtin_amdgcn_s_setprio(0);
  LDV(11); PV(5);
  const u32x4 gf1 = LD128(rs, o_f, 0, SC1);
  SB();
  LDV(12); PV(6);
  LDV(13); PV(7);
  LDV(14);
  LDV(15);
  PV(8); PV(9); PV(10); PV(11); PV(12); PV(13);
  if (w >= 8) { dl = te + (15 - w) * STG + WOD * 16; PACE(); }
  const rsrc_t rwo = mk_rsrc(po, w >= 8 ? 4u << 20 : 0);
  const u32x4 wov0 = LD128(rwo, o_wo, (h * DH + 16 * wc) * 1024 + s * 128, NT);
  const u32x4 wov1 = LD128(rwo, o_wo + 1024, (h * DH + 16 * wc) * 1024 + s * 128, NT);
  SB();
  f32x4 fw = z4;
  if (w < 4) {
    SPIN (unsigned spins = 0; LDS_LD(&L.cnt) != 17u && spins < (SPIN_CAP << 6); ++spins) __builtin_amdgcn_s_sleep(1);
    MEMFENCE();
    fw = *(const f32x4*)&L.wmax[4 * r];
    const float ls = L.lse;
#pragma unroll
    for (int i = 0; i < 4; ++i) fw[i] = __builtin_amdgcn_exp2f(fw[i] - ls);
  }
  asm volatile("" :: "v"(gf1), "v"(xo), "v"(ptv));
  PV(14);
#define LAST() do { PV(15); *(f32x4*)(lds + a_part) = oa; BARRIER(); } while (0)
  if (w >= 8) {
    LAST();
    __builtin_amdgcn_s_sleep(DL1);
    SPIN (unsigned spins = 0; LDS_LD(&L.go) == 0u && spins < (SPIN_CAP << 6); ++spins) __builtin_amdgcn_s_sleep(1);
    MEMFENCE();
    const float wg = L.wgt[c8];
    u32x4 go;
    unsigned cnt_;
    asm volatile("s_mov_b32 %[cnt], 0x40000\n s_branch 2f\n"
                 "fz_hop2: s_branch fz_hop1\n"
                 "1: s_sub_u32 %[cnt], %[cnt], 1\n s_cbranch_scc1 3f\n"
                 "2: buffer_load_dwordx4 v[64:67], %[off], %[rs], 0 offen sc1\n fz_hop1: s_cbranch_execz fz_hopc\n s_waitcnt vmcnt(0)\n"
                 " v_cmp_ne_u32 vcc, v65, %[tag]\n v_cmp_ne_u32 s[90:91], v67, %[tag]\n s_or_b64 exec, vcc, s[90:91]\n s_cbranch_scc1 1b\n"
                 "3: s_mov_b64 exec, -1\n v_mul_f32 v64, v64, %[wg]\n v_mul_f32 v66, v66, %[wg]\n s_nop 0\n"
                 " v_add_f32_dpp v64, v64, v64 quad_perm:[1,0,3,2] row_mask:0xf bank_mask:0xf\n v_add_f32_dpp v66, v66, v66 quad_perm:[1,0,3,2] row_mask:0xf bank_mask:0xf\n s_nop 0\n"
                 " v_add_f32_dpp v64, v64, v64 quad_perm:[2,3,0,1] row_mask:0xf bank_mask:0xf\n v_add_f32_dpp v66, v66, v66 quad_perm:[2,3,0,1] row_mask:0xf bank_mask:0xf\n fz_hopc: s_cbranch_execz fz_hop8\n"
                 " v_add_f32_dpp v64, v64, v64 row_half_mirror row_mask:0xf bank_mask:0xf\n v_add_f32_dpp v66, v66, v66 row_half_mirror row_mask:0xf bank_mask:0xf\n"
                 " v_cvt_pk_bf16_f32 v64, v64, v64\n v_cvt_pk_bf16_f32 v66, v66, v66"
                 : "=&{v[64:67]}"(go), [cnt] "=&s"(cnt_) : [off] "v"(o_o), [rs] "s"(rs), [tag] "v"(tag), [wg] "v"(wg)
                 : "scc", "vcc", "s90", "s91", "memory");
    f32x4 y = mfma(diag(go[0], m8), wov0, z4);
    y = mfma(diag(go[2], m8), wov1, y);
    SB();
    HOP(8, "fz_hopd");
    SB();
    *(f32x4*)(lds + a_yw) = y;
    BARRIER();
  } else if (w < 4) {
    LAST();
    const float* src = (const float*)(lds + a_mrg);
    const f32x2 v0 = *(const f32x2*)src, v1 = *(const f32x2*)(src + 128), v2 = *(const f32x2*)(src + 256), v3 = *(const f32x2*)(src + 384);
    HOP(0, "fz_hopa");
    const f32x2 av = (fw[0] * v0 + fw[1] * v1) + (fw[2] * v2 + fw[3] * v3);
    ST64(x16_sum(x32_sum2(av[0], av[1])), tag, rs, o_pub);
    HOP(a, "fz_hopb");
    BARRIER();
    const float* src2 = (const float*)(lds + a_yr);
    const f32x2 a = *(const f32x2*)src2 + *(const f32x2*)(src2 + 64);
    ST64(col_sum(a[0], a[1]), tag, rs, o_y);
    HOP(b, "fz_hop2");
  } else {
    LAST();
    if (w == 4) {
      HOP(e, "fz_hopf");
      u32x4 gf = gf1;
#define FBAD() __any(c8 != s && ((gf[1] != tag) | (gf[3] != tag)))
      if (__builtin_expect(FBAD(), 0)) {
        SPIN (unsigned spins = 0; spins < SPIN_CAP; ++spins) {
          MEMFENCE();
          gf = LD128(rs, o_f, 0, SC1);
          if (!FBAD()) break;
          __builtin_amdgcn_s_sleep(2);
        }
      }
      const float ls = c8 == s ? L.lse : F(gf[0]);
      const float e = __builtin_amdgcn_exp2f(ls - oct_max(ls));
      L.wgt[c8] = e * __builtin_amdgcn_rcpf(oct_sum(e));
      __hip_atomic_store(&L.go, 1u, __ATOMIC_RELEASE, __HIP_MEMORY_SCOPE_WORKGROUP);
      HOP(f, "fz_hop4");
    } else if (w == 6) {
      HOP(d, "fz_hope");
      const f32x2 zz = *(const f32x2*)&L.z[2 * (lane & 31)];
      ST128(zz[0], zz[1], tag, rs, lane < 32 ? WS_Z + (s * 32 + h) * 512 + lane * 16 : OOB);
    }
    u32x4 gz[4];
    if (fin) __builtin_amdgcn_s_sleep(ZSL);
    {
      const rsrc_t rz = mk_rsrc(pws, fin ? WS_END : 0);
#pragma unroll
      for (int i = 0; i < 4; ++i) gz[i] = LD128(rz, WS_Z + o_fin + i * 512, 0, SC1);
      SB();
    }
    BARRIER();
    if (!fin) return;
    HOP(4, "fz_hop5");
#define ZBAD() __any((gz[0][1] != tag) | (gz[0][3] != tag) | (gz[1][1] != tag) | (gz[1][3] != tag) | (gz[2][1] != tag) | (gz[2][3] != tag) | (gz[3][1] != tag) | (gz[3][3] != tag))
    if (__builtin_expect(ZBAD(), 0)) {
      SPIN (unsigned spins = 0; spins < SPIN_CAP; ++spins) {
        MEMFENCE();
#pragma unroll
        for (int i = 0; i < 4; ++i) gz[i] = LD128(rs, WS_Z + o_fin + i * 512, 0, SC1);
        if (!ZBAD()) break;
      }
    }
    float gate = rbf(silu(rbf(col_sum((F(gz[0][0]) + F(gz[1][0])) + (F(gz[2][0]) + F(gz[3][0])), (F(gz[0][2]) + F(gz[1][2])) + (F(gz[2][2]) + F(gz[3][2]))))));
    float xres = blo(xo);
    o_fin += WS_Y;
    asm volatile("" : "+v"(gate), "+v"(xres), "+v"(o_fin));
    HOP(5, "fz_hopg");
    __builtin_amdgcn_s_sleep(DL2);
    float va, vb, vc2, vd;
    u32x16 keepA, keepB;
    unsigned cnt_;
#define SWEEP(B0, B1, B2, B3) "buffer_load_dwordx4 " B0 ", %[off], %[rs], 0 offen sc1\n buffer_load_dwordx4 " B1 ", %[off], %[rs], 0 offen offset:512 sc1\n" \
    " buffer_load_dwordx4 " B2 ", %[off], %[rs], 0 offen offset:1024 sc1\n buffer_load_dwordx4 " B3 ", %[off], %[rs], 0 offen offset:1536 sc1\n"
#define SWEEP_A SWEEP("v[64:67]", "v[68:71]", "v[72:75]", "v[76:79]")
#define SWEEP_B SWEEP("v[80:83]", "v[84:87]", "v[88:91]", "v[92:95]")
#define CK(v) " v_cmpx_eq_u32 " v ", %[tag]\n"
#define CK_END " s_cmp_eq_u64 exec, -1\n s_mov_b64 exec, -1\n"
    asm volatile(SWEEP_A " s_sleep 4\n s_mov_b32 %[cnt], 0x40000\n"
      "1: " SWEEP_B "fz_hopg: s_cbranch_execz fz_hop6\n s_waitcnt vmcnt(4)\n" CK("v65") CK("v67") CK("v69") CK("v71") CK("v73") CK("v75") CK("v77") CK("v79") CK_END " s_cbranch_scc1 2f\n"
      SWEEP_A "fz_hop6: s_cbranch_execz fz_hop9\n s_waitcnt vmcnt(4)\n" CK("v81") CK("v83") CK("v85") CK("v87") CK("v89") CK("v91") CK("v93") CK("v95") CK_END " s_cbranch_scc1 3f\n"
      " s_sub_u32 %[cnt], %[cnt], 1\n s_cmp_lg_u32 %[cnt], 0\n s_cbranch_scc1 1b\n"
      "3: v_add_f32 %[a], v80, v84\n v_add_f32 %[b], v82, v86\n v_add_f32 %[c], v88, v92\n v_add_f32 %[d], v90, v94\n s_branch 4f\n"
      "fz_hop9: s_branch fz_hop3\n"
      "2: v_add_f32 %[a], v64, v68\n v_add_f32 %[b], v66, v70\n v_add_f32 %[c], v72, v76\n v_add_f32 %[d], v74, v78\n 4:"
      : [a] "=&v"(va), [b] "=&v"(vb), [c] "=&v"(vc2), [d] "=&v"(vd), [cnt] "=&s"(cnt_), "=&{v[64:79]}"(keepA), "=&{v[80:95]}"(keepB)
      : [off] "v"(o_fin), [rs] "s"(rs), [tag] "v"(tag) : "scc", "vcc", "memory");
    float sv = x32_sum2(va + vc2, vb + vd);
    asm volatile("fz_hop3: s_cbranch_execz fz_hop7" : "+v"(sv));
    sv = x16_sum(sv);
    sv += dppf<ROR8>(sv);
    const float ov = xres + rbf(rbf(sv) * gate);
    __builtin_amdgcn_raw_buffer_store_b16((u16)pkbf(ov, ov), mk_rsrc(pout, 1024), o_out, 0, SC1);
    HOP(7, "fz_ret");
    asm volatile("fz_last:");
    __builtin_amdgcn_raw_buffer_store_b32(tag, rs, (h == 0 && lane == 0) ? WS_EPOCH : OOB, 0, SC1);
    asm volatile("" :: "v"(keepA), "v"(keepB));
  }
}

extern "C" void kernel_launch(void* const* d_in, const int* in_sizes, int n_in, void* d_out, int out_size, void* d_ws, size_t ws_size, hipStream_t stream) {
  fused<<<256, 1024, 0, stream>>>((const u16*)d_in[1], (const u16*)d_in[2], (const u16*)d_in[0], (const u16*)d_in[3], (const u16*)d_in[6],
                                  (unsigned char*)d_ws, (const u16*)d_in[4], (const u16*)d_in[7], (u16*)d_out);
}
```
